```python
import torch

def _softmax(x, axis=-1):
    return torch.softmax(x, dim=axis)
def _gelu(x):
    return torch.nn.functional.gelu(x, approximate="tanh")
def _silu(x):
    return torch.nn.functional.silu(x)
def _sigmoid(x):
    return torch.sigmoid(x)
def _layernorm(x, axis=-1, eps=1e-5):
    m = x.mean(dim=axis, keepdim=True)
    v = x.var(dim=axis, keepdim=True, unbiased=False)
    return (x - m) / torch.sqrt(v + eps)
def _scatter_add(tgt, idx, src, axis=0):
    return tgt.clone().index_add_(axis, idx, src)
def _segment_sum(src, seg, n):
    out = torch.zeros((n,) + src.shape[1:], dtype=src.dtype)
    return out.index_add_(0, seg, src)
def _pad(x, pw):
    flat = tuple(v for pair in reversed(pw) for v in pair)
    return torch.nn.functional.pad(x, flat)

kernel_name = 'sequential_preLN__grouped_kv_mqa__glu_gated_bilinear__prompt'

def setup_inputs():

    _w = (lambda a: (lambda t: t.to(torch.bfloat16) if t.is_floating_point() else t)(torch.as_tensor(a)))
    def _A_setup():
        B, S, D, H = 1, 2048, 640, 32
        Dh = D // H
        return {
            "x": _w((lambda *s: torch.randn(*s, dtype=torch.bfloat16) * 0.05)(B, S, D)),
            "wq": _w((lambda *s: torch.randn(*s, dtype=torch.bfloat16) * 0.05)(D, H * Dh)),
            "wk": _w((lambda *s: torch.randn(*s, dtype=torch.bfloat16) * 0.05)(D, Dh)),
            "wv": _w((lambda *s: torch.randn(*s, dtype=torch.bfloat16) * 0.05)(D, Dh)),
            "wo": _w((lambda *s: torch.randn(*s, dtype=torch.bfloat16) * 0.05)(H * Dh, D)),
        }
    _A_in = _A_setup()

    def _B_setup():
        B, S, D, F = 1, 2048, 640, 1728
        return {
            "x": _w((lambda *s: torch.randn(*s, dtype=torch.bfloat16) * 0.05)(B, S, D)),
            "w_gate": _w((lambda *s: torch.randn(*s, dtype=torch.bfloat16) * 0.8)(D, F) * (D ** -0.5)),
            "w_up": _w((lambda *s: torch.randn(*s, dtype=torch.bfloat16) * 0.8)(D, F) * (D ** -0.5)),
            "w_down": _w((lambda *s: torch.randn(*s, dtype=torch.bfloat16) * 0.8)(F, D) * (F ** -0.5)),
        }
    _B_in = _B_setup()

    return {
        "A_x": _A_in["x"],
        "A_wq": _A_in["wq"],
        "A_wk": _A_in["wk"],
        "A_wv": _A_in["wv"],
        "A_wo": _A_in["wo"],
        "B_x": _B_in["x"],
        "B_w_gate": _B_in["w_gate"],
        "B_w_up": _B_in["w_up"],
        "B_w_down": _B_in["w_down"],
    }

def reference(A_x, A_wq, A_wk, A_wv, A_wo, B_x, B_w_gate, B_w_up, B_w_down):
    def _A_ref(x, wq, wk, wv, wo):
        B, S, D = x.shape
        H = 32
        Dh = D // H
        q = (lambda x, *d: x.permute(*d))((x @ wq).reshape(B, S, H, Dh), 0, 2, 1, 3)
        k = x @ wk
        v = x @ wv
        scores = torch.einsum("bhqd,bkd->bhqk", q, k) * (Dh ** -0.5)
        p = _softmax(scores, axis=-1)
        o = torch.einsum("bhqk,bkd->bhqd", p, v)
        return (lambda x, *d: x.permute(*d))(o, 0, 2, 1, 3).reshape(B, S, H * Dh) @ wo

    def _B_ref(x, w_gate, w_up, w_down):
        g = torch.einsum("bsd,df->bsf", x, w_gate)
        u = torch.einsum("bsd,df->bsf", x, w_up)
        h = g * u
        return torch.einsum("bsf,fd->bsd", h, w_down)

    x = A_x
    h = x + _A_ref(_layernorm(x, axis=-1), A_wq, A_wk, A_wv, A_wo)
    h2 = h + _B_ref(_layernorm(h, axis=-1), B_w_gate, B_w_up, B_w_down)
    return h2
```

```cpp
#include <hip/hip_runtime.h>
#include <stdint.h>

#define DI __device__ __forceinline__
#define LAS __attribute__((address_space(3)))
#define GAS __attribute__((address_space(1)))
typedef short bf16x8 __attribute__((ext_vector_type(8)));
typedef short bf16x4 __attribute__((ext_vector_type(4)));
typedef __bf16 bfv2 __attribute__((ext_vector_type(2)));
typedef float f32x2 __attribute__((ext_vector_type(2)));
typedef float f32x4 __attribute__((ext_vector_type(4)));
typedef float f32x16 __attribute__((ext_vector_type(16)));
typedef unsigned u32x2 __attribute__((ext_vector_type(2)));
typedef unsigned u32x4 __attribute__((ext_vector_type(4)));
typedef LAS char* lptr;

constexpr int SEQ = 2048, DM = 640, FF = 1728;
constexpr int NWG = 256, NTHR = 512;
constexpr float LN_EPS = 1e-5f;
constexpr float QSCALE = 0.22360679774997896f * 1.4426950408889634f;

constexpr unsigned W_CNTO = 0, W_CNTH = 8192, W_CNTG = 16384, W_KMAX = 24576, W_TMO = 28672, W_CTRL_END = 32768;
constexpr unsigned W_QF = 65536;
constexpr unsigned W_KV = W_QF + 256 * 8 * 1280;
constexpr unsigned W_OF = W_KV + 16 * 10240;
constexpr unsigned W_HF = W_OF + SEQ * DM * 2;
constexpr unsigned W_HST = W_HF + SEQ * DM * 2;
constexpr unsigned W_GF = W_HST + 32 * 8 * 64 * 8;
constexpr unsigned W_WGU = W_GF + 64 * 108 * 1024;
constexpr unsigned W_WD = W_WGU + 108 * 40 * 1024;
constexpr unsigned W_END = W_WD + 8 * FF * 160;

struct Params {
  const char* x; const char* wq; const char* wk; const char* wv; const char* wo;
  const char* wg; const char* wu; const char* wd;
  char* out; char* ws;
};

constexpr int AUX_SC1 = 16;
#define MFMA16(a, b, c) __builtin_amdgcn_mfma_f32_32x32x16_bf16((a), (b), (c), 0, 0, 0)
#define MFMA8(a, b, c) __builtin_amdgcn_mfma_f32_32x32x8bf16_1k((a), (b), (c), 0, 0, 0)
#define MFMAS(a, b, c) __builtin_amdgcn_mfma_f32_16x16x32_bf16((a), (b), (c), 0, 0, 0)
#define SBAR() __builtin_amdgcn_sched_barrier(0)
#define WAITV(n) asm volatile("s_waitcnt vmcnt(" #n ")" ::: "memory")
#define LBAR() asm volatile("s_waitcnt lgkmcnt(0)\n\ts_barrier" ::: "memory")

DI unsigned pk2(float lo, float hi) { f32x2 v = {lo, hi}; return __builtin_bit_cast(unsigned, __builtin_convertvector(v, bfv2)); }
DI float bflo(unsigned v) { return __uint_as_float(v << 16); }
DI float bfhi(unsigned v) { return __uint_as_float(v & 0xffff0000u); }
DI float dot2(unsigned a, unsigned b, float c) { return __builtin_amdgcn_fdot2_f32_bf16(__builtin_bit_cast(bfv2, a), __builtin_bit_cast(bfv2, b), c, false); }
constexpr unsigned ONES2 = 0x3f803f80u;
DI float swap32_sum(float v) { auto r = __builtin_amdgcn_permlane32_swap(__float_as_uint(v), __float_as_uint(v), false, false); return __uint_as_float(r[0]) + __uint_as_float(r[1]); }
DI float swap32_max(float v) { auto r = __builtin_amdgcn_permlane32_swap(__float_as_uint(v), __float_as_uint(v), false, false); return fmaxf(__uint_as_float(r[0]), __uint_as_float(r[1])); }
DI void glds16(const void* g, unsigned lds_base) {
  unsigned sv;
  asm volatile("s_mov_b32 %0, m0\n\ts_mov_b32 m0, %2\n\ts_nop 0\n\tglobal_load_lds_dwordx4 %1, off\n\ts_mov_b32 m0, %0" : "=&s"(sv) : "v"(g), "s"(lds_base) : "memory");
}
typedef short v4i16_t __attribute__((ext_vector_type(4)));
DI bf16x4 trread(lptr p) { return __builtin_bit_cast(bf16x4, __builtin_amdgcn_ds_read_tr16_b64_v4i16((LAS v4i16_t*)p)); }
DI bf16x8 cat8(bf16x4 a, bf16x4 b) { return __builtin_shufflevector(a, b, 0, 1, 2, 3, 4, 5, 6, 7); }
DI u32x4 lds128(lptr p) { return *(LAS u32x4*)p; }
DI u32x2 lds64(lptr p) { return *(LAS u32x2*)p; }
DI bf16x8 asbf8(u32x4 v) { return __builtin_bit_cast(bf16x8, v); }
DI bf16x4 asbf4(u32x2 v) { return __builtin_bit_cast(bf16x4, v); }
DI unsigned rfl(unsigned v) { return __builtin_amdgcn_readfirstlane(v); }
typedef __amdgpu_buffer_rsrc_t rsrc_t;
template <int AUX> DI u32x4 bld128(rsrc_t r, unsigned off) { return __builtin_amdgcn_raw_buffer_load_b128(r, off, 0, AUX); }
template <int AUX> DI u32x2 bld64(rsrc_t r, unsigned off) { return __builtin_amdgcn_raw_buffer_load_b64(r, off, 0, AUX); }
template <int AUX> DI unsigned bld32(rsrc_t r, unsigned off) { return __builtin_amdgcn_raw_buffer_load_b32(r, off, 0, AUX); }
template <int AUX> DI void bst128(rsrc_t r, unsigned off, u32x4 v) { __builtin_amdgcn_raw_buffer_store_b128(v, r, off, 0, AUX); }
template <int AUX> DI void bst64(rsrc_t r, unsigned off, u32x2 v) { __builtin_amdgcn_raw_buffer_store_b64(v, r, off, 0, AUX); }
template <int AUX> DI void bst32(rsrc_t r, unsigned off, unsigned v) { __builtin_amdgcn_raw_buffer_store_b32(v, r, off, 0, AUX); }

constexpr unsigned L1_X = 0, L1_S = 81920, L1_WK = L1_S + 4 * 10240, L1_PART = L1_WK + 25600, L1_TAB = L1_PART + 13824;
constexpr unsigned L1_ONES = L1_TAB, L1_CS = L1_TAB + 16  , L1_ST = L1_CS + 1024  ;
static_assert(L1_ST + 256 <= 163840, "lds");
constexpr unsigned L1_SSQ = L1_PART;

DI unsigned wq_slot(int c) { return c < 4 ? L1_S + c * 10240 : (c < 6 ? L1_WK + (c - 4) * 10240 : L1_S + (c - 6) * 10240); }

DI void dma_wq_chunk_instr(const Params& p, unsigned lds0, int hg, int c, int instr, int lane) {
  const int q = instr * 64 + lane, row = (q * 6554) >> 16, cc = q - row * 10;
  glds16(p.wq + (size_t)(64 * c + row) * 1280 + hg * 160 + cc * 16, rfl(lds0 + wq_slot(c) + instr * 1024));
}

DI void phase1(const Params& p, lptr lds, rsrc_t ws, int b) {
  const int tid = threadIdx.x, lane = tid & 63, w = rfl(tid >> 6);
  const int r32 = lane & 31, h = lane >> 5;
  const int xcd = b & 7, rk = b >> 3, tg = 4 * xcd + (rk >> 3), hg = rk & 7;
  const unsigned lds0 = (unsigned)(uintptr_t)lds;
#pragma unroll
  for (int i = 0; i < 10; ++i) {
    const int pc = (w * 10 + i) * 64 + lane, t = (pc * 52429) >> 22, cp = pc - t * 80, c = (cp & ~15) | ((cp ^ t) & 15);
    glds16(p.x + (size_t)(tg * 64 + t) * 1280 + c * 16, rfl(lds0 + L1_X + (w * 10 + i) * 1024));
  }
  if (hg < 4) {
    const char* W = hg < 2 ? p.wk : p.wv;
#pragma unroll
    for (int i = 0; i < 4; ++i) { const int idx = w + 8 * i; if (idx < 25) glds16(W + idx * 1024 + lane * 16, rfl(lds0 + L1_WK + idx * 1024)); }
  }
#pragma unroll
  for (int i = 0; i < 5; ++i) { const int n = w * 5 + i; dma_wq_chunk_instr(p, lds0, hg, n / 10, n % 10, lane); }
  u32x4 rt[4];
  const int gj = b + 256 * w;
  const bool has_gu = (w <= 4) && gj < 1080;
  const int gu_cg = gj / 40, gu_s = gj % 40;
  const int dj0 = (b * 3 + (w - 5)) * 3;
  if (has_gu) {
#pragma unroll
    for (int i = 0; i < 4; ++i) {
      const char* W = (i >> 1) ? p.wu : p.wg;
      rt[i] = *(const GAS u32x4*)(W + (size_t)(16 * gu_s + 8 * (i & 1) + (lane >> 3)) * (FF * 2) + gu_cg * 128 + (lane & 7) * 16);
    }
  } else if (w >= 5) {
#pragma unroll
    for (int i = 0; i < 3; ++i) { const int j = dj0 + i; if (j < 2160) rt[i] = *(const GAS u32x4*)(p.wd + (size_t)j * 1024 + lane * 16); }
  }
  if (tid < 4) *(LAS unsigned*)(lds + L1_ONES + tid * 4) = ONES2;
  auto xaddr = [&](int tok, int s) -> unsigned { const int c = 2 * s + h; return L1_X + tok * 1280 + ((c & ~15) | ((c ^ tok) & 15)) * 16; };
  const int i16 = lane & 15, tq = i16 >> 2, tp = i16 & 3, grp = (lane >> 4) & 1;
  const bool ones_lane = grp && tp > 0;
  WAITV(0);
  LBAR();
  if (hg < 4) {
    const int th = hg & 1; const bool isV = hg >= 2;
    f32x16 acc = {}; float ssq = 0.f, sx = 0.f, cs = 0.f;
    if (w < 4) {
      const unsigned wb = L1_WK + (8 * h + tq) * 40 + (grp ? 32 : 8 * tp);
#pragma unroll
      for (int i = 0; i < 10; ++i) {
        const int s = 10 * w + i;
        const u32x4 xf = lds128(lds + xaddr(th * 32 + r32, s));
        const unsigned a0 = ones_lane ? L1_ONES : wb + s * 640, a1 = ones_lane ? L1_ONES : wb + s * 640 + 160;
        const bf16x8 wf = cat8(trread(lds + a0), trread(lds + a1));
        const u32x4 wu = __builtin_bit_cast(u32x4, wf);
        acc = isV ? MFMA16(asbf8(xf), wf, acc) : MFMA16(wf, asbf8(xf), acc);
#pragma unroll
        for (int j = 0; j < 4; ++j) { ssq = dot2(xf[j], xf[j], ssq); sx = dot2(xf[j], ONES2, sx); cs = dot2(wu[j], ONES2, cs); }
      }
      ssq = swap32_sum(ssq); sx = swap32_sum(sx); cs = swap32_sum(cs);
      if (w > 0) {
        LAS f32x4* pa = (LAS f32x4*)(lds + L1_PART + (w - 1) * 4096) + lane;
#pragma unroll
        for (int g = 0; g < 4; ++g) pa[g * 64] = f32x4{acc[4 * g], acc[4 * g + 1], acc[4 * g + 2], acc[4 * g + 3]};
        if (h == 0) { LAS float* ps = (LAS float*)(lds + L1_PART + 12288 + (w - 1) * 384) + r32; ps[0] = ssq; ps[32] = sx; ps[64] = cs; }
      }
    }
    LBAR();
    if (w == 0) {
#pragma unroll
      for (int v = 0; v < 3; ++v) {
        LAS f32x4* pa = (LAS f32x4*)(lds + L1_PART + v * 4096) + lane;
#pragma unroll
        for (int g = 0; g < 4; ++g) { const f32x4 t = pa[g * 64]; acc[4 * g] += t[0]; acc[4 * g + 1] += t[1]; acc[4 * g + 2] += t[2]; acc[4 * g + 3] += t[3]; }
        LAS float* ps = (LAS float*)(lds + L1_PART + 12288 + v * 384) + r32; ssq += ps[0]; sx += ps[32]; cs += ps[64];
      }
      const float mean = sx * (1.f / DM), rstd = rsqrtf(fmaxf(ssq * (1.f / DM) - mean * mean, 0.f) + LN_EPS);
      const int tile = tg * 2 + th, chunk = tile >> 2, tl = tile & 3;
      LAS float* tab = (LAS float*)(lds + L1_ST);
      if (!isV) {
        if (h == 0) tab[r32] = cs;
        asm volatile("s_waitcnt lgkmcnt(0)" ::: "memory");
        float kv[12];
#pragma unroll
        for (int g = 0; g < 3; ++g) { const f32x4 c4 = *(LAS f32x4*)(tab + 8 * g + 4 * h);
#pragma unroll
          for (int j = 0; j < 4; ++j) kv[4 * g + j] = (acc[4 * g + j] - mean * c4[j]) * rstd; }
        u32x4 k1 = {pk2(kv[0], kv[1]), pk2(kv[2], kv[3]), pk2(kv[4], kv[5]), pk2(kv[6], kv[7])};
        u32x2 k2 = {pk2(kv[8], kv[9]), pk2(kv[10], kv[11])};
        bst128<AUX_SC1>(ws, W_KV + chunk * 10240 + tl * 1024 + lane * 16, k1);
        if (h == 0) bst64<AUX_SC1>(ws, W_KV + chunk * 10240 + 4096 + tl * 256 + r32 * 8, k2);
        float n2 = 0.f;
#pragma unroll
        for (int j = 0; j < 4; ++j) n2 = dot2(k1[j], k1[j], n2);
        if (h == 0) { n2 = dot2(k2[0], k2[0], n2); n2 = dot2(k2[1], k2[1], n2); }
        n2 = swap32_sum(n2);
#pragma unroll
        for (int o = 16; o > 0; o >>= 1) n2 = fmaxf(n2, __shfl_xor(n2, o));
        if (lane == 0) bst32<AUX_SC1>(ws, W_KMAX + tile * 4, __float_as_uint(n2));
      } else {
        if (h == 0) { tab[r32] = mean; tab[32 + r32] = rstd; }
        asm volatile("s_waitcnt lgkmcnt(0)" ::: "memory");
        float vv[16];
#pragma unroll
        for (int g = 0; g < 4; ++g) { const f32x4 m4 = *(LAS f32x4*)(tab + 8 * g + 4 * h), r4 = *(LAS f32x4*)(tab + 32 + 8 * g + 4 * h);
#pragma unroll
          for (int j = 0; j < 4; ++j) vv[4 * g + j] = (acc[4 * g + j] - m4[j] * cs) * r4[j]; }
        if (r32 < 20) {
#pragma unroll
          for (int s = 0; s < 2; ++s) {
            u32x4 o = {pk2(vv[8 * s], vv[8 * s + 1]), pk2(vv[8 * s + 2], vv[8 * s + 3]), pk2(vv[8 * s + 4], vv[8 * s + 5]), pk2(vv[8 * s + 6], vv[8 * s + 7])};
            bst128<AUX_SC1>(ws, W_KV + chunk * 10240 + 5120 + tl * 1280 + s * 640 + h * 320 + r32 * 16, o);
          }
        }
      }
    }
  }
  LBAR();
  {
    const int hh = w & 3, th = w >> 2;
    { const int n = w; dma_wq_chunk_instr(p, lds0, hg, 4 + n / 10, n % 10, lane); const int n2 = w + 8; dma_wq_chunk_instr(p, lds0, hg, 4 + n2 / 10, n2 % 10, lane);
      if (w < 4) { const int n3 = w + 16; dma_wq_chunk_instr(p, lds0, hg, 4 + n3 / 10, n3 % 10, lane); } }
    f32x16 acc = {}; float ssq = 0.f, cs = 0.f;
    const unsigned wrow = (8 * h + tq) * 160 + hh * 40 + (grp ? 32 : 8 * tp);
    const int tok = th * 32 + r32;
#pragma unroll
    for (int c = 0; c < 10; ++c) {
      if (c == 2 || c == 4) {
        LBAR();
        const int cb = c == 2 ? 6 : 8;
        { const int n = w; dma_wq_chunk_instr(p, lds0, hg, cb + n / 10, n % 10, lane); const int n2 = w + 8; dma_wq_chunk_instr(p, lds0, hg, cb + n2 / 10, n2 % 10, lane);
          if (w < 4) { const int n3 = w + 16; dma_wq_chunk_instr(p, lds0, hg, cb + n3 / 10, n3 % 10, lane); } }
      }
      if (c == 4 || c == 6 || c == 8) { WAITV(0); LBAR(); }
#pragma unroll
      for (int ks = 0; ks < 4; ++ks) {
        const int s = 4 * c + ks;
        const u32x4 xf = lds128(lds + xaddr(tok, s));
        const unsigned base = wq_slot(c) + ks * 2560 + wrow;
        const unsigned a0 = ones_lane ? L1_ONES : base, a1 = ones_lane ? L1_ONES : base + 640;
        const bf16x8 wf = cat8(trread(lds + a0), trread(lds + a1));
        acc = MFMA16(wf, asbf8(xf), acc);
        if ((s & 3) == hh) {
#pragma unroll
          for (int j = 0; j < 4; ++j) ssq = dot2(xf[j], xf[j], ssq);
        }
        if ((s & 1) == th) { const u32x4 wu = __builtin_bit_cast(u32x4, wf);
#pragma unroll
          for (int j = 0; j < 4; ++j) cs = dot2(wu[j], ONES2, cs);
        }
      }
    }
    ssq = swap32_sum(ssq); cs = swap32_sum(cs);
    if (h == 0) { ((LAS float*)(lds + L1_SSQ))[(th * 4 + hh) * 32 + r32] = ssq; ((LAS float*)(lds + L1_CS))[w * 32 + r32] = cs; }
    LBAR();
    float sq = 0.f;
#pragma unroll
    for (int v = 0; v < 4; ++v) sq += ((LAS float*)(lds + L1_SSQ))[(th * 4 + v) * 32 + r32];
    const float mean = acc[12] * (1.f / DM), rstd = rsqrtf(fmaxf(sq * (1.f / DM) - mean * mean, 0.f) + LN_EPS) * QSCALE;
    float qv[12];
#pragma unroll
    for (int g = 0; g < 3; ++g) {
      const f32x4 ca = *(LAS f32x4*)(lds + L1_CS + (hh * 32 + 8 * g + 4 * h) * 4), cb = *(LAS f32x4*)(lds + L1_CS + ((hh + 4) * 32 + 8 * g + 4 * h) * 4);
#pragma unroll
      for (int j = 0; j < 4; ++j) qv[4 * g + j] = (acc[4 * g + j] - mean * (ca[j] + cb[j])) * rstd;
    }
    const u32x4 q1 = {pk2(qv[0], qv[1]), pk2(qv[2], qv[3]), pk2(qv[4], qv[5]), pk2(qv[6], qv[7])};
    const u32x2 q2 = {pk2(qv[8], qv[9]), pk2(qv[10], qv[11])};
    bst128<0>(ws, W_QF + (b * 8 + w) * 1280 + lane * 16, q1);
    if (h == 0) bst64<0>(ws, W_QF + (b * 8 + w) * 1280 + 1024 + r32 * 8, q2);
  }
  LBAR();
  if (has_gu) {
    lptr sc = lds + w * 4096;
#pragma unroll
    for (int i = 0; i < 4; ++i) *(LAS u32x4*)(sc + i * 1024 + lane * 16) = rt[i];
    asm volatile("s_waitcnt lgkmcnt(0)" ::: "memory");
#pragma unroll
    for (int u = 0; u < 4; ++u) {
      lptr a = sc + grp * 2048 + (8 * h + tq) * 128 + u * 32 + tp * 8;
      const bf16x8 f = cat8(trread(a), trread(a + 512));
      bst128<AUX_SC1>(ws, W_WGU + ((gu_cg * 4 + u) * 40 + gu_s) * 1024 + lane * 16, __builtin_bit_cast(u32x4, f));
    }
  } else if (w >= 5) {
#pragma unroll
    for (int i = 0; i < 3; ++i) { const int j = dj0 + i;
      if (j < 2160) { const int pc = j * 64 + lane, f = pc / 80, c = pc - f * 80, cg = c / 10, cc = c - cg * 10;
        bst128<AUX_SC1>(ws, W_WD + (cg * FF + f) * 160 + cc * 16, rt[i]); } }
  }
}

constexpr unsigned SPIN_CAP = 1u << 17;
DI void wait_cnt(char* wsp, unsigned off, unsigned want) {
  if (threadIdx.x == 0) {
    GAS unsigned* word = (GAS unsigned*)(wsp + off); GAS unsigned* tmo = (GAS unsigned*)(wsp + W_TMO);
    for (unsigned spins = 0;;) {
      if (__hip_atomic_load(word, __ATOMIC_RELAXED, __HIP_MEMORY_SCOPE_AGENT) >= want) break;
      __builtin_amdgcn_s_sleep(1);
      if (++spins > SPIN_CAP) { __hip_atomic_store(tmo, 1u, __ATOMIC_RELAXED, __HIP_MEMORY_SCOPE_AGENT); break; }
      if ((spins & 255u) == 0u && __hip_atomic_load(tmo, __ATOMIC_RELAXED, __HIP_MEMORY_SCOPE_AGENT)) break;
    }
    __builtin_amdgcn_fence(__ATOMIC_ACQUIRE, "wavefront");
  }
  LBAR();
}
DI void signal_cnt(char* wsp, unsigned off) {
  WAITV(0);
  LBAR();
  if (threadIdx.x == 0) (void)__hip_atomic_fetch_add((GAS unsigned*)(wsp + off), 1u, __ATOMIC_RELAXED, __HIP_MEMORY_SCOPE_AGENT);
}

constexpr unsigned L2_WO = 0, L2_KV = 102400, KV_SLOT = 10560, L2_FLAG = L2_KV + 3 * KV_SLOT, L2_RED = L2_KV;
constexpr unsigned KV_K2 = 4096, KV_V = 5184;

DI void split3(float b, u32x2& q2) {
  const unsigned ph = pk2(b, 0.f); const float r1 = b - bflo(ph);
  const unsigned pm = pk2(r1, 0.f); const float r2 = r1 - bflo(pm);
  q2[0] = (ph & 0xffffu) | (pm << 16); q2[1] = pk2(r2, 0.f) & 0xffffu;
}

DI void phase_attn(const Params& p, lptr lds, rsrc_t ws, int b) {
  const int tid = threadIdx.x, lane = tid & 63, w = rfl(tid >> 6);
  const int r32 = lane & 31, h = lane >> 5;
  const int xcd = b & 7, rk = b >> 3, tg = 4 * xcd + (rk >> 3), hg = rk & 7;
  const int hh = w & 3, th = w >> 2;
  const unsigned lds0 = (unsigned)(uintptr_t)lds;
  auto kv_dst = [&](int pc) -> unsigned {
    if (pc < 256) return pc * 16;
    if (pc < 320) { const int q = pc - 256; return KV_K2 + (q >> 4) * 272 + (q & 15) * 16; }
    const int q = pc - 320, t = q / 80, e = q - t * 80, sh = e / 20, d = e - sh * 20;
    return KV_V + t * 1344 + sh * 336 + d * 16;
  };
  const unsigned dstA = L2_KV + kv_dst(tid), dstB = L2_KV + kv_dst(512 + (tid & 127));
  const unsigned srcA = W_KV + tid * 16, srcB = W_KV + (512 + (tid & 127)) * 16;
  const bool hasB = tid < 128;
  u32x4 q1 = bld128<0>(ws, W_QF + (b * 8 + w) * 1280 + lane * 16);
  u32x2 q2 = bld64<0>(ws, W_QF + (b * 8 + w) * 1280 + 1024 + r32 * 8);
  float kmx = __uint_as_float(bld32<AUX_SC1>(ws, W_KMAX + lane * 4));
  u32x4 sa = bld128<AUX_SC1>(ws, srcA), sb; if (hasB) sb = bld128<AUX_SC1>(ws, srcB);
  u32x4 sa1 = bld128<AUX_SC1>(ws, srcA + 10240), sb1; if (hasB) sb1 = bld128<AUX_SC1>(ws, srcB + 10240);
  if (tid < 12) { const int sl = tid >> 2, t = tid & 3; *(LAS u32x2*)(lds + L2_KV + sl * KV_SLOT + KV_K2 + t * 272 + 256) = u32x2{ONES2, 0x00003f80u}; }
  if (tid < 48) { const int sl = tid >> 4, e = tid & 15; *(LAS u32x4*)(lds + L2_KV + sl * KV_SLOT + KV_V + (e >> 2) * 1344 + (e & 3) * 336 + 320) = u32x4{ONES2, ONES2, ONES2, ONES2}; }
  if (tid == 0) *(LAS unsigned*)(lds + L2_FLAG) = 0u;
  *(LAS u32x4*)(lds + dstA) = sa; if (hasB) *(LAS u32x4*)(lds + dstB) = sb;
  *(LAS u32x4*)(lds + dstA + KV_SLOT) = sa1; if (hasB) *(LAS u32x4*)(lds + dstB + KV_SLOT) = sb1;
  {
#pragma unroll
    for (int o = 32; o > 0; o >>= 1) kmx = fmaxf(kmx, __shfl_xor(kmx, o));
    float n2 = 0.f;
#pragma unroll
    for (int j = 0; j < 4; ++j) n2 = dot2(q1[j], q1[j], n2);
    if (h == 0) { n2 = dot2(q2[0], q2[0], n2); n2 = dot2(q2[1], q2[1], n2); }
    n2 = swap32_sum(n2);
    const float bound = sqrtf(n2 * kmx) * 1.001f;
    if (h == 1) split3(-bound, q2);
  }
  const int rv = r32 < 20 ? r32 : 20;
  const unsigned aK1 = lane * 16, aK2 = KV_K2 + (h ? 256 : r32 * 8), aV = KV_V + h * 336 + rv * 16;
  f32x16 o;
  for (int attempt = 0;; ++attempt) {
    o = f32x16{};
    sa = bld128<AUX_SC1>(ws, srcA + 2 * 10240); if (hasB) sb = bld128<AUX_SC1>(ws, srcB + 2 * 10240);
    LBAR();
    lptr s0 = lds + L2_KV;
    u32x4 k1 = lds128(s0 + aK1); u32x2 k2 = lds64(s0 + aK2);
    f32x16 sc = MFMA8(asbf4(k2), asbf4(q2), f32x16{}); sc = MFMA16(asbf8(k1), asbf8(q1), sc);
    k1 = lds128(s0 + aK1 + 1024); k2 = lds64(s0 + aK2 + 272);
    u32x4 v0 = lds128(s0 + aV), v1 = lds128(s0 + aV + 672);
    int sl = 0;
    for (int c = 0; c < 16; ++c) {
      const int sl1 = sl == 2 ? 0 : sl + 1, sl2 = sl1 == 2 ? 0 : sl1 + 1;
      lptr cur = lds + L2_KV + sl * KV_SLOT; lptr nxt = lds + L2_KV + sl1 * KV_SLOT;
      if (c + 2 < 16) { *(LAS u32x4*)(lds + dstA + sl2 * KV_SLOT) = sa; if (hasB) *(LAS u32x4*)(lds + dstB + sl2 * KV_SLOT) = sb; }
      if (c + 3 < 16) { sa = bld128<AUX_SC1>(ws, srcA + (c + 3) * 10240); if (hasB) sb = bld128<AUX_SC1>(ws, srcB + (c + 3) * 10240); }
      if (attempt == 0 && c < 13) { const int n = w + 8 * c;
        if (n < 100) { const int q = n * 64 + lane, row = (q * 52429) >> 19, cc = q - row * 10; glds16(p.wo + (size_t)row * 1280 + hg * 160 + cc * 16, rfl(lds0 + L2_WO + n * 1024)); } }
#pragma unroll
      for (int t = 0; t < 4; ++t) {
        lptr pk = (t + 2 < 4) ? cur + (t + 2) * 1024 : nxt + (t - 2) * 1024;
        lptr pk2a = (t + 2 < 4) ? cur + (t + 2) * 272 : nxt + (t - 2) * 272;
        lptr pv = (t + 1 < 4) ? cur + (t + 1) * 1344 : nxt;
        f32x16 sn = MFMA8(asbf4(k2), asbf4(q2), f32x16{});
        sn = MFMA16(asbf8(k1), asbf8(q1), sn);
        k1 = lds128(pk + aK1); k2 = lds64(pk2a + aK2);
        float e[16];
#pragma unroll
        for (int j = 0; j < 16; ++j) e[j] = __builtin_amdgcn_exp2f(sc[j]);
        const u32x4 p0 = {pk2(e[0], e[1]), pk2(e[2], e[3]), pk2(e[4], e[5]), pk2(e[6], e[7])};
        const u32x4 p1 = {pk2(e[8], e[9]), pk2(e[10], e[11]), pk2(e[12], e[13]), pk2(e[14], e[15])};
        o = MFMA16(asbf8(v0), asbf8(p0), o);
        o = MFMA16(asbf8(v1), asbf8(p1), o);
        v0 = lds128(pv + aV); v1 = lds128(pv + aV + 672);
        sc = sn;
      }
      LBAR();
      sl = sl1;
    }
    const float lsum = __uint_as_float(__builtin_amdgcn_permlane32_swap(__float_as_uint(o[8]), __float_as_uint(o[8]), false, false)[1]);
    if (attempt == 1) break;
    const bool bad = !(lsum >= 5.421010862427522e-20f);
    if (__any(bad)) *(LAS unsigned*)(lds + L2_FLAG) = 1u;
    LBAR();
    if (*(LAS unsigned*)(lds + L2_FLAG) == 0u) break;
    {
      u32x2 qz = q2; if (h == 1) qz = u32x2{0u, 0u};
      float mx = -3.0e38f;
      for (int t = 0; t < 64; ++t) {
        const unsigned base = W_KV + (t >> 2) * 10240;
        const u32x4 a1 = bld128<AUX_SC1>(ws, base + (t & 3) * 1024 + lane * 16);
        const u32x2 a2 = bld64<AUX_SC1>(ws, base + 4096 + (t & 3) * 256 + r32 * 8);
        f32x16 s = MFMA8(asbf4(a2), asbf4(qz), f32x16{}); s = MFMA16(asbf8(a1), asbf8(q1), s);
#pragma unroll
        for (int j = 0; j < 16; ++j) mx = fmaxf(mx, s[j]);
      }
      mx = swap32_max(mx);
      if (h == 1) split3(-mx, q2);
      const u32x4 t0 = bld128<AUX_SC1>(ws, srcA), t1 = bld128<AUX_SC1>(ws, srcA + 10240);
      *(LAS u32x4*)(lds + dstA) = t0; *(LAS u32x4*)(lds + dstA + KV_SLOT) = t1;
      if (hasB) { const u32x4 t2 = bld128<AUX_SC1>(ws, srcB), t3 = bld128<AUX_SC1>(ws, srcB + 10240); *(LAS u32x4*)(lds + dstB) = t2; *(LAS u32x4*)(lds + dstB + KV_SLOT) = t3; }
    }
  }
  {
    const float lsum = __uint_as_float(__builtin_amdgcn_permlane32_swap(__float_as_uint(o[8]), __float_as_uint(o[8]), false, false)[1]);
    const float inv = 1.0f / lsum;
    const u32x4 oa = {pk2(o[0] * inv, o[1] * inv), pk2(o[2] * inv, o[3] * inv), pk2(o[4] * inv, o[5] * inv), pk2(o[6] * inv, o[7] * inv)};
    const u32x2 ob = {pk2(o[8] * inv, o[9] * inv), pk2(o[10] * inv, o[11] * inv)};
    const unsigned tile16 = tg * 4 + th * 2 + (r32 >> 4), g0 = 10 * hg + 5 * (hh >> 1);
    bst128<AUX_SC1>(ws, W_OF + ((tile16 * 80 + g0 + 2 * (hh & 1) + h) * 16 + (r32 & 15)) * 16, oa);
    if (h == 0) bst64<AUX_SC1>(ws, W_OF + ((tile16 * 80 + g0 + 4) * 16 + (r32 & 15)) * 16 + 8 * (hh & 1), ob);
  }
  signal_cnt(p.ws, W_CNTO + tg * 256);
}

DI void phase_oproj(const Params& p, lptr lds, rsrc_t ws, int b, u32x2 (&hres)[5]) {
  const int tid = threadIdx.x, lane = tid & 63, w = rfl(tid >> 6);
  const int xcd = b & 7, rk = b >> 3, tg = 4 * xcd + (rk >> 3), cg = rk & 7;
  const int mt = w & 3, kh = w >> 2, t16 = lane & 15, g = lane >> 4, tq = t16 >> 2, tp = t16 & 3;
  const int tok = tg * 64 + mt * 16 + t16;
  u32x2 xr[5];
  if (kh == 0) {
#pragma unroll
    for (int nt = 0; nt < 5; ++nt) xr[nt] = *(const GAS u32x2*)(p.x + (size_t)tok * 1280 + cg * 160 + nt * 32 + g * 8);
  }
  wait_cnt(p.ws, W_CNTO + tg * 256, 8u);
  u32x4 a[10];
#pragma unroll
  for (int i = 0; i < 10; ++i) a[i] = bld128<AUX_SC1>(ws, W_OF + ((tg * 4 + mt) * 80 + 4 * (10 * kh + i)) * 256 + lane * 16);
  f32x4 acc[5] = {};
#pragma unroll
  for (int i = 0; i < 10; ++i) {
    const int t = 4 * i + g, a5 = (t * 13) >> 6, m = t - 5 * a5, pair = 8 * kh + a5;
    const int r1 = m < 4 ? (2 * pair + (m >> 1)) * 20 + 4 * (m & 1) : 2 * pair * 20 + 16, r2 = m < 4 ? r1 + 8 : r1 + 20;
    lptr b1 = lds + L2_WO + (r1 + tq) * 160 + tp * 8; lptr b2 = lds + L2_WO + (r2 + tq) * 160 + tp * 8;
#pragma unroll
    for (int nt = 0; nt < 5; ++nt) acc[nt] = MFMAS(cat8(trread(b1 + nt * 32), trread(b2 + nt * 32)), asbf8(a[i]), acc[nt]);
  }
  LAS f32x4* red = (LAS f32x4*)(lds + L2_RED) + mt * 320 + lane;
  if (kh == 1) {
#pragma unroll
    for (int nt = 0; nt < 5; ++nt) red[nt * 64] = acc[nt];
  }
  LBAR();
  if (kh == 0) {
    float s1 = 0.f, s2 = 0.f;
    const unsigned tile32 = tg * 2 + (mt >> 1);
#pragma unroll
    for (int nt = 0; nt < 5; ++nt) {
      const f32x4 o = red[nt * 64];
      const float h0 = bflo(xr[nt][0]) + acc[nt][0] + o[0], h1 = bfhi(xr[nt][0]) + acc[nt][1] + o[1], h2 = bflo(xr[nt][1]) + acc[nt][2] + o[2], h3 = bfhi(xr[nt][1]) + acc[nt][3] + o[3];
      const u32x2 hv = {pk2(h0, h1), pk2(h2, h3)};
      hres[nt] = hv;
      s1 = dot2(hv[0], ONES2, s1); s1 = dot2(hv[1], ONES2, s1); s2 = dot2(hv[0], hv[0], s2); s2 = dot2(hv[1], hv[1], s2);
      bst64<AUX_SC1>(ws, W_HF + ((tile32 * 40 + 5 * cg + nt) * 64 + (mt & 1) * 16 + t16 + 32 * (g >> 1)) * 16 + (g & 1) * 8, hv);
    }
    s1 += __shfl_xor(s1, 16); s2 += __shfl_xor(s2, 16); s1 += __shfl_xor(s1, 32); s2 += __shfl_xor(s2, 32);
    if (g == 0) bst64<AUX_SC1>(ws, W_HST + ((tg * 8 + cg) * 64 + mt * 16 + t16) * 8, u32x2{__float_as_uint(s1), __float_as_uint(s2)});
  }
  signal_cnt(p.ws, W_CNTH + tg * 256);
}

constexpr unsigned L4_S = 0  , L4_ST = 122880  , L4_CS = L4_ST + 1024  , L4_END = L4_CS + 1024;
constexpr int GWIN = 12;
DI void phase_gateup(const Params& p, lptr lds, rsrc_t ws, int b) {
  const int tid = threadIdx.x, lane = tid & 63, w = rfl(tid >> 6);
  const int xcd = b & 7, rk = b >> 3, mb = 8 * (xcd >> 2) + (rk >> 2), ngl = rk & 3, u0 = 27 * (xcd & 3) + 7 * ngl, nu = ngl == 3 ? 6 : 7;
  const int r32 = lane & 31, h = lane >> 5;
  const bool act = w < nu;
  const unsigned wbase = W_WGU + (u0 + (act ? w : 0)) * 40960 + lane * 16;
  u32x4 a[GWIN];
  if (act) {
#pragma unroll
    for (int i = 0; i < GWIN; ++i) a[i] = bld128<0>(ws, wbase + i * 1024);
  }
  SBAR();
  wait_cnt(p.ws, W_CNTH + (2 * mb) * 256, 8u);
  wait_cnt(p.ws, W_CNTH + (2 * mb + 1) * 256, 8u);
  u32x4 st[4][5];
#pragma unroll
  for (int j = 0; j < 4; ++j)
#pragma unroll
    for (int i = 0; i < 5; ++i) { const int pc = 5 * w + i; st[j][i] = bld128<AUX_SC1>(ws, W_HF + ((mb * 4 + (pc & 3)) * 40 + 10 * j + (pc >> 2)) * 1024 + lane * 16); }
  if (tid < 128) {
    float s1 = 0.f, s2 = 0.f;
#pragma unroll
    for (int c = 0; c < 8; ++c) { const u32x2 v = bld64<AUX_SC1>(ws, W_HST + (((2 * mb + (tid >> 6)) * 8 + c) * 64 + (tid & 63)) * 8); s1 += __uint_as_float(v[0]); s2 += __uint_as_float(v[1]); }
    const float mean = s1 * (1.f / DM), rstd = rsqrtf(fmaxf(s2 * (1.f / DM) - mean * mean, 0.f) + LN_EPS);
    *(LAS f32x2*)(lds + L4_ST + tid * 8) = f32x2{mean, rstd};
  }
#pragma unroll
  for (int j = 0; j < 3; ++j)
#pragma unroll
    for (int i = 0; i < 5; ++i) *(LAS u32x4*)(lds + L4_S + j * 40960 + (5 * w + i) * 1024 + lane * 16) = st[j][i];
  LBAR();
  f32x16 acc[4] = {}; float cs = 0.f;
#pragma unroll
  for (int s = 0; s < 40; ++s) {
    if (s == 10) {
      LBAR();
#pragma unroll
      for (int i = 0; i < 5; ++i) *(LAS u32x4*)(lds + L4_S + (5 * w + i) * 1024 + lane * 16) = st[3][i];
    }
    if (s == 30) LBAR();
    if (act) {
      const u32x4 cur = a[s % GWIN];
      if (s + GWIN < 40) a[s % GWIN] = bld128<0>(ws, wbase + (s + GWIN) * 1024);
      const int j = s / 10, slot = j == 3 ? 0 : j;
      lptr hb = lds + L4_S + slot * 40960 + (s % 10) * 4096 + lane * 16;
#pragma unroll
      for (int t = 0; t < 4; ++t) acc[t] = MFMA16(asbf8(cur), asbf8(lds128(hb + t * 1024)), acc[t]);
#pragma unroll
      for (int q = 0; q < 4; ++q) cs = dot2(cur[q], ONES2, cs);
    }
    SBAR();
  }
  if (act) {
    cs = swap32_sum(cs);
    LAS float* ct = (LAS float*)(lds + L4_CS) + w * 32;
    if (h == 0) ct[r32] = cs;
    asm volatile("s_waitcnt lgkmcnt(0)" ::: "memory");
    f32x4 c4[4];
#pragma unroll
    for (int g = 0; g < 4; ++g) c4[g] = *(LAS f32x4*)(ct + 8 * g + 4 * h);
#pragma unroll
    for (int t = 0; t < 4; ++t) {
      const f32x2 mr = *(LAS f32x2*)(lds + L4_ST + (t * 32 + r32) * 8);
      const float r2 = mr[1] * mr[1];
      float hv[8];
#pragma unroll
      for (int i = 0; i < 8; ++i) hv[i] = (acc[t][i] - mr[0] * c4[i >> 2][i & 3]) * (acc[t][i + 8] - mr[0] * c4[2 + (i >> 2)][i & 3]) * r2;
      bst128<AUX_SC1>(ws, W_GF + ((mb * 4 + t) * 108 + u0 + w) * 1024 + lane * 16, u32x4{pk2(hv[0], hv[1]), pk2(hv[2], hv[3]), pk2(hv[4], hv[5]), pk2(hv[6], hv[7])});
    }
  }
  signal_cnt(p.ws, W_CNTG + mb * 256);
}

constexpr unsigned L5_R = 0  , L5_RED = 122880  ;
DI void phase_down(const Params& p, lptr lds, rsrc_t ws, int b, const u32x2 (&hres)[5]) {
  const int tid = threadIdx.x, lane = tid & 63, w = rfl(tid >> 6);
  const int xcd = b & 7, rk = b >> 3, tg = 4 * xcd + (rk >> 3), cg = rk & 7;
  const int mt = w & 3, kh = w >> 2, t16 = lane & 15, g = lane >> 4, tq = t16 >> 2, tp = t16 & 3;
  const unsigned lds0 = (unsigned)(uintptr_t)lds;
  const char* wd = p.ws + W_WD + (size_t)cg * FF * 160;
  auto dma_chunk = [&](int c) {
#pragma unroll
    for (int i = 0; i < 4; ++i) { int n = w + 8 * i; if (n >= 30) n -= 8; const int k2 = n >= 15, j = n - 15 * k2;
      glds16(wd + (size_t)(864 * k2 + 96 * c) * 160 + j * 1024 + lane * 16, rfl(lds0 + L5_R + (c & 3) * 30720 + n * 1024)); }
  };
  dma_chunk(0); dma_chunk(1); dma_chunk(2);
  wait_cnt(p.ws, W_CNTG + (tg >> 1) * 256, 16u);
  const unsigned abase = W_GF + ((tg * 2 + (mt >> 1)) * 108 + 54 * kh + (g >> 1)) * 1024 + ((g & 1) * 32 + (mt & 1) * 16 + t16) * 16;
  constexpr int AW = 9;
  u32x4 a[AW];
#pragma unroll
  for (int i = 0; i < AW; ++i) a[i] = bld128<AUX_SC1>(ws, abase + i * 2048);
  f32x4 acc[5] = {};
  const unsigned brow = kh * 15360 + (16 * (g >> 1) + 4 * (g & 1) + tq) * 160 + tp * 8;
#pragma unroll
  for (int c = 0; c < 9; ++c) {
    WAITV(0);
    LBAR();
    if (c + 3 < 9) dma_chunk(c + 3);
#pragma unroll
    for (int k = 0; k < 3; ++k) {
      const int s = 3 * c + k;
      const u32x4 cur = a[s % AW];
      if (s + AW < 27) a[s % AW] = bld128<AUX_SC1>(ws, abase + (s + AW) * 2048);
      lptr bb = lds + L5_R + (c & 3) * 30720 + brow + k * 5120;
#pragma unroll
      for (int nt = 0; nt < 5; ++nt) acc[nt] = MFMAS(cat8(trread(bb + nt * 32), trread(bb + nt * 32 + 1280)), asbf8(cur), acc[nt]);
    }
  }
  LAS f32x4* red = (LAS f32x4*)(lds + L5_RED) + mt * 320 + lane;
  if (kh == 1) {
#pragma unroll
    for (int nt = 0; nt < 5; ++nt) red[nt * 64] = acc[nt];
  }
  LBAR();
  if (kh == 0) {
    const int tok = tg * 64 + mt * 16 + t16;
#pragma unroll
    for (int nt = 0; nt < 5; ++nt) {
      const f32x4 o = red[nt * 64];
      const u32x2 r = {pk2(bflo(hres[nt][0]) + acc[nt][0] + o[0], bfhi(hres[nt][0]) + acc[nt][1] + o[1]), pk2(bflo(hres[nt][1]) + acc[nt][2] + o[2], bfhi(hres[nt][1]) + acc[nt][3] + o[3])};
      *(GAS u32x2*)(p.out + (size_t)tok * 1280 + cg * 160 + nt * 32 + g * 8) = r;
    }
  }
}

__global__ void __launch_bounds__(NTHR, 2) k_front(Params p) {
  extern __shared__ __attribute__((aligned(16))) char smem[];
  lptr lds = (lptr)smem;
  const rsrc_t ws = __builtin_amdgcn_make_buffer_rsrc((void*)p.ws, 0, (int)W_END, 0x00020000);
  if (blockIdx.x == 0 && threadIdx.x < 96) {
    const int i = threadIdx.x;
    if (i < 32) bst32<AUX_SC1>(ws, W_CNTO + i * 256, 0u); else if (i < 64) bst32<AUX_SC1>(ws, W_CNTH + (i - 32) * 256, 0u);
    else if (i < 80) bst32<AUX_SC1>(ws, W_CNTG + (i - 64) * 256, 0u); else if (i == 80) bst32<AUX_SC1>(ws, W_TMO, 0u);
  }
  phase1(p, lds, ws, blockIdx.x);
}

__global__ void __launch_bounds__(NTHR, 2) k_main(Params p) {
  extern __shared__ __attribute__((aligned(16))) char smem[];
  lptr lds = (lptr)smem;
  const rsrc_t ws = __builtin_amdgcn_make_buffer_rsrc((void*)p.ws, 0, (int)W_END, 0x00020000);
  const int b = blockIdx.x;
  u32x2 hres[5] = {};
  phase_attn(p, lds, ws, b);
  phase_oproj(p, lds, ws, b, hres);
  phase_gateup(p, lds, ws, b);
  phase_down(p, lds, ws, b, hres);
}

extern "C" void kernel_launch(void* const* d_in, const int* in_sizes, int n_in, void* d_out, int out_size, void* d_ws, size_t ws_size, hipStream_t stream) {
  Params p;
  p.x = (const char*)d_in[0]; p.wq = (const char*)d_in[1]; p.wk = (const char*)d_in[2]; p.wv = (const char*)d_in[3]; p.wo = (const char*)d_in[4];
  p.wg = (const char*)d_in[6]; p.wu = (const char*)d_in[7]; p.wd = (const char*)d_in[8];
  p.out = (char*)d_out; p.ws = (char*)d_ws;
  constexpr int LDS_BYTES = 163840;
  hipFuncSetAttribute((const void*)k_front, hipFuncAttributeMaxDynamicSharedMemorySize, LDS_BYTES);
  hipFuncSetAttribute((const void*)k_main, hipFuncAttributeMaxDynamicSharedMemorySize, LDS_BYTES);
  k_front<<<NWG, NTHR, LDS_BYTES, stream>>>(p);
  k_main<<<NWG, NTHR, LDS_BYTES, stream>>>(p);
}
```

```cpp
#include <hip/hip_runtime.h>
#include <stdint.h>

#define DI __device__ __forceinline__
#define LAS __attribute__((address_space(3)))
#define GAS __attribute__((address_space(1)))
typedef short bf16x8 __attribute__((ext_vector_type(8)));
typedef short bf16x4 __attribute__((ext_vector_type(4)));
typedef __bf16 bfv2 __attribute__((ext_vector_type(2)));
typedef float f32x2 __attribute__((ext_vector_type(2)));
typedef float f32x4 __attribute__((ext_vector_type(4)));
typedef float f32x16 __attribute__((ext_vector_type(16)));
typedef unsigned u32x2 __attribute__((ext_vector_type(2)));
typedef unsigned u32x4 __attribute__((ext_vector_type(4)));
typedef LAS char* lptr;

constexpr int SEQ = 2048, DM = 640, FF = 1728;
constexpr int NWG = 256, NTHR = 512;
constexpr float LN_EPS = 1e-5f;
constexpr float QSCALE = 0.22360679774997896f * 1.4426950408889634f;
constexpr unsigned KV_TILE = 2688, KV_K2 = 1024, KV_V = 1280, KV_VS = 352;

constexpr unsigned W_EPOCH = 0, W_TMO = 256;
constexpr unsigned W_FKV = 1024  , W_FRT = 2048  , W_KMAX = 4096  ;
constexpr unsigned W_CNTO = 8192, W_CNTH = 16384, W_CNTG = 24576;
constexpr unsigned W_KV = 65536;
constexpr unsigned W_OF = W_KV + 64 * KV_TILE;
constexpr unsigned W_HF = W_OF + SEQ * DM * 2;
constexpr unsigned W_HST = W_HF + SEQ * DM * 2;
constexpr unsigned W_GF = W_HST + 32 * 8 * 64 * 8;
constexpr unsigned W_WGU = W_GF + 128 * 54 * 1024;
constexpr unsigned W_WD = W_WGU + 108 * 40 * 1024;
constexpr unsigned W_END = W_WD + 8 * FF * 160;

#define STAMPW() do { } while (0)
#define STAMP(i) do { } while (0)
#define STW 0
#define STAMPX(i) do { STAMP(i); if (STW == (i)) STAMPW(); } while (0)

struct Params {
  const char* x; const char* wq; const char* wk; const char* wv; const char* wo;
  const char* wg; const char* wu; const char* wd;
  char* out; char* ws;
};

constexpr int AUX_SC1 = 16;
#define MFMA16(a, b, c) __builtin_amdgcn_mfma_f32_32x32x16_bf16((a), (b), (c), 0, 0, 0)
#define MFMA8(a, b, c) __builtin_amdgcn_mfma_f32_32x32x8bf16_1k((a), (b), (c), 0, 0, 0)
#define MFMAS(a, b, c) __builtin_amdgcn_mfma_f32_16x16x32_bf16((a), (b), (c), 0, 0, 0)
#define SBAR() __builtin_amdgcn_sched_barrier(0)
#define WAITV(n) asm volatile("s_waitcnt vmcnt(" #n ")" ::: "memory")
#define BAR() asm volatile("s_barrier" ::: "memory")
#define WAITL() __builtin_amdgcn_s_waitcnt(0xC07F)
#define LBAR() do { asm volatile("" ::: "memory"); WAITL(); BAR(); } while (0)
#define PIN(x) asm volatile("" : "+v"(x))

DI unsigned pk2(float lo, float hi) { f32x2 v = {lo, hi}; return __builtin_bit_cast(unsigned, __builtin_convertvector(v, bfv2)); }
DI float bflo(unsigned v) { return __uint_as_float(v << 16); }
DI float bfhi(unsigned v) { return __uint_as_float(v & 0xffff0000u); }
DI float dot2(unsigned a, unsigned b, float c) { return __builtin_amdgcn_fdot2_f32_bf16(__builtin_bit_cast(bfv2, a), __builtin_bit_cast(bfv2, b), c, false); }
constexpr unsigned ONES2 = 0x3f803f80u;
DI float swap32_sum(float v) { auto r = __builtin_amdgcn_permlane32_swap(__float_as_uint(v), __float_as_uint(v), false, false); return __uint_as_float(r[0]) + __uint_as_float(r[1]); }
DI float swap32_max(float v) { auto r = __builtin_amdgcn_permlane32_swap(__float_as_uint(v), __float_as_uint(v), false, false); return fmaxf(__uint_as_float(r[0]), __uint_as_float(r[1])); }
DI float swap16_sum(float v) { auto r = __builtin_amdgcn_permlane16_swap(__float_as_uint(v), __float_as_uint(v), false, false); return __uint_as_float(r[0]) + __uint_as_float(r[1]); }
template <int CTRL> DI float dppf(float v) { return __uint_as_float(__builtin_amdgcn_update_dpp(0u, __float_as_uint(v), CTRL, 0xf, 0xf, true)); }
DI float sum8(float v) { v += dppf<0xB1>(v); v += dppf<0x4E>(v); return v + dppf<0x141>(v); }
DI float max16(float v) { v = fmaxf(v, dppf<0x128>(v)); v = fmaxf(v, dppf<0x124>(v)); v = fmaxf(v, dppf<0x122>(v)); return fmaxf(v, dppf<0x121>(v)); }
DI float max64(float v) { v = max16(v); auto r = __builtin_amdgcn_permlane16_swap(__float_as_uint(v), __float_as_uint(v), false, false);
  return swap32_max(fmaxf(__uint_as_float(r[0]), __uint_as_float(r[1]))); }
typedef short v4i16_t __attribute__((ext_vector_type(4)));
DI bf16x4 trread(lptr p) { return __builtin_bit_cast(bf16x4, __builtin_amdgcn_ds_read_tr16_b64_v4i16((LAS v4i16_t*)p)); }
DI bf16x8 cat8(bf16x4 a, bf16x4 b) { return __builtin_shufflevector(a, b, 0, 1, 2, 3, 4, 5, 6, 7); }
DI u32x4 lds128(lptr p) { return *(LAS u32x4*)p; }
DI u32x2 lds64(lptr p) { return *(LAS u32x2*)p; }
DI bf16x8 asbf8(u32x4 v) { return __builtin_bit_cast(bf16x8, v); }
DI bf16x4 asbf4(u32x2 v) { return __builtin_bit_cast(bf16x4, v); }
DI u32x4 asu4(bf16x8 v) { return __builtin_bit_cast(u32x4, v); }
DI unsigned rfl(unsigned v) { return __builtin_amdgcn_readfirstlane(v); }
typedef __amdgpu_buffer_rsrc_t rsrc_t;
template <int AUX> DI u32x4 bld128(rsrc_t r, unsigned off) { return __builtin_amdgcn_raw_buffer_load_b128(r, off, 0, AUX); }
template <int AUX> DI u32x2 bld64(rsrc_t r, unsigned off) { return __builtin_amdgcn_raw_buffer_load_b64(r, off, 0, AUX); }
template <int AUX> DI unsigned bld32(rsrc_t r, unsigned off) { return __builtin_amdgcn_raw_buffer_load_b32(r, off, 0, AUX); }
template <int AUX> DI void bst128(rsrc_t r, unsigned off, u32x4 v) { __builtin_amdgcn_raw_buffer_store_b128(v, r, off, 0, AUX); }
template <int AUX> DI void bst64(rsrc_t r, unsigned off, u32x2 v) { __builtin_amdgcn_raw_buffer_store_b64(v, r, off, 0, AUX); }
template <int AUX> DI void bst32(rsrc_t r, unsigned off, unsigned v) { __builtin_amdgcn_raw_buffer_store_b32(v, r, off, 0, AUX); }
DI void glds(unsigned voff, const char* sbase, unsigned lds_base) {
  unsigned sv; lds_base = rfl(lds_base);
  asm volatile("s_mov_b32 %0, m0\n\ts_mov_b32 m0, %3\n\ts_nop 0\n\tglobal_load_lds_dwordx4 %1, %2\n\ts_mov_b32 m0, %0" : "=&s"(sv) : "v"(voff), "s"(sbase), "s"(lds_base) : "memory");
}
DI void glds_sc1(unsigned voff, const char* sbase, unsigned lds_base) {
  unsigned sv; lds_base = rfl(lds_base);
  asm volatile("s_mov_b32 %0, m0\n\ts_mov_b32 m0, %3\n\ts_nop 0\n\tglobal_load_lds_dwordx4 %1, %2 sc1\n\ts_mov_b32 m0, %0" : "=&s"(sv) : "v"(voff), "s"(sbase), "s"(lds_base) : "memory");
}
DI void touch(unsigned voff, const char* sbase, unsigned lds_dump) {
  unsigned sv; lds_dump = rfl(lds_dump);
  asm volatile("s_mov_b32 %0, m0\n\ts_mov_b32 m0, %3\n\ts_nop 0\n\tglobal_load_lds_dword %1, %2\n\ts_mov_b32 m0, %0" : "=&s"(sv) : "v"(voff), "s"(sbase), "s"(lds_dump) : "memory");
}
DI void touch_sc1(unsigned voff, const char* sbase, unsigned lds_dump) {
  unsigned sv; lds_dump = rfl(lds_dump);
  asm volatile("s_mov_b32 %0, m0\n\ts_mov_b32 m0, %3\n\ts_nop 0\n\tglobal_load_lds_dword %1, %2 sc1\n\ts_mov_b32 m0, %0" : "=&s"(sv) : "v"(voff), "s"(sbase), "s"(lds_dump) : "memory");
}

#define RLX_AGENT __ATOMIC_RELAXED, __HIP_MEMORY_SCOPE_AGENT
constexpr unsigned SPIN_CAP = 1u << 17;
DI bool spin_fail(unsigned& spins, char* wsp, unsigned epoch) {
  __builtin_amdgcn_s_sleep(1);
  GAS unsigned* tmo = (GAS unsigned*)(wsp + W_TMO);
  if (++spins > SPIN_CAP) { __hip_atomic_store(tmo, epoch, RLX_AGENT); return true; }
  return (spins & 255u) == 0u && rfl(__hip_atomic_load(tmo, RLX_AGENT)) == epoch;
}
template <int NW> DI void wait_flags(char* wsp, unsigned off, unsigned epoch, int lane) {
  for (unsigned spins = 0;;) {
    bool ok = true;
#pragma unroll
    for (int i = 0; i < NW; ++i) ok &= __hip_atomic_load((GAS unsigned*)(wsp + off) + i * 64 + lane, RLX_AGENT) == epoch;
    if (__all(ok)) break;
    if (spin_fail(spins, wsp, epoch)) break;
  }
  __builtin_amdgcn_fence(__ATOMIC_ACQUIRE, "wavefront");
}
DI void wait_cnt2(char* wsp, unsigned off0, unsigned off1, unsigned want, unsigned epoch) {
  if (threadIdx.x < 64) {
    GAS unsigned* word = (GAS unsigned*)(wsp + ((threadIdx.x & 1) ? off1 : off0));
    for (unsigned spins = 0;;) {
      if (__all(__hip_atomic_load(word, RLX_AGENT) >= want)) break;
      if (spin_fail(spins, wsp, epoch)) break;
    }
    __builtin_amdgcn_fence(__ATOMIC_ACQUIRE, "wavefront");
  }
  LBAR();
}
DI void signal_cnt(char* wsp, unsigned off) {
  WAITV(0);
  LBAR();
  if (threadIdx.x == 0) (void)__hip_atomic_fetch_add((GAS unsigned*)(wsp + off), 1u, RLX_AGENT);
}
DI void lds_add1(lptr p) { (void)__hip_atomic_fetch_add((LAS unsigned*)p, 1u, __ATOMIC_RELAXED, __HIP_MEMORY_SCOPE_WORKGROUP); }
DI void lds_wait_ge(lptr p, unsigned want) {
  while ((unsigned)rfl(__hip_atomic_load((LAS unsigned*)p, __ATOMIC_RELAXED, __HIP_MEMORY_SCOPE_WORKGROUP)) < want) __builtin_amdgcn_s_sleep(2);
  asm volatile("" ::: "memory");
}

constexpr unsigned L1_X = 0, L1_S = 81920, L1_WK = L1_S + 4 * 10240, L1_PART = L1_WK + 25600;
constexpr unsigned L1_CS = L1_PART + 12672  , L1_ST = L1_CS + 1024  , L1_KC = L1_ST + 512  , L1_DUMP = L1_KC + 128;
static_assert(L1_DUMP + 256 <= 163840, "lds");

DI void phase1(const Params& p, lptr lds, rsrc_t ws, int b, u32x4& q1, u32x2& q2, unsigned& epoch) {
  const int tid = threadIdx.x, lane = tid & 63, w = rfl(tid >> 6);
  const int r32 = lane & 31, h = lane >> 5;
  const int xcd = b & 7, rk = b >> 3, tg = 4 * xcd + (rk >> 3), hg = rk & 7;
  const unsigned lds0 = (unsigned)(uintptr_t)lds, l16 = lane * 16;
  unsigned ep = __hip_atomic_load((GAS unsigned*)(p.ws + W_EPOCH), RLX_AGENT);
  {
    const char* xb = p.x + (size_t)(tg * 64 + 8 * w) * 1280;
    const int tl = lane >> 4;
    unsigned v0 = tl * 1280 + ((lane ^ (8 * w + tl)) & 15) * 16, v1 = (4 + tl) * 1280 + ((lane ^ (8 * w + 4 + tl)) & 15) * 16, xd = lds0 + L1_X + 8 * w * 256;
#pragma unroll 1
    for (int a = 0; a < 5; ++a) { glds(v0, xb, xd); glds(v1, xb, xd + 1024); v0 += 256; v1 += 256; xd += 16384; }
  }
  if (hg < 4) {
    const char* W = (hg < 2 ? p.wk : p.wv) + w * 1024;
#pragma unroll
    for (int i = 0; i < 3; ++i) glds(l16 + i * 8192, W, lds0 + L1_WK + w * 1024 + i * 8192);
    if (w == 0) glds(l16 + 3 * 8192, W, lds0 + L1_WK + 3 * 8192);
  }
  BAR();
  const char* wqb = p.wq + hg * 160;
  unsigned vq[3], dq[3];
#pragma unroll
  for (int i = 0; i < 3; ++i) {
    const int n = i < 2 ? w + 8 * i : (w < 6 ? w + 16 : w + 8), cc = n >= 11, pc = n - 11 * cc;
    const int slot = 6 * pc + lane / 10, row = (slot & ~7) | ((slot & 1) << 2) | ((slot >> 1) & 3);
    vq[i] = (64 * cc + row) * 1280 + (lane % 10) * 16; dq[i] = cc * 10240 + pc * 960;
  }
  const bool m1 = w != 2 || lane < 40, m2 = w < 6 ? (w != 5 || lane < 40) : lane == 0;
  auto dma_pair = [&](const char* src, unsigned dst) {
    glds(vq[0], src, dst + dq[0]);
    if (m1) glds(vq[1], src, dst + dq[1]);
    if (m2) glds(vq[2], src, dst + dq[2]);
  };
  dma_pair(wqb, lds0 + L1_S); dma_pair(wqb + 128 * 1280, lds0 + L1_S + 20480);
  SBAR();
  STAMPX(8);
  const int i16 = lane & 15, tq = i16 >> 2, tp = i16 & 3, grp = (lane >> 4) & 1;
  WAITV(6);
  LBAR();
  PIN(ep); epoch = rfl(ep) + 1u;
  STAMPX(1);
  const int kth = hg & 1; const bool isV = hg >= 2;
  f32x16 kacc = {}; float kcs = 0.f;
  if (w >= 4) {
#pragma unroll
    for (int ps = 0; ps < 2; ++ps) {
      const int t = (ps ? 32 - 32 * kth : 32 * kth) + 8 * (w - 4) + (lane >> 3), e = lane & 7;
      u32x4 v[10];
#pragma unroll
      for (int i = 0; i < 10; ++i) v[i] = lds128(lds + L1_X + (i >> 1) * 16384 + t * 256 + ((e + 8 * (i & 1) + 8 * ((t >> 1) & 1)) & 15) * 16);
      float a4[4] = {}, b4[4] = {};
#pragma unroll
      for (int i = 0; i < 10; ++i)
#pragma unroll
        for (int j = 0; j < 4; ++j) { a4[j] = dot2(v[i][j], v[i][j], a4[j]); b4[j] = dot2(v[i][j], ONES2, b4[j]); }
      const float sq = sum8((a4[0] + a4[1]) + (a4[2] + a4[3])), sm = sum8((b4[0] + b4[1]) + (b4[2] + b4[3]));
      const float mean = sm * (1.f / DM), rstd = rsqrtf(fmaxf(sq * (1.f / DM) - mean * mean, 0.f) + LN_EPS);
      if (e == 0) { ((LAS float*)(lds + L1_ST))[t] = mean; ((LAS float*)(lds + L1_ST))[64 + t] = rstd; }
      if (ps == 0) {
        LBAR(); dma_pair(wqb + 256 * 1280, lds0 + L1_WK);
        const int ln = ((rk >> 3) * 4 + (w - 4)) * 32 + r32;
        if (h == 0) touch((ln >> 1) * 1280 + (ln & 1) * 128, wqb + 384 * 1280 - ((hg * 160) & 127), lds0 + L1_DUMP);
      }
    }
  } else if (hg < 4) {
    lptr wb = lds + L1_WK + (160 * w + 8 * h + tq) * 40 + grp * 32 + 8 * tp;
    const int tok = kth * 32 + r32;
    u32x4 xf[10]; bf16x8 wf[10];
#pragma unroll
    for (int i = 0; i < 10; ++i) { const int c = 20 * w + 2 * i + h;
      xf[i] = lds128(lds + L1_X + (c >> 4) * 16384 + tok * 256 + ((c ^ tok) & 15) * 16); wf[i] = cat8(trread(wb + i * 640), trread(wb + i * 640 + 160)); }
    if (isV) {
#pragma unroll
      for (int i = 0; i < 10; ++i) kacc = MFMA16(asbf8(xf[i]), wf[i], kacc);
    } else {
#pragma unroll
      for (int i = 0; i < 10; ++i) kacc = MFMA16(wf[i], asbf8(xf[i]), kacc);
    }
    float c4[4] = {};
#pragma unroll
    for (int i = 0; i < 10; ++i) { const u32x4 wu = asu4(wf[i]);
#pragma unroll
      for (int j = 0; j < 4; ++j) c4[j] = dot2(wu[j], ONES2, c4[j]); }
    kcs = swap32_sum((c4[0] + c4[1]) + (c4[2] + c4[3]));
    if (w > 0) {
      LAS f32x4* pa = (LAS f32x4*)(lds + L1_PART + (w - 1) * 4224) + lane;
#pragma unroll
      for (int g = 0; g < 4; ++g) pa[g * 64] = f32x4{kacc[4 * g], kacc[4 * g + 1], kacc[4 * g + 2], kacc[4 * g + 3]};
      if (h == 0) ((LAS float*)(lds + L1_PART + (w - 1) * 4224 + 4096))[r32] = kcs;
    }
  }
  if (w < 4) {
    LBAR();
    dma_pair(wqb + 256 * 1280, lds0 + L1_WK);
  }
  if (hg < 4 && w == 0) {
#pragma unroll
    for (int v = 0; v < 3; ++v) {
      LAS f32x4* pa = (LAS f32x4*)(lds + L1_PART + v * 4224) + lane;
#pragma unroll
      for (int g = 0; g < 4; ++g) { const f32x4 t = pa[g * 64]; kacc[4 * g] += t[0]; kacc[4 * g + 1] += t[1]; kacc[4 * g + 2] += t[2]; kacc[4 * g + 3] += t[3]; }
      kcs += ((LAS float*)(lds + L1_PART + v * 4224 + 4096))[r32];
    }
    const int tile = tg * 2 + kth;
    const LAS float* st = (const LAS float*)(lds + L1_ST) + kth * 32;
    if (lane == 0 && hg == 0) { bst32<AUX_SC1>(ws, W_CNTO + tg * 256, 0u); bst32<AUX_SC1>(ws, W_CNTH + tg * 256, 0u); }
    if (lane == 0 && hg == 1 && !(tg & 1)) bst32<AUX_SC1>(ws, W_CNTG + (tg >> 1) * 256, 0u);
    if (!isV) {
      LAS float* tab = (LAS float*)(lds + L1_KC);
      if (h == 0) tab[r32] = kcs;
      const float mean = st[r32], rstd = st[64 + r32];
      asm volatile("" ::: "memory"); WAITL(); asm volatile("" ::: "memory");
      float kv[12];
#pragma unroll
      for (int g = 0; g < 3; ++g) { const f32x4 c4 = *(LAS f32x4*)(tab + 8 * g + 4 * h);
#pragma unroll
        for (int j = 0; j < 4; ++j) kv[4 * g + j] = (kacc[4 * g + j] - mean * c4[j]) * rstd; }
      const u32x4 k1 = {pk2(kv[0], kv[1]), pk2(kv[2], kv[3]), pk2(kv[4], kv[5]), pk2(kv[6], kv[7])};
      const u32x2 k2 = {pk2(kv[8], kv[9]), pk2(kv[10], kv[11])};
      bst128<AUX_SC1>(ws, W_KV + tile * KV_TILE + l16, k1);
      if (h == 0) bst64<AUX_SC1>(ws, W_KV + tile * KV_TILE + KV_K2 + r32 * 8, k2);
      float n2 = 0.f;
#pragma unroll
      for (int j = 0; j < 4; ++j) n2 = dot2(k1[j], k1[j], n2);
      if (h == 0) { n2 = dot2(k2[0], k2[0], n2); n2 = dot2(k2[1], k2[1], n2); }
      n2 = max64(swap32_sum(n2));
      if (lane == 0) bst32<AUX_SC1>(ws, W_KMAX + tile * 4, __float_as_uint(n2));
    } else {
      const unsigned cv = r32 == 20 ? ONES2 : 0u;
#pragma unroll
      for (int s = 0; s < 2; ++s) {
        float vv[8];
#pragma unroll
        for (int g = 0; g < 2; ++g) { const f32x4 m4 = *(const LAS f32x4*)(st + 16 * s + 8 * g + 4 * h), r4 = *(const LAS f32x4*)(st + 64 + 16 * s + 8 * g + 4 * h);
#pragma unroll
          for (int j = 0; j < 4; ++j) vv[4 * g + j] = (kacc[8 * s + 4 * g + j] - m4[j] * kcs) * r4[j]; }
        u32x4 o = {pk2(vv[0], vv[1]), pk2(vv[2], vv[3]), pk2(vv[4], vv[5]), pk2(vv[6], vv[7])};
        if (r32 >= 20) o = u32x4{cv, cv, cv, cv};
        if (r32 < 22) bst128<AUX_SC1>(ws, W_KV + tile * KV_TILE + KV_V + (2 * s + h) * KV_VS + r32 * 16, o);
      }
    }
  }
  if (w >= 4) WAITV(4); else if (hg < 4 && w == 0) WAITV(5); else WAITV(3);
  LBAR();
  STAMPX(2);
  {
    const int hh = w & 3, th = w >> 2, tok = th * 32 + r32;
    f32x16 acc = {}; float c4[4] = {};
    const unsigned wrow = (8 * h + 2 * tq) * 160 + hh * 40 + grp * 32 + 8 * tp;
    unsigned xo[8];
#pragma unroll
    for (int i = 0; i < 8; ++i) xo[i] = L1_X + tok * 256 + (((2 * i + h) ^ tok) & 15) * 16;
    u32x4 xf[2][4]; bf16x8 wf[2][4];
    unsigned xa = 0, wa = L1_S + wrow;
#define Q_RD1(e, ks, XA, WA) do { xf[e][ks] = lds128(lds + xo[4 * (e) + (ks)] + (XA)); lptr a_ = lds + (WA) + (e) * 10240 + (ks) * 2560; wf[e][ks] = cat8(trread(a_), trread(a_ + 160)); } while (0)
#define Q_STEP(e, XA, WA) do { _Pragma("unroll") for (int ks = 0; ks < 4; ++ks) { acc = MFMA16(wf[e][ks], asbf8(xf[e][ks]), acc); Q_RD1(1 - (e), ks, XA, WA); SBAR(); } } while (0)
#define Q_CS(e) do { _Pragma("unroll") for (int ks = 0; ks < 4; ++ks) { const u32x4 wu = asu4(wf[e][ks]); _Pragma("unroll") for (int j = 0; j < 4; ++j) c4[j] = dot2(wu[j], ONES2, c4[j]); } } while (0)
#pragma unroll
    for (int ks = 0; ks < 4; ++ks) Q_RD1(0, ks, xa, wa);
#pragma unroll 1
    for (int pr = 0; pr < 5; ++pr) {
      Q_STEP(0, xa, wa);
      if (pr == 1 || pr == 2) WAITV(3); else if (pr == 3) WAITV(0);
      if (pr == 1 && w == 0 && hg < 4 && lane == 0)
        __hip_atomic_store((GAS unsigned*)(p.ws + W_FKV) + tg * 4 + hg, epoch, RLX_AGENT);
      LBAR();
      const unsigned wn = pr == 1 ? L1_WK + wrow : (pr == 2 || pr == 4 ? L1_S + wrow : wa + 20480);
      if (pr < 2) dma_pair(wqb + (size_t)(pr + 3) * 128 * 1280, lds0 + L1_S + pr * 20480);
      if (th == 0) Q_CS(0); else Q_CS(1);
      SBAR();
      xa = pr < 4 ? xa + 16384 : 0;
      Q_STEP(1, xa, wn);
      wa = wn;
    }
#undef Q_RD1
#undef Q_STEP
#undef Q_CS
    const float cs = swap32_sum((c4[0] + c4[1]) + (c4[2] + c4[3]));
    if (h == 0) ((LAS float*)(lds + L1_CS))[w * 32 + r32] = cs;
    const float mean = ((LAS float*)(lds + L1_ST))[tok], rstd = ((LAS float*)(lds + L1_ST))[64 + tok] * QSCALE;
    LBAR();
    float qv[12];
#pragma unroll
    for (int g = 0; g < 3; ++g) {
      const f32x4 ca = *(LAS f32x4*)(lds + L1_CS + (hh * 32 + 8 * g + 4 * h) * 4), cb = *(LAS f32x4*)(lds + L1_CS + ((hh + 4) * 32 + 8 * g + 4 * h) * 4);
#pragma unroll
      for (int j = 0; j < 4; ++j) qv[4 * g + j] = (acc[4 * g + j] - mean * (ca[j] + cb[j])) * rstd;
    }
    q1 = u32x4{pk2(qv[0], qv[1]), pk2(qv[2], qv[3]), pk2(qv[4], qv[5]), pk2(qv[6], qv[7])};
    q2 = h ? u32x2{0u, 0u} : u32x2{pk2(qv[8], qv[9]), pk2(qv[10], qv[11])};
  }
  STAMPX(3);
}

constexpr unsigned L2_FLAG = 60 * KV_TILE, L2_CR = L2_FLAG + 16, L2_CP = L2_FLAG + 32, L2_CS = L2_FLAG + 48, L2_DUMP = L2_FLAG + 256;
constexpr unsigned L2_RT = 52 * KV_TILE;
constexpr unsigned L3_WO = 0, L3_RED = 102400;
#define PRIO_M 6

DI void phase_attn(const Params& p, lptr lds, rsrc_t ws, int b, const u32x4 q1, const u32x2 q2, unsigned epoch) {
  const int tid = threadIdx.x, lane = tid & 63, w = rfl(tid >> 6);
  const int r32 = lane & 31, h = lane >> 5;
  const int xcd = b & 7, rk = b >> 3, tg = 4 * xcd + (rk >> 3), hg = rk & 7;
  const int hh = w & 3, th = w >> 2;
  const unsigned lds0 = (unsigned)(uintptr_t)lds, l16 = lane * 16;
  const char* kvg = p.ws + W_KV;
  auto dma3 = [&](int c) {
    const unsigned dst = lds0 + (c == 15 ? 0 : c) * 4 * KV_TILE; const char* src = kvg + c * 4 * KV_TILE;
    glds_sc1(l16, src + hh * 1024, rfl(dst + hh * 1024));
    glds_sc1(l16, src + (hh + 4) * 1024, rfl(dst + (hh + 4) * 1024));
    const int n = hh == 3 ? 7 : hh + 8;
    if (hh == 2) { if (lane < 32) glds_sc1(l16, src + n * 1024, rfl(dst + n * 1024)); } else glds_sc1(l16, src + n * 1024, rfl(dst + n * 1024));
  };
  auto stage_first = [&]() { if (th == 0) { dma3(0); dma3(2); } else dma3(1); };
  u32x4 rg[2][4], rd[3];
  const int ga = b * 4 + hh, dj0 = (ga - 304) * 3;
  if (th == 0) {
#pragma unroll
    for (int e = 0; e < 2; ++e) { const int gj = ga + 1024 * e;
      if (gj < 1080) { const int cgf = gj / 40, r = gj % 40; const char* W = (r & 1) ? p.wu : p.wg;
#pragma unroll
        for (int i = 0; i < 4; ++i) rg[e][i] = *(const GAS u32x4*)(W + (size_t)(32 * (r >> 1) + 8 * i + (lane >> 3)) * (FF * 2) + cgf * 128 + (lane & 7) * 16); } }
    if (ga >= 304) {
#pragma unroll
      for (int i = 0; i < 3; ++i) rd[i] = *(const GAS u32x4*)(p.wd + (size_t)(dj0 + i) * 1024 + l16);
    }
  }
  auto retile = [&]() {
    const int i16 = lane & 15, tq = i16 >> 2, tp = i16 & 3, g4 = lane >> 4;
    lptr sc = lds + L2_RT + hh * 4096;
#pragma unroll
    for (int e = 0; e < 2; ++e) { const int gj = ga + 1024 * e;
      if (gj < 1080) { const int cgf = gj / 40, r = gj % 40;
#pragma unroll
        for (int i = 0; i < 4; ++i) *(LAS u32x4*)(sc + i * 1024 + l16) = rg[e][i];
#pragma unroll
        for (int u = 0; u < 4; ++u) { lptr a = sc + (8 * g4 + tq) * 128 + u * 32 + tp * 8;
          bst128<AUX_SC1>(ws, W_WGU + (((cgf * 4 + u) * 20 + (r >> 1)) * 2 + (r & 1)) * 1024 + l16, asu4(cat8(trread(a), trread(a + 512)))); } } }
    if (ga >= 304) {
#pragma unroll
      for (int i = 0; i < 3; ++i) { const int a5 = (dj0 + i) * 4 + (lane >> 4), f = (a5 * 52429) >> 18, c = (a5 - 5 * f) * 16 + (lane & 15), cg = (c * 6554) >> 16, cc = c - cg * 10;
        bst128<AUX_SC1>(ws, W_WD + (cg * FF + ((f & ~12) | ((f & 4) << 1) | ((f & 8) >> 1))) * 160 + cc * 16, rd[i]); } }
  };
  if (w == 0) wait_flags<2>(p.ws, W_FKV, epoch, lane);
  if (tid < 7) *(LAS unsigned*)(lds + L2_FLAG + tid * 16) = 0u;
  LBAR();
  STAMPX(4);
  float kmx = __uint_as_float(bld32<AUX_SC1>(ws, W_KMAX + lane * 4));
  stage_first();
  float ref;
  {
    float n2 = 0.f;
#pragma unroll
    for (int j = 0; j < 4; ++j) n2 = dot2(q1[j], q1[j], n2);
    n2 = dot2(q2[0], q2[0], n2); n2 = dot2(q2[1], q2[1], n2);
    n2 = swap32_sum(n2);
    kmx = max64(kmx);
    ref = sqrtf(n2 * kmx) * 1.001f;
  }
  const int rv = r32 < 21 ? r32 : 21;
  const unsigned oK1 = l16, oK2 = KV_K2 + r32 * 8, oV = KV_V + h * KV_VS + rv * 16;
  f32x16 o;
  for (int attempt = 0;; ++attempt) {
    o = f32x16{};
    f32x16 cb;
#pragma unroll
    for (int j = 0; j < 16; ++j) cb[j] = -ref;
    PIN(cb);
    WAITV(0);
    LBAR();
    STAMPX(9);
    lptr aK1 = lds + oK1; lptr aK2 = lds + oK2; lptr aV = lds + oV;
    u32x4 k1 = lds128(aK1); u32x2 k2 = lds64(aK2);
    f32x16 sc = MFMA8(asbf4(k2), asbf4(q2), cb); sc = MFMA16(asbf8(k1), asbf8(q1), sc);
    k1 = lds128(aK1 + KV_TILE); k2 = lds64(aK2 + KV_TILE);
    u32x4 v0 = lds128(aV), v1 = {}, p0, p1 = {};
    u32x4 k1b, v0b, v1b; u32x2 k2b;
    f32x16 sn;
#define EXP2(a) __builtin_amdgcn_exp2f(a)
#define GRP(MF, SC, i, P, j) do { MF; SBAR(); { const float e0 = EXP2(SC[i]), e1 = EXP2(SC[i + 1]), e2 = EXP2(SC[i + 2]), e3 = EXP2(SC[i + 3]); P[j] = pk2(e0, e1); P[j + 1] = pk2(e2, e3); } PIN(P); SBAR(); } while (0)
#define TILE(SC, SN, K1, K2, V0, V1, K1N, K2N, V0N, V1N, PK1, PK2, PV0, PV1) do { \
      WAITL(); \
      K2N = lds64(PK2); K1N = lds128(PK1); V0N = lds128(PV0); V1N = lds128(PV1); SBAR(); \
      GRP(SN = MFMA8(asbf4(K2), asbf4(q2), cb), SC, 0, p0, 0); \
      GRP(o = MFMA16(asbf8(V1), asbf8(p1), o), SC, 4, p0, 2); \
      GRP(SN = MFMA16(asbf8(K1), asbf8(q1), SN), SC, 8, p1, 0); \
      GRP(o = MFMA16(asbf8(V0), asbf8(p0), o), SC, 12, p1, 2); } while (0)
#define TA(T) TILE(sc, sn, k1, k2, v0, v1, k1b, k2b, v0b, v1b, aK1 + ((T) + 2) * KV_TILE, aK2 + ((T) + 2) * KV_TILE, aV + ((T) + 1) * KV_TILE, aV + (T) * KV_TILE + 2 * KV_VS)
#define TB(T) TILE(sn, sc, k1b, k2b, v0b, v1b, k1, k2, v0, v1, aK1 + ((T) + 2) * KV_TILE, aK2 + ((T) + 2) * KV_TILE, aV + ((T) + 1) * KV_TILE, aV + (T) * KV_TILE + 2 * KV_VS)
#define ADV() do { aK1 += 8 * KV_TILE; aK2 += 8 * KV_TILE; aV += 8 * KV_TILE; } while (0)
#define BODY8() do { TA(0); TB(1); TA(2); TB(3); TA(4); TB(5); TA(6); TB(7); ADV(); } while (0)
    if (th == 1) {
      __builtin_amdgcn_s_setprio(2);
      dma3(3); SBAR(); TA(0); dma3(4); SBAR(); TB(1); dma3(5); SBAR(); TA(2); dma3(6); SBAR(); TB(3);
      dma3(7); SBAR(); TA(4); dma3(8); SBAR(); TB(5); dma3(9); SBAR(); TA(6); dma3(10); SBAR(); TB(7); ADV();
      if (lane == 0) lds_add1(lds + L2_CP);
      WAITV(6); if (lane == 0) lds_add1(lds + L2_CS);
      lds_wait_ge(lds + L2_CS, 4u);
      dma3(11); SBAR(); TA(0); dma3(12); SBAR(); TB(1); TA(2); TB(3); TA(4); TB(5); TA(6); TB(7); ADV();
      WAITV(0); if (lane == 0) lds_add1(lds + L2_CS + 16);
#pragma nounroll
      for (int m = 2; m < 7; ++m) {
        if (m == 3) { if (attempt == 0) lds_wait_ge(lds + L2_CR, 4u); dma3(13); dma3(14); }
        if (m == 4) { WAITV(0); if (lane == 0) lds_add1(lds + L2_CS + 32); lds_wait_ge(lds + L2_CS + 16, 4u); }
        if (m == 5) { lds_wait_ge(lds + L2_CP, 8u); dma3(15); }
        if (m == 6) { WAITV(0); if (lane == 0) lds_add1(lds + L2_CS + 48); lds_wait_ge(lds + L2_CS + 32, 4u); }
        if (m == PRIO_M) __builtin_amdgcn_s_setprio(0);
        BODY8();
      }
    } else {
      __builtin_amdgcn_s_setprio(1);
      TA(0); TB(1); TA(2); TB(3);
      if (attempt == 0) retile();
      TA(4); TB(5); TA(6); TB(7); ADV();
      if (attempt == 0) {
        WAITV(0);
        unsigned old = 0; if (lane == 0) old = __hip_atomic_fetch_add((LAS unsigned*)(lds + L2_CR), 1u, __ATOMIC_RELAXED, __HIP_MEMORY_SCOPE_WORKGROUP);
        if (lane == 0 && old == 3u) __hip_atomic_store((GAS unsigned*)(p.ws + W_FRT) + b, epoch, RLX_AGENT);
      }
#pragma nounroll
      for (int m = 1; m < 7; ++m) {
        if (m == 1) { if (lane == 0) lds_add1(lds + L2_CP); lds_wait_ge(lds + L2_CS, 4u); }
        if (m == 3 && attempt == 0) {
          wait_flags<4>(p.ws, W_FRT, epoch, lane);
          if (b == 0 && w == 0 && lane == 0) __hip_atomic_store((GAS unsigned*)(p.ws + W_EPOCH), epoch, RLX_AGENT);
          const int ngl = rk & 3, u0 = 27 * (xcd & 3) + 7 * ngl, part = (rk >> 2) * 4 + hh;
          const char* src = p.ws + W_WGU + (size_t)u0 * 40960;
          const int nl = (ngl == 3 ? 6 : 7) * 10;
          touch_sc1((part * nl + lane) * 128, src, lds0 + L2_DUMP);
          if (lane < nl - 64) touch_sc1((part * nl + 64 + lane) * 128, src, lds0 + L2_DUMP);
          const int ln = ((rk >> 3) * 4 + hh) * 80 + lane; const char* wsrc = p.wo + ((hg * 160) & ~127);
          touch((ln >> 1) * 1280 + (ln & 1) * 128, wsrc, lds0 + L2_DUMP);
          if (lane < 16) touch(((ln + 64) >> 1) * 1280 + (ln & 1) * 128, wsrc, lds0 + L2_DUMP);
        }
        if (m == 4) lds_wait_ge(lds + L2_CS + 16, 4u);
        if (m == 6) lds_wait_ge(lds + L2_CS + 32, 4u);
        BODY8();
      }
    }
    lds_wait_ge(lds + L2_CS + 48, 4u);
    {
      lptr zK1 = lds + oK1; lptr zK2 = lds + oK2; lptr zV = lds + oV;
      TA(0); TB(1);
      TILE(sc, sn, k1, k2, v0, v1, k1b, k2b, v0b, v1b, zK1, zK2, aV + 3 * KV_TILE, aV + 2 * KV_TILE + 2 * KV_VS);
      TILE(sn, sc, k1b, k2b, v0b, v1b, k1, k2, v0, v1, zK1 + KV_TILE, zK2 + KV_TILE, zV, aV + 3 * KV_TILE + 2 * KV_VS);
      aK1 = zK1; aK2 = zK2; aV = zV;
      TA(0); TB(1); TA(2); TB(3);
    }
    WAITL();
    o = MFMA16(asbf8(v1), asbf8(p1), o);
    __builtin_amdgcn_s_setprio(0);
    STAMPX(10);
    const float lsum = __uint_as_float(__builtin_amdgcn_permlane32_swap(__float_as_uint(o[8]), __float_as_uint(o[8]), false, false)[1]);
    if (attempt == 1) { LBAR(); break; }
    const bool bad = !(lsum >= 5.421010862427522e-20f);
    if (__any(bad)) *(LAS unsigned*)(lds + L2_FLAG) = 1u;
    LBAR();
    if (*(LAS unsigned*)(lds + L2_FLAG) == 0u) break;
    {
      float mx = -3.0e38f;
      for (int t = 0; t < 64; ++t) {
        const unsigned base = W_KV + t * KV_TILE;
        const u32x4 a1 = bld128<AUX_SC1>(ws, base + l16);
        const u32x2 a2 = bld64<AUX_SC1>(ws, base + KV_K2 + r32 * 8);
        f32x16 s = MFMA8(asbf4(a2), asbf4(q2), f32x16{}); s = MFMA16(asbf8(a1), asbf8(q1), s);
#pragma unroll
        for (int j = 0; j < 16; ++j) mx = fmaxf(mx, s[j]);
      }
      ref = swap32_max(mx);
      if (tid < 5) *(LAS unsigned*)(lds + L2_CP + tid * 16) = 0u;
      stage_first();
    }
  }
  {
    const float lsum = __uint_as_float(__builtin_amdgcn_permlane32_swap(__float_as_uint(o[8]), __float_as_uint(o[8]), false, false)[1]);
    const float inv = 1.0f / lsum;
    const u32x4 oa = {pk2(o[0] * inv, o[1] * inv), pk2(o[2] * inv, o[3] * inv), pk2(o[4] * inv, o[5] * inv), pk2(o[6] * inv, o[7] * inv)};
    const u32x2 ob = {pk2(o[8] * inv, o[9] * inv), pk2(o[10] * inv, o[11] * inv)};
    const unsigned tile16 = tg * 4 + th * 2 + (r32 >> 4), g0 = 10 * hg + 5 * (hh >> 1);
    bst128<AUX_SC1>(ws, W_OF + ((tile16 * 80 + g0 + 2 * (hh & 1) + h) * 16 + (r32 & 15)) * 16, oa);
    if (h == 0) bst64<AUX_SC1>(ws, W_OF + ((tile16 * 80 + g0 + 4) * 16 + (r32 & 15)) * 16 + 8 * (hh & 1), ob);
  }
  {
    const unsigned vo = (lane / 10) * 1280 + (lane % 10) * 16; const char* src = p.wo + hg * 160;
#pragma unroll
    for (int i = 0; i < 14; ++i) { int n = w + 8 * i; if (n > 106) n = 105;
      if (n == 106) { if (lane < 40) glds(vo, src + (size_t)n * 7680, rfl(lds0 + L3_WO + n * 960)); } else glds(vo, src + (size_t)n * 7680, rfl(lds0 + L3_WO + n * 960)); }
  }
  WAITV(14);
  LBAR();
  if (tid == 0) (void)__hip_atomic_fetch_add((GAS unsigned*)(p.ws + W_CNTO + tg * 256), 1u, RLX_AGENT);
}

DI void phase_oproj(const Params& p, lptr lds, rsrc_t ws, int b, u32x2 (&hres)[5], unsigned epoch) {
  const int tid = threadIdx.x, lane = tid & 63, w = rfl(tid >> 6);
  const int xcd = b & 7, rk = b >> 3, tg = 4 * xcd + (rk >> 3), cg = rk & 7;
  const int mt = w & 3, kh = w >> 2, t16 = lane & 15, g = lane >> 4, tq = t16 >> 2, tp = t16 & 3;
  const int tok = tg * 64 + mt * 16 + t16;
  u32x2 xr[5] = {};
  if (kh == 0) {
#pragma unroll
    for (int nt = 0; nt < 5; ++nt) xr[nt] = *(const GAS u32x2*)(p.x + (size_t)tok * 1280 + cg * 160 + nt * 32 + g * 8);
  }
  unsigned r1[10], r2[10];
#pragma unroll
  for (int i = 0; i < 10; ++i) {
    const int t = 4 * i + g, a5 = (t * 13) >> 6, m = t - 5 * a5, pair = 8 * kh + a5;
    const int last = m >> 2, ra = pair * 40 + (m >> 1) * 20 + (m & 1) * 4 - last * 24;
    r1[i] = L3_WO + (ra + tq) * 160 + tp * 8; r2[i] = r1[i] + (8 + 12 * last) * 160;
  }
  WAITV(0);
  wait_cnt2(p.ws, W_CNTO + tg * 256, W_CNTO + tg * 256, 8u, epoch);
  STAMPX(13);
  bf16x8 bf[2][5];
  u32x4 a[10];
#pragma unroll
  for (int i = 0; i < 10; ++i) a[i] = bld128<AUX_SC1>(ws, W_OF + ((tg * 4 + mt) * 80 + 4 * (10 * kh + i)) * 256 + lane * 16);
#pragma unroll
  for (int nt = 0; nt < 5; ++nt) bf[0][nt] = cat8(trread(lds + r1[0] + nt * 32), trread(lds + r2[0] + nt * 32));
  f32x4 acc[5] = {};
#pragma unroll
  for (int i = 0; i < 10; ++i) {
    if (i + 1 < 10) {
#pragma unroll
      for (int nt = 0; nt < 5; ++nt) bf[(i + 1) & 1][nt] = cat8(trread(lds + r1[i + 1] + nt * 32), trread(lds + r2[i + 1] + nt * 32));
    }
    SBAR();
#pragma unroll
    for (int nt = 0; nt < 5; ++nt) acc[nt] = MFMAS(bf[i & 1][nt], asbf8(a[i]), acc[nt]);
    SBAR();
  }
  STAMPX(14);
  LAS f32x4* red = (LAS f32x4*)(lds + L3_RED) + mt * 320 + lane;
  if (kh == 1) {
#pragma unroll
    for (int nt = 0; nt < 5; ++nt) red[nt * 64] = acc[nt];
  }
  LBAR();
  if (kh == 0) {
    float s1 = 0.f, s2 = 0.f;
#pragma unroll
    for (int nt = 0; nt < 5; ++nt) {
      const f32x4 o = red[nt * 64];
      const float h0 = bflo(xr[nt][0]) + acc[nt][0] + o[0], h1 = bfhi(xr[nt][0]) + acc[nt][1] + o[1], h2 = bflo(xr[nt][1]) + acc[nt][2] + o[2], h3 = bfhi(xr[nt][1]) + acc[nt][3] + o[3];
      const u32x2 hv = {pk2(h0, h1), pk2(h2, h3)};
      hres[nt] = hv;
      s1 = dot2(hv[0], ONES2, s1); s1 = dot2(hv[1], ONES2, s1); s2 = dot2(hv[0], hv[0], s2); s2 = dot2(hv[1], hv[1], s2);
      const int gam = 10 * cg + 2 * nt + (g >> 1);
      bst64<AUX_SC1>(ws, W_HF + (((tg * 4 + mt) * 20 + (gam >> 2)) * 64 + (gam & 3) * 16 + t16) * 16 + (g & 1) * 8, hv);
    }
    s1 = swap32_sum(swap16_sum(s1)); s2 = swap32_sum(swap16_sum(s2));
    if (g == 0) bst64<AUX_SC1>(ws, W_HST + ((tg * 8 + cg) * 64 + mt * 16 + t16) * 8, u32x2{__float_as_uint(s1), __float_as_uint(s2)});
  }
  signal_cnt(p.ws, W_CNTH + tg * 256);
}

constexpr unsigned L4_S = 0  , L4_ST = 122880  , L4_DUMP = 163584;
DI constexpr unsigned l5_slot(int c) { return (c & 3) == 0 ? 123904u : ((c & 3) == 1 ? 40960u : ((c & 3) == 2 ? 71680u : 0u)); }
constexpr unsigned L5_RED = 102400;
static_assert(123904 + 30720 <= L4_DUMP && L4_DUMP + 256 <= 163840, "lds");
DI void dma_wd_chunk(const char* wd, unsigned lds0, int w, unsigned l16, int c) {
#pragma unroll
  for (int i = 0; i < 4; ++i) { int n = w + 8 * i; if (n >= 30) n -= 8; const int k2 = n >= 15, j = n - 15 * k2;
    glds_sc1(l16, wd + (size_t)(864 * k2 + 96 * c) * 160 + j * 1024, rfl(lds0 + l5_slot(c) + n * 1024)); }
}
constexpr int GWIN = 5;
DI void phase_gateup(const Params& p, lptr lds, rsrc_t ws, int b, unsigned epoch) {
  const int tid = threadIdx.x, lane = tid & 63, w = rfl(tid >> 6);
  const int xcd = b & 7, rk = b >> 3, mb = 8 * (xcd >> 2) + (rk >> 2), ngl = rk & 3, u0 = 27 * (xcd & 3) + 7 * ngl, nu = ngl == 3 ? 6 : 7;
  const int t16 = lane & 15, g = lane >> 4;
  const unsigned lds0 = (unsigned)(uintptr_t)lds, l16 = lane * 16;
  const char* wd = p.ws + W_WD + (size_t)(rk & 7) * FF * 160;
  const bool act = w < nu;
  const int u = u0 + (act ? w : 0);
  const unsigned wbase = W_WGU + u * 40960 + l16;
  u32x4 ag[GWIN], au[GWIN];
  if (act) {
#pragma unroll
    for (int i = 0; i < GWIN; ++i) { ag[i] = bld128<AUX_SC1>(ws, wbase + i * 2048); au[i] = bld128<AUX_SC1>(ws, wbase + i * 2048 + 1024); }
  } else if (w == 7) {
#pragma unroll
    for (int i = 0; i < 9; ++i) { const int ln = i * 64 + lane; touch_sc1((ln < 540 ? ln : 539) * 128, wd + (rk >> 3) * 69120, rfl(lds0 + L4_DUMP)); }
  }
  dma_wd_chunk(wd, lds0, w, l16, 0);
  SBAR();
  wait_cnt2(p.ws, W_CNTH + (2 * mb) * 256, W_CNTH + (2 * mb + 1) * 256, 8u, epoch);
  STAMPX(16);
  u32x2 sv[8];
  if (tid < 128) {
#pragma unroll
    for (int c = 0; c < 8; ++c) sv[c] = bld64<AUX_SC1>(ws, W_HST + (((2 * mb + (tid >> 6)) * 8 + c) * 64 + (tid & 63)) * 8);
  }
  u32x4 st[4][5];
#pragma unroll
  for (int j = 0; j < 4; ++j)
#pragma unroll
    for (int i = 0; i < 5; ++i) st[j][i] = bld128<AUX_SC1>(ws, W_HF + ((mb * 8 + w) * 20 + 5 * j + i) * 1024 + l16);
  SBAR();
  if (tid < 128) {
    float s1 = 0.f, s2 = 0.f;
#pragma unroll
    for (int c = 0; c < 8; ++c) { s1 += __uint_as_float(sv[c][0]); s2 += __uint_as_float(sv[c][1]); }
    const float mean = s1 * (1.f / DM), rstd = rsqrtf(fmaxf(s2 * (1.f / DM) - mean * mean, 0.f) + LN_EPS);
    *(LAS f32x2*)(lds + L4_ST + tid * 8) = f32x2{mean, rstd};
  }
  lptr hw = lds + L4_S + w * 1024 + l16;
#pragma unroll
  for (int i = 0; i < 5; ++i) *(LAS u32x4*)(hw + i * 8192) = st[0][i];
  LBAR();
  STAMPX(17);
#pragma unroll
  for (int j = 1; j < 3; ++j)
#pragma unroll
    for (int i = 0; i < 5; ++i) *(LAS u32x4*)(hw + j * 40960 + i * 8192) = st[j][i];
  if (act) {
    f32x4 aG[8] = {}, aU[8] = {}; float csg[4] = {}, csu[4] = {};
    lptr hb = lds + L4_S + l16;
    u32x4 bb[2][4];
#pragma unroll
    for (int t = 0; t < 4; ++t) bb[0][t] = lds128(hb + t * 1024);
#pragma unroll
    for (int s = 0; s < 20; ++s) {
      const int slot = (s / 5) % 3, nslot = ((s + 1) / 5) % 3;
      if (s == 4) { asm volatile("" ::: "memory"); WAITL(); asm volatile("" ::: "memory"); BAR(); }
      if (s == 5) { BAR();
#pragma unroll
        for (int i = 0; i < 5; ++i) *(LAS u32x4*)(hw + i * 8192) = st[3][i]; }
      if (s == 14) BAR();
      if (s == 15) { BAR(); dma_wd_chunk(wd, lds0, w, l16, 1); dma_wd_chunk(wd, lds0, w, l16, 2); }
      const u32x4 cg_ = ag[s % GWIN], cu_ = au[s % GWIN];
      if (s + GWIN < 20) { ag[s % GWIN] = bld128<AUX_SC1>(ws, wbase + (s + GWIN) * 2048); au[s % GWIN] = bld128<AUX_SC1>(ws, wbase + (s + GWIN) * 2048 + 1024); }
#pragma unroll
      for (int t = 0; t < 4; ++t) bb[1][t] = lds128(hb + slot * 40960 + (s % 5) * 8192 + (4 + t) * 1024);
      SBAR();
#pragma unroll
      for (int t = 0; t < 4; ++t) { aG[t] = MFMAS(asbf8(cg_), asbf8(bb[0][t]), aG[t]); aU[t] = MFMAS(asbf8(cu_), asbf8(bb[0][t]), aU[t]); csg[t] = dot2(cg_[t], ONES2, csg[t]); }
      SBAR();
      if (s + 1 < 20) {
#pragma unroll
        for (int t = 0; t < 4; ++t) bb[0][t] = lds128(hb + nslot * 40960 + ((s + 1) % 5) * 8192 + t * 1024);
      }
      SBAR();
#pragma unroll
      for (int t = 0; t < 4; ++t) { aG[4 + t] = MFMAS(asbf8(cg_), asbf8(bb[1][t]), aG[4 + t]); aU[4 + t] = MFMAS(asbf8(cu_), asbf8(bb[1][t]), aU[4 + t]); csu[t] = dot2(cu_[t], ONES2, csu[t]); }
      SBAR();
    }
    STAMPX(18);
    const float sg = swap32_sum(swap16_sum((csg[0] + csg[1]) + (csg[2] + csg[3]))), su = swap32_sum(swap16_sum((csu[0] + csu[1]) + (csu[2] + csu[3])));
    float cg4[4], cu4[4];
#pragma unroll
    for (int j = 0; j < 4; ++j) { cg4[j] = __shfl(sg, 4 * g + j); cu4[j] = __shfl(su, 4 * g + j); }
#pragma unroll
    for (int i = 0; i < 4; ++i) {
      u32x2 pk[2];
#pragma unroll
      for (int e = 0; e < 2; ++e) {
        const int t = 2 * i + e;
        const f32x2 mr = *(LAS f32x2*)(lds + L4_ST + (t * 16 + t16) * 8);
        const float r2 = mr[1] * mr[1];
        float hv[4];
#pragma unroll
        for (int j = 0; j < 4; ++j) hv[j] = (aG[t][j] - mr[0] * cg4[j]) * (aU[t][j] - mr[0] * cu4[j]) * r2;
        pk[e] = u32x2{pk2(hv[0], hv[1]), pk2(hv[2], hv[3])};
      }
      const auto s0 = __builtin_amdgcn_permlane16_swap(pk[0][0], pk[1][0], false, false), s1 = __builtin_amdgcn_permlane16_swap(pk[0][1], pk[1][1], false, false);
      bst128<AUX_SC1>(ws, W_GF + (((mb * 8 + 2 * i + (g & 1)) * 54 + (u >> 1)) * 64 + (2 * (u & 1) + (g >> 1)) * 16 + t16) * 16, u32x4{s0[0], s1[0], s0[1], s1[1]});
    }
  } else {
    asm volatile("" ::: "memory"); WAITL(); asm volatile("" ::: "memory"); BAR();
    BAR();
#pragma unroll
    for (int i = 0; i < 5; ++i) *(LAS u32x4*)(hw + i * 8192) = st[3][i];
    asm volatile("" ::: "memory"); WAITL(); asm volatile("" ::: "memory");
    BAR();
    BAR(); dma_wd_chunk(wd, lds0, w, l16, 1); dma_wd_chunk(wd, lds0, w, l16, 2);
  }
  STAMPX(21);
  signal_cnt(p.ws, W_CNTG + mb * 256);
}

DI void gld128_sc1(u32x4& d, unsigned voff, const char* sbase) { asm volatile("global_load_dwordx4 %0, %1, %2 sc1" : "=v"(d) : "v"(voff), "s"(sbase) : "memory"); }
DI void phase_down(const Params& p, lptr lds, rsrc_t ws, int b, const u32x2 (&hres)[5], unsigned epoch) {
  const int tid = threadIdx.x, lane = tid & 63, w = rfl(tid >> 6);
  const int xcd = b & 7, rk = b >> 3, tg = 4 * xcd + (rk >> 3), cg = rk & 7;
  const int mt = w & 3, kh = w >> 2, t16 = lane & 15, g = lane >> 4, tq = t16 >> 2, tp = t16 & 3;
  const unsigned lds0 = (unsigned)(uintptr_t)lds, l16 = lane * 16;
  const char* wd = p.ws + W_WD + (size_t)cg * FF * 160;
  wait_cnt2(p.ws, W_CNTG + (tg >> 1) * 256, W_CNTG + (tg >> 1) * 256, 16u, epoch);
  STAMPX(20);
  const char* abase = p.ws + W_GF + (size_t)((tg * 4 + mt) * 54 + 27 * kh) * 1024;
  u32x4 a[4][3];
#pragma unroll
  for (int c = 0; c < 3; ++c)
#pragma unroll
    for (int k = 0; k < 3; ++k) gld128_sc1(a[c][k], l16, abase + (3 * c + k) * 1024);
  f32x4 acc[5] = {};
  const unsigned brow = kh * 15360 + (16 * (g >> 1) + 4 * (g & 1) + tq) * 160 + tp * 8;
#define DOWN_WAIT(N, c) asm volatile("s_waitcnt vmcnt(" #N ")\n\ts_barrier" : "+v"(a[(c) & 3][0]), "+v"(a[(c) & 3][1]), "+v"(a[(c) & 3][2]) :: "memory")
  bf16x8 bf[2][5];
  { lptr bb = lds + l5_slot(0) + brow;
#pragma unroll
    for (int nt = 0; nt < 5; ++nt) bf[0][nt] = cat8(trread(bb + nt * 32), trread(bb + nt * 32 + 1280)); }
#pragma unroll
  for (int c = 0; c < 9; ++c) {
    asm volatile("" ::: "memory"); WAITL(); asm volatile("" ::: "memory");
    if (c == 0) DOWN_WAIT(6, c); else if (c == 1) DOWN_WAIT(10, c); else if (c <= 5) DOWN_WAIT(10, c); else if (c == 6) DOWN_WAIT(10, c); else if (c == 7) DOWN_WAIT(3, c); else DOWN_WAIT(0, c);
    if (c + 3 < 9) { dma_wd_chunk(wd, lds0, w, l16, c + 3);
#pragma unroll
      for (int k = 0; k < 3; ++k) gld128_sc1(a[(c + 3) & 3][k], l16, abase + (3 * (c + 3) + k) * 1024); }
#pragma unroll
    for (int k = 0; k < 3; ++k) {
      const int sn = 3 * c + k + 1;
      if (sn < 27) { lptr bb = lds + l5_slot(sn / 3) + brow + (sn % 3) * 5120;
#pragma unroll
        for (int nt = 0; nt < 5; ++nt) bf[sn & 1][nt] = cat8(trread(bb + nt * 32), trread(bb + nt * 32 + 1280)); }
      SBAR();
#pragma unroll
      for (int nt = 0; nt < 5; ++nt) acc[nt] = MFMAS(bf[(3 * c + k) & 1][nt], asbf8(a[c & 3][k]), acc[nt]);
      SBAR();
    }
  }
  STAMPX(22);
  LAS f32x4* red = (LAS f32x4*)(lds + L5_RED) + mt * 320 + lane;
  if (kh == 1) {
#pragma unroll
    for (int nt = 0; nt < 5; ++nt) red[nt * 64] = acc[nt];
  }
  LBAR();
  if (kh == 0) {
    const int tok = tg * 64 + mt * 16 + t16;
#pragma unroll
    for (int nt = 0; nt < 5; ++nt) {
      const f32x4 o = red[nt * 64];
      const u32x2 r = {pk2(bflo(hres[nt][0]) + acc[nt][0] + o[0], bfhi(hres[nt][0]) + acc[nt][1] + o[1]), pk2(bflo(hres[nt][1]) + acc[nt][2] + o[2], bfhi(hres[nt][1]) + acc[nt][3] + o[3])};
      *(GAS u32x2*)(p.out + (size_t)tok * 1280 + cg * 160 + nt * 32 + g * 8) = r;
    }
  }
}

__global__ void __launch_bounds__(NTHR, 2) k_block(Params p) {
  extern __shared__ __attribute__((aligned(16))) char smem[];
  lptr lds = (lptr)smem;
  const rsrc_t ws = __builtin_amdgcn_make_buffer_rsrc((void*)p.ws, 0, (int)W_END, 0x00020000);
  const int b = blockIdx.x;
  u32x4 q1; u32x2 q2; unsigned epoch;
  u32x2 hres[5] = {};
  STAMPX(0);
  phase1(p, lds, ws, b, q1, q2, epoch);
  STAMPX(5);
  phase_attn(p, lds, ws, b, q1, q2, epoch);
  STAMPX(12);
  phase_oproj(p, lds, ws, b, hres, epoch);
  STAMPX(15);
  phase_gateup(p, lds, ws, b, epoch);
  STAMPX(19);
  phase_down(p, lds, ws, b, hres, epoch);
  STAMPX(23);
}

extern "C" void kernel_launch(void* const* d_in, const int* in_sizes, int n_in, void* d_out, int out_size, void* d_ws, size_t ws_size, hipStream_t stream) {
  Params p;
  p.x = (const char*)d_in[0]; p.wq = (const char*)d_in[1]; p.wk = (const char*)d_in[2]; p.wv = (const char*)d_in[3]; p.wo = (const char*)d_in[4];
  p.wg = (const char*)d_in[6]; p.wu = (const char*)d_in[7]; p.wd = (const char*)d_in[8];
  p.out = (char*)d_out; p.ws = (char*)d_ws;
  constexpr int LDS_BYTES = 163840;
  (void)hipFuncSetAttribute((const void*)k_block, hipFuncAttributeMaxDynamicSharedMemorySize, LDS_BYTES);
  k_block<<<NWG, NTHR, LDS_BYTES, stream>>>(p);
}
```

```cpp
#include <hip/hip_runtime.h>
#include <stdint.h>
#define CODEPF 55296

#define DI __device__ __forceinline__
#define LAS __attribute__((address_space(3)))
#define GAS __attribute__((address_space(1)))
typedef short bf16x8 __attribute__((ext_vector_type(8)));
typedef short bf16x4 __attribute__((ext_vector_type(4)));
typedef __bf16 bfv2 __attribute__((ext_vector_type(2)));
typedef float f32x2 __attribute__((ext_vector_type(2)));
typedef float f32x4 __attribute__((ext_vector_type(4)));
typedef float f32x16 __attribute__((ext_vector_type(16)));
typedef unsigned u32x2 __attribute__((ext_vector_type(2)));
typedef unsigned u32x4 __attribute__((ext_vector_type(4)));
typedef LAS char* lptr;

constexpr int SEQ = 2048, DM = 640, FF = 1728;
constexpr int NWG = 256, NTHR = 512;
constexpr float LN_EPS = 1e-5f;
constexpr float QSCALE = 0.22360679774997896f * 1.4426950408889634f;
constexpr unsigned KV_TILE = 2688, KV_K2 = 1024, KV_V = 1280, KV_VS = 352;

constexpr unsigned W_EPOCH = 0, W_TMO = 256;
constexpr unsigned W_FKV = 1024  , W_FRT = 2048  , W_KMAX = 4096  ;
constexpr unsigned W_CNTO = 8192, W_CNTH = 16384, W_CNTG = 24576;
constexpr unsigned W_KV = 65536;
constexpr unsigned W_OF = W_KV + 64 * KV_TILE;
constexpr unsigned W_HF = W_OF + SEQ * DM * 2;
constexpr unsigned W_HST = W_HF + SEQ * DM * 2;
constexpr unsigned W_GF = W_HST + 32 * 8 * 64 * 8;
constexpr unsigned W_WGU = W_GF + 128 * 54 * 1024;
constexpr unsigned W_WD = W_WGU + 108 * 40 * 1024;
constexpr unsigned W_END = W_WD + 8 * FF * 160;

#define STAMPW() do { } while (0)
#define STAMP(i) do { } while (0)
#define STW 0
#define STAMPX(i) do { STAMP(i); if (STW == (i)) STAMPW(); } while (0)

struct Params {
  const char* x; const char* wq; const char* wk; const char* wv; const char* wo;
  const char* wg; const char* wu; const char* wd;
  char* out; char* ws;
};

constexpr int AUX_SC1 = 16;
#define MFMA16(a, b, c) __builtin_amdgcn_mfma_f32_32x32x16_bf16((a), (b), (c), 0, 0, 0)
#define MFMA8(a, b, c) __builtin_amdgcn_mfma_f32_32x32x8bf16_1k((a), (b), (c), 0, 0, 0)
#define MFMAS(a, b, c) __builtin_amdgcn_mfma_f32_16x16x32_bf16((a), (b), (c), 0, 0, 0)
#define SBAR() __builtin_amdgcn_sched_barrier(0)
#define WAITV(n) asm volatile("s_waitcnt vmcnt(" #n ")" ::: "memory")
#define BAR() asm volatile("s_barrier" ::: "memory")
#define WAITL() __builtin_amdgcn_s_waitcnt(0xC07F)
#define LBAR() do { asm volatile("" ::: "memory"); WAITL(); BAR(); } while (0)
#define PIN(x) asm volatile("" : "+v"(x))

DI unsigned pk2(float lo, float hi) { f32x2 v = {lo, hi}; return __builtin_bit_cast(unsigned, __builtin_convertvector(v, bfv2)); }
DI float bflo(unsigned v) { return __uint_as_float(v << 16); }
DI float bfhi(unsigned v) { return __uint_as_float(v & 0xffff0000u); }
DI float dot2(unsigned a, unsigned b, float c) { return __builtin_amdgcn_fdot2_f32_bf16(__builtin_bit_cast(bfv2, a), __builtin_bit_cast(bfv2, b), c, false); }
constexpr unsigned ONES2 = 0x3f803f80u;
DI float swap32_sum(float v) { auto r = __builtin_amdgcn_permlane32_swap(__float_as_uint(v), __float_as_uint(v), false, false); return __uint_as_float(r[0]) + __uint_as_float(r[1]); }
DI float swap32_max(float v) { auto r = __builtin_amdgcn_permlane32_swap(__float_as_uint(v), __float_as_uint(v), false, false); return fmaxf(__uint_as_float(r[0]), __uint_as_float(r[1])); }
DI float swap16_sum(float v) { auto r = __builtin_amdgcn_permlane16_swap(__float_as_uint(v), __float_as_uint(v), false, false); return __uint_as_float(r[0]) + __uint_as_float(r[1]); }
template <int CTRL> DI float dppf(float v) { return __uint_as_float(__builtin_amdgcn_update_dpp(0u, __float_as_uint(v), CTRL, 0xf, 0xf, true)); }
DI float sum8(float v) { v += dppf<0xB1>(v); v += dppf<0x4E>(v); return v + dppf<0x141>(v); }
DI float max16(float v) { v = fmaxf(v, dppf<0x128>(v)); v = fmaxf(v, dppf<0x124>(v)); v = fmaxf(v, dppf<0x122>(v)); return fmaxf(v, dppf<0x121>(v)); }
DI float max64(float v) { v = max16(v); auto r = __builtin_amdgcn_permlane16_swap(__float_as_uint(v), __float_as_uint(v), false, false);
  return swap32_max(fmaxf(__uint_as_float(r[0]), __uint_as_float(r[1]))); }
typedef short v4i16_t __attribute__((ext_vector_type(4)));
DI bf16x4 trread(lptr p) { return __builtin_bit_cast(bf16x4, __builtin_amdgcn_ds_read_tr16_b64_v4i16((LAS v4i16_t*)p)); }
DI bf16x8 cat8(bf16x4 a, bf16x4 b) { return __builtin_shufflevector(a, b, 0, 1, 2, 3, 4, 5, 6, 7); }
DI u32x4 lds128(lptr p) { return *(LAS u32x4*)p; }
DI u32x2 lds64(lptr p) { return *(LAS u32x2*)p; }
DI bf16x8 asbf8(u32x4 v) { return __builtin_bit_cast(bf16x8, v); }
DI bf16x4 asbf4(u32x2 v) { return __builtin_bit_cast(bf16x4, v); }
DI u32x4 asu4(bf16x8 v) { return __builtin_bit_cast(u32x4, v); }
DI unsigned rfl(unsigned v) { return __builtin_amdgcn_readfirstlane(v); }
typedef __amdgpu_buffer_rsrc_t rsrc_t;
template <int AUX> DI u32x4 bld128(rsrc_t r, unsigned off) { return __builtin_amdgcn_raw_buffer_load_b128(r, off, 0, AUX); }
template <int AUX> DI u32x2 bld64(rsrc_t r, unsigned off) { return __builtin_amdgcn_raw_buffer_load_b64(r, off, 0, AUX); }
template <int AUX> DI unsigned bld32(rsrc_t r, unsigned off) { return __builtin_amdgcn_raw_buffer_load_b32(r, off, 0, AUX); }
template <int AUX> DI void bst128(rsrc_t r, unsigned off, u32x4 v) { __builtin_amdgcn_raw_buffer_store_b128(v, r, off, 0, AUX); }
template <int AUX> DI void bst64(rsrc_t r, unsigned off, u32x2 v) { __builtin_amdgcn_raw_buffer_store_b64(v, r, off, 0, AUX); }
template <int AUX> DI void bst32(rsrc_t r, unsigned off, unsigned v) { __builtin_amdgcn_raw_buffer_store_b32(v, r, off, 0, AUX); }
DI void glds(unsigned voff, const char* sbase, unsigned lds_base) {
  unsigned sv; lds_base = rfl(lds_base);
  asm volatile("s_mov_b32 %0, m0\n\ts_mov_b32 m0, %3\n\ts_nop 0\n\tglobal_load_lds_dwordx4 %1, %2\n\ts_mov_b32 m0, %0" : "=&s"(sv) : "v"(voff), "s"(sbase), "s"(lds_base) : "memory");
}
DI void glds_sc1(unsigned voff, const char* sbase, unsigned lds_base) {
  unsigned sv; lds_base = rfl(lds_base);
  asm volatile("s_mov_b32 %0, m0\n\ts_mov_b32 m0, %3\n\ts_nop 0\n\tglobal_load_lds_dwordx4 %1, %2 sc1\n\ts_mov_b32 m0, %0" : "=&s"(sv) : "v"(voff), "s"(sbase), "s"(lds_base) : "memory");
}
DI void touch(unsigned voff, const char* sbase, unsigned lds_dump) {
  unsigned sv; lds_dump = rfl(lds_dump);
  asm volatile("s_mov_b32 %0, m0\n\ts_mov_b32 m0, %3\n\ts_nop 0\n\tglobal_load_lds_dword %1, %2\n\ts_mov_b32 m0, %0" : "=&s"(sv) : "v"(voff), "s"(sbase), "s"(lds_dump) : "memory");
}
DI void touch_sc1(unsigned voff, const char* sbase, unsigned lds_dump) {
  unsigned sv; lds_dump = rfl(lds_dump);
  asm volatile("s_mov_b32 %0, m0\n\ts_mov_b32 m0, %3\n\ts_nop 0\n\tglobal_load_lds_dword %1, %2 sc1\n\ts_mov_b32 m0, %0" : "=&s"(sv) : "v"(voff), "s"(sbase), "s"(lds_dump) : "memory");
}

#define RLX_AGENT __ATOMIC_RELAXED, __HIP_MEMORY_SCOPE_AGENT
constexpr unsigned SPIN_CAP = 1u << 17;
DI bool spin_fail(unsigned& spins, char* wsp, unsigned epoch) {
  __builtin_amdgcn_s_sleep(1);
  GAS unsigned* tmo = (GAS unsigned*)(wsp + W_TMO);
  if (++spins > SPIN_CAP) { __hip_atomic_store(tmo, epoch, RLX_AGENT); return true; }
  return (spins & 255u) == 0u && rfl(__hip_atomic_load(tmo, RLX_AGENT)) == epoch;
}
template <int NW> DI void wait_flags(char* wsp, unsigned off, unsigned epoch, int lane) {
  for (unsigned spins = 0;;) {
    bool ok = true;
#pragma unroll
    for (int i = 0; i < NW; ++i) ok &= __hip_atomic_load((GAS unsigned*)(wsp + off) + i * 64 + lane, RLX_AGENT) == epoch;
    if (__all(ok)) break;
    if (spin_fail(spins, wsp, epoch)) break;
  }
  __builtin_amdgcn_fence(__ATOMIC_ACQUIRE, "wavefront");
}
DI void wait_cnt2(char* wsp, unsigned off0, unsigned off1, unsigned want, unsigned epoch) {
  if (threadIdx.x < 64) {
    GAS unsigned* word = (GAS unsigned*)(wsp + ((threadIdx.x & 1) ? off1 : off0));
    for (unsigned spins = 0;;) {
      if (__all(__hip_atomic_load(word, RLX_AGENT) >= want)) break;
      if (spin_fail(spins, wsp, epoch)) break;
    }
    __builtin_amdgcn_fence(__ATOMIC_ACQUIRE, "wavefront");
  }
  LBAR();
}
DI void signal_cnt(char* wsp, unsigned off) {
  WAITV(0);
  LBAR();
  if (threadIdx.x == 0) (void)__hip_atomic_fetch_add((GAS unsigned*)(wsp + off), 1u, RLX_AGENT);
}
DI void lds_add1(lptr p) { (void)__hip_atomic_fetch_add((LAS unsigned*)p, 1u, __ATOMIC_RELAXED, __HIP_MEMORY_SCOPE_WORKGROUP); }
DI void lds_wait_ge(lptr p, unsigned want) {
  while ((unsigned)rfl(__hip_atomic_load((LAS unsigned*)p, __ATOMIC_RELAXED, __HIP_MEMORY_SCOPE_WORKGROUP)) < want) __builtin_amdgcn_s_sleep(2);
  asm volatile("" ::: "memory");
}

constexpr unsigned L1_X = 0, L1_S = 81920, L1_WK = L1_S + 4 * 10240, L1_PART = L1_WK + 25600;
constexpr unsigned L1_CS = L1_PART + 12672  , L1_ST = L1_CS + 1024  , L1_KC = L1_ST + 512  , L1_DUMP = L1_KC + 128;
static_assert(L1_DUMP + 256 <= 163840, "lds");

DI void phase1(const Params& p, lptr lds, rsrc_t ws, int b, u32x4& q1, u32x2& q2, unsigned& epoch) {
  const int tid = threadIdx.x, lane = tid & 63, w = rfl(tid >> 6);
  const int r32 = lane & 31, h = lane >> 5;
  const int xcd = b & 7, rk = b >> 3, tg = 4 * xcd + (rk >> 3), hg = rk & 7;
  const unsigned lds0 = (unsigned)(uintptr_t)lds, l16 = lane * 16;
  unsigned ep = __hip_atomic_load((GAS unsigned*)(p.ws + W_EPOCH), RLX_AGENT);
  {
    const char* xb = p.x + (size_t)(tg * 64 + 8 * w) * 1280;
    const int tl = lane >> 4;
    unsigned v0 = tl * 1280 + ((lane ^ (8 * w + tl)) & 15) * 16, v1 = (4 + tl) * 1280 + ((lane ^ (8 * w + 4 + tl)) & 15) * 16, xd = lds0 + L1_X + 8 * w * 256;
#pragma unroll 1
    for (int a = 0; a < 5; ++a) { glds(v0, xb, xd); glds(v1, xb, xd + 1024); v0 += 256; v1 += 256; xd += 16384; }
  }
  if (hg < 4) {
    const char* W = (hg < 2 ? p.wk : p.wv) + w * 1024;
#pragma unroll
    for (int i = 0; i < 3; ++i) glds(l16 + i * 8192, W, lds0 + L1_WK + w * 1024 + i * 8192);
    if (w == 0) glds(l16 + 3 * 8192, W, lds0 + L1_WK + 3 * 8192);
  }
  BAR();
  const char* wqb = p.wq + hg * 160;
  unsigned vq[3], dq[3];
#pragma unroll
  for (int i = 0; i < 3; ++i) {
    const int n = i < 2 ? w + 8 * i : (w < 6 ? w + 16 : w + 8), cc = n >= 11, pc = n - 11 * cc;
    const int slot = 6 * pc + lane / 10, row = (slot & ~7) | ((slot & 1) << 2) | ((slot >> 1) & 3);
    vq[i] = (64 * cc + row) * 1280 + (lane % 10) * 16; dq[i] = cc * 10240 + pc * 960;
  }
  const bool m1 = w != 2 || lane < 40, m2 = w < 6 ? (w != 5 || lane < 40) : lane == 0;
  auto dma_pair = [&](const char* src, unsigned dst) {
    glds(vq[0], src, dst + dq[0]);
    if (m1) glds(vq[1], src, dst + dq[1]);
    if (m2) glds(vq[2], src, dst + dq[2]);
  };
  dma_pair(wqb, lds0 + L1_S); dma_pair(wqb + 128 * 1280, lds0 + L1_S + 20480);
  SBAR();
  STAMPX(8);
  const int i16 = lane & 15, tq = i16 >> 2, tp = i16 & 3, grp = (lane >> 4) & 1;
  WAITV(6);
  LBAR();
  PIN(ep); epoch = rfl(ep) + 1u;
  STAMPX(1);
  const int kth = hg & 1; const bool isV = hg >= 2;
  f32x16 kacc = {}; float kcs = 0.f;
  if (w >= 4) {
#pragma unroll
    for (int ps = 0; ps < 2; ++ps) {
      const int t = (ps ? 32 - 32 * kth : 32 * kth) + 8 * (w - 4) + (lane >> 3), e = lane & 7;
      u32x4 v[10];
#pragma unroll
      for (int i = 0; i < 10; ++i) v[i] = lds128(lds + L1_X + (i >> 1) * 16384 + t * 256 + ((e + 8 * (i & 1) + 8 * ((t >> 1) & 1)) & 15) * 16);
      float a4[4] = {}, b4[4] = {};
#pragma unroll
      for (int i = 0; i < 10; ++i)
#pragma unroll
        for (int j = 0; j < 4; ++j) { a4[j] = dot2(v[i][j], v[i][j], a4[j]); b4[j] = dot2(v[i][j], ONES2, b4[j]); }
      const float sq = sum8((a4[0] + a4[1]) + (a4[2] + a4[3])), sm = sum8((b4[0] + b4[1]) + (b4[2] + b4[3]));
      const float mean = sm * (1.f / DM), rstd = rsqrtf(fmaxf(sq * (1.f / DM) - mean * mean, 0.f) + LN_EPS);
      if (e == 0) { ((LAS float*)(lds + L1_ST))[t] = mean; ((LAS float*)(lds + L1_ST))[64 + t] = rstd; }
      if (ps == 0) {
        LBAR(); dma_pair(wqb + 256 * 1280, lds0 + L1_WK);
        const int ln = ((rk >> 3) * 4 + (w - 4)) * 32 + r32;
        if (h == 0) touch((ln >> 1) * 1280 + (ln & 1) * 128, wqb + 384 * 1280 - ((hg * 160) & 127), lds0 + L1_DUMP);
      }
    }
  } else if (hg < 4) {
    lptr wb = lds + L1_WK + (160 * w + 8 * h + tq) * 40 + grp * 32 + 8 * tp;
    const int tok = kth * 32 + r32;
    u32x4 xf[10]; bf16x8 wf[10];
#pragma unroll
    for (int i = 0; i < 10; ++i) { const int c = 20 * w + 2 * i + h;
      xf[i] = lds128(lds + L1_X + (c >> 4) * 16384 + tok * 256 + ((c ^ tok) & 15) * 16); wf[i] = cat8(trread(wb + i * 640), trread(wb + i * 640 + 160)); }
    if (isV) {
#pragma unroll
      for (int i = 0; i < 10; ++i) kacc = MFMA16(asbf8(xf[i]), wf[i], kacc);
    } else {
#pragma unroll
      for (int i = 0; i < 10; ++i) kacc = MFMA16(wf[i], asbf8(xf[i]), kacc);
    }
    float c4[4] = {};
#pragma unroll
    for (int i = 0; i < 10; ++i) { const u32x4 wu = asu4(wf[i]);
#pragma unroll
      for (int j = 0; j < 4; ++j) c4[j] = dot2(wu[j], ONES2, c4[j]); }
    kcs = swap32_sum((c4[0] + c4[1]) + (c4[2] + c4[3]));
    if (w > 0) {
      LAS f32x4* pa = (LAS f32x4*)(lds + L1_PART + (w - 1) * 4224) + lane;
#pragma unroll
      for (int g = 0; g < 4; ++g) pa[g * 64] = f32x4{kacc[4 * g], kacc[4 * g + 1], kacc[4 * g + 2], kacc[4 * g + 3]};
      if (h == 0) ((LAS float*)(lds + L1_PART + (w - 1) * 4224 + 4096))[r32] = kcs;
    }
  }
  if (w < 4) {
    LBAR();
    dma_pair(wqb + 256 * 1280, lds0 + L1_WK);
  }
  if (hg < 4 && w == 0) {
#pragma unroll
    for (int v = 0; v < 3; ++v) {
      LAS f32x4* pa = (LAS f32x4*)(lds + L1_PART + v * 4224) + lane;
#pragma unroll
      for (int g = 0; g < 4; ++g) { const f32x4 t = pa[g * 64]; kacc[4 * g] += t[0]; kacc[4 * g + 1] += t[1]; kacc[4 * g + 2] += t[2]; kacc[4 * g + 3] += t[3]; }
      kcs += ((LAS float*)(lds + L1_PART + v * 4224 + 4096))[r32];
    }
    const int tile = tg * 2 + kth;
    const LAS float* st = (const LAS float*)(lds + L1_ST) + kth * 32;
    if (lane == 0 && hg == 0) { bst32<AUX_SC1>(ws, W_CNTO + tg * 256, 0u); bst32<AUX_SC1>(ws, W_CNTH + tg * 256, 0u); }
    if (lane == 0 && hg == 1 && !(tg & 1)) bst32<AUX_SC1>(ws, W_CNTG + (tg >> 1) * 256, 0u);
    if (!isV) {
      LAS float* tab = (LAS float*)(lds + L1_KC);
      if (h == 0) tab[r32] = kcs;
      const float mean = st[r32], rstd = st[64 + r32];
      asm volatile("" ::: "memory"); WAITL(); asm volatile("" ::: "memory");
      float kv[12];
#pragma unroll
      for (int g = 0; g < 3; ++g) { const f32x4 c4 = *(LAS f32x4*)(tab + 8 * g + 4 * h);
#pragma unroll
        for (int j = 0; j < 4; ++j) kv[4 * g + j] = (kacc[4 * g + j] - mean * c4[j]) * rstd; }
      const u32x4 k1 = {pk2(kv[0], kv[1]), pk2(kv[2], kv[3]), pk2(kv[4], kv[5]), pk2(kv[6], kv[7])};
      const u32x2 k2 = {pk2(kv[8], kv[9]), pk2(kv[10], kv[11])};
      bst128<AUX_SC1>(ws, W_KV + tile * KV_TILE + l16, k1);
      if (h == 0) bst64<AUX_SC1>(ws, W_KV + tile * KV_TILE + KV_K2 + r32 * 8, k2);
      float n2 = 0.f;
#pragma unroll
      for (int j = 0; j < 4; ++j) n2 = dot2(k1[j], k1[j], n2);
      if (h == 0) { n2 = dot2(k2[0], k2[0], n2); n2 = dot2(k2[1], k2[1], n2); }
      n2 = max64(swap32_sum(n2));
      if (lane == 0) bst32<AUX_SC1>(ws, W_KMAX + tile * 4, __float_as_uint(n2));
    } else {
      const unsigned cv = r32 == 20 ? ONES2 : 0u;
#pragma unroll
      for (int s = 0; s < 2; ++s) {
        float vv[8];
#pragma unroll
        for (int g = 0; g < 2; ++g) { const f32x4 m4 = *(const LAS f32x4*)(st + 16 * s + 8 * g + 4 * h), r4 = *(const LAS f32x4*)(st + 64 + 16 * s + 8 * g + 4 * h);
#pragma unroll
          for (int j = 0; j < 4; ++j) vv[4 * g + j] = (kacc[8 * s + 4 * g + j] - m4[j] * kcs) * r4[j]; }
        u32x4 o = {pk2(vv[0], vv[1]), pk2(vv[2], vv[3]), pk2(vv[4], vv[5]), pk2(vv[6], vv[7])};
        if (r32 >= 20) o = u32x4{cv, cv, cv, cv};
        if (r32 < 22) bst128<AUX_SC1>(ws, W_KV + tile * KV_TILE + KV_V + (2 * s + h) * KV_VS + r32 * 16, o);
      }
    }
  }
  if (w >= 4) WAITV(4); else if (hg < 4 && w == 0) WAITV(5); else WAITV(3);
  LBAR();
  STAMPX(2);
  {
    const int hh = w & 3, th = w >> 2, tok = th * 32 + r32;
    f32x16 acc = {}; float c4[4] = {};
    const unsigned wrow = (8 * h + 2 * tq) * 160 + hh * 40 + grp * 32 + 8 * tp;
    unsigned xo[8];
#pragma unroll
    for (int i = 0; i < 8; ++i) xo[i] = L1_X + tok * 256 + (((2 * i + h) ^ tok) & 15) * 16;
    u32x4 xf[2][4]; bf16x8 wf[2][4];
    unsigned xa = 0, wa = L1_S + wrow;
#define Q_RD1(e, ks, XA, WA) do { xf[e][ks] = lds128(lds + xo[4 * (e) + (ks)] + (XA)); lptr a_ = lds + (WA) + (e) * 10240 + (ks) * 2560; wf[e][ks] = cat8(trread(a_), trread(a_ + 160)); } while (0)
#define Q_STEP(e, XA, WA) do { _Pragma("unroll") for (int ks = 0; ks < 4; ++ks) { acc = MFMA16(wf[e][ks], asbf8(xf[e][ks]), acc); Q_RD1(1 - (e), ks, XA, WA); SBAR(); } } while (0)
#define Q_CS(e) do { _Pragma("unroll") for (int ks = 0; ks < 4; ++ks) { const u32x4 wu = asu4(wf[e][ks]); _Pragma("unroll") for (int j = 0; j < 4; ++j) c4[j] = dot2(wu[j], ONES2, c4[j]); } } while (0)
#pragma unroll
    for (int ks = 0; ks < 4; ++ks) Q_RD1(0, ks, xa, wa);
#pragma unroll 1
    for (int pr = 0; pr < 5; ++pr) {
      Q_STEP(0, xa, wa);
      if (pr == 1 || pr == 2) WAITV(3); else if (pr == 3) WAITV(0);
      if (pr == 1 && w == 0 && hg < 4 && lane == 0)
        __hip_atomic_store((GAS unsigned*)(p.ws + W_FKV) + tg * 4 + hg, epoch, RLX_AGENT);
      LBAR();
      const unsigned wn = pr == 1 ? L1_WK + wrow : (pr == 2 || pr == 4 ? L1_S + wrow : wa + 20480);
      if (pr < 2) dma_pair(wqb + (size_t)(pr + 3) * 128 * 1280, lds0 + L1_S + pr * 20480);
      if (th == 0) Q_CS(0); else Q_CS(1);
      SBAR();
      xa = pr < 4 ? xa + 16384 : 0;
      Q_STEP(1, xa, wn);
      wa = wn;
    }
#undef Q_RD1
#undef Q_STEP
#undef Q_CS
    const float cs = swap32_sum((c4[0] + c4[1]) + (c4[2] + c4[3]));
    if (h == 0) ((LAS float*)(lds + L1_CS))[w * 32 + r32] = cs;
    const float mean = ((LAS float*)(lds + L1_ST))[tok], rstd = ((LAS float*)(lds + L1_ST))[64 + tok] * QSCALE;
    LBAR();
    float qv[12];
#pragma unroll
    for (int g = 0; g < 3; ++g) {
      const f32x4 ca = *(LAS f32x4*)(lds + L1_CS + (hh * 32 + 8 * g + 4 * h) * 4), cb = *(LAS f32x4*)(lds + L1_CS + ((hh + 4) * 32 + 8 * g + 4 * h) * 4);
#pragma unroll
      for (int j = 0; j < 4; ++j) qv[4 * g + j] = (acc[4 * g + j] - mean * (ca[j] + cb[j])) * rstd;
    }
    q1 = u32x4{pk2(qv[0], qv[1]), pk2(qv[2], qv[3]), pk2(qv[4], qv[5]), pk2(qv[6], qv[7])};
    q2 = h ? u32x2{0u, 0u} : u32x2{pk2(qv[8], qv[9]), pk2(qv[10], qv[11])};
  }
  STAMPX(3);
}

constexpr unsigned L2_FLAG = 60 * KV_TILE, L2_CR = L2_FLAG + 16, L2_CP = L2_FLAG + 32, L2_CS = L2_FLAG + 48, L2_DUMP = L2_FLAG + 256;
constexpr unsigned L2_RT = 52 * KV_TILE;
constexpr unsigned L3_WO = 0, L3_RED = 102400;
#define PRIO_M 6

DI void phase_attn(const Params& p, lptr lds, rsrc_t ws, int b, const u32x4 q1, const u32x2 q2, unsigned epoch) {
  const int tid = threadIdx.x, lane = tid & 63, w = rfl(tid >> 6);
  const int r32 = lane & 31, h = lane >> 5;
  const int xcd = b & 7, rk = b >> 3, tg = 4 * xcd + (rk >> 3), hg = rk & 7;
  const int hh = w & 3, th = w >> 2;
  const unsigned lds0 = (unsigned)(uintptr_t)lds, l16 = lane * 16;
  const char* kvg = p.ws + W_KV;
  auto dma3 = [&](int c) {
    const unsigned dst = lds0 + (c == 15 ? 0 : c) * 4 * KV_TILE; const char* src = kvg + c * 4 * KV_TILE;
    glds_sc1(l16, src + hh * 1024, rfl(dst + hh * 1024));
    glds_sc1(l16, src + (hh + 4) * 1024, rfl(dst + (hh + 4) * 1024));
    const int n = hh == 3 ? 7 : hh + 8;
    if (hh == 2) { if (lane < 32) glds_sc1(l16, src + n * 1024, rfl(dst + n * 1024)); } else glds_sc1(l16, src + n * 1024, rfl(dst + n * 1024));
  };
  auto stage_first = [&]() { if (th == 0) { dma3(0); dma3(2); } else dma3(1); };
  u32x4 rg[2][4], rd[3];
  const int ga = b * 4 + hh, dj0 = (ga - 304) * 3;
  if (th == 0) {
#pragma unroll
    for (int e = 0; e < 2; ++e) { const int gj = ga + 1024 * e;
      if (gj < 1080) { const int cgf = gj / 40, r = gj % 40; const char* W = (r & 1) ? p.wu : p.wg;
#pragma unroll
        for (int i = 0; i < 4; ++i) rg[e][i] = *(const GAS u32x4*)(W + (size_t)(32 * (r >> 1) + 8 * i + (lane >> 3)) * (FF * 2) + cgf * 128 + (lane & 7) * 16); } }
    if (ga >= 304) {
#pragma unroll
      for (int i = 0; i < 3; ++i) rd[i] = *(const GAS u32x4*)(p.wd + (size_t)(dj0 + i) * 1024 + l16);
    }
  }
  auto retile = [&]() {
    const int i16 = lane & 15, tq = i16 >> 2, tp = i16 & 3, g4 = lane >> 4;
    lptr sc = lds + L2_RT + hh * 4096;
#pragma unroll
    for (int e = 0; e < 2; ++e) { const int gj = ga + 1024 * e;
      if (gj < 1080) { const int cgf = gj / 40, r = gj % 40;
#pragma unroll
        for (int i = 0; i < 4; ++i) *(LAS u32x4*)(sc + i * 1024 + l16) = rg[e][i];
#pragma unroll
        for (int u = 0; u < 4; ++u) { lptr a = sc + (8 * g4 + tq) * 128 + u * 32 + tp * 8;
          bst128<AUX_SC1>(ws, W_WGU + (((cgf * 4 + u) * 20 + (r >> 1)) * 2 + (r & 1)) * 1024 + l16, asu4(cat8(trread(a), trread(a + 512)))); } } }
    if (ga >= 304) {
#pragma unroll
      for (int i = 0; i < 3; ++i) { const int a5 = (dj0 + i) * 4 + (lane >> 4), f = (a5 * 52429) >> 18, c = (a5 - 5 * f) * 16 + (lane & 15), cg = (c * 6554) >> 16, cc = c - cg * 10;
        bst128<AUX_SC1>(ws, W_WD + (cg * FF + ((f & ~12) | ((f & 4) << 1) | ((f & 8) >> 1))) * 160 + cc * 16, rd[i]); } }
  };
  if (w == 0) wait_flags<2>(p.ws, W_FKV, epoch, lane);
  if (tid < 7) *(LAS unsigned*)(lds + L2_FLAG + tid * 16) = 0u;
  LBAR();
  STAMPX(4);
  float kmx = __uint_as_float(bld32<AUX_SC1>(ws, W_KMAX + lane * 4));
  stage_first();
  float ref;
  {
    float n2 = 0.f;
#pragma unroll
    for (int j = 0; j < 4; ++j) n2 = dot2(q1[j], q1[j], n2);
    n2 = dot2(q2[0], q2[0], n2); n2 = dot2(q2[1], q2[1], n2);
    n2 = swap32_sum(n2);
    kmx = max64(kmx);
    ref = sqrtf(n2 * kmx) * 1.001f;
  }
  const int rv = r32 < 21 ? r32 : 21;
  const unsigned oK1 = l16, oK2 = KV_K2 + r32 * 8, oV = KV_V + h * KV_VS + rv * 16;
  f32x16 o;
  for (int attempt = 0;; ++attempt) {
    o = f32x16{};
    f32x16 cb;
#pragma unroll
    for (int j = 0; j < 16; ++j) cb[j] = -ref;
    PIN(cb);
    WAITV(0);
    LBAR();
    STAMPX(9);
    lptr aK1 = lds + oK1; lptr aK2 = lds + oK2; lptr aV = lds + oV;
    u32x4 k1 = lds128(aK1); u32x2 k2 = lds64(aK2);
    f32x16 sc = MFMA8(asbf4(k2), asbf4(q2), cb); sc = MFMA16(asbf8(k1), asbf8(q1), sc);
    k1 = lds128(aK1 + KV_TILE); k2 = lds64(aK2 + KV_TILE);
    u32x4 v0 = lds128(aV), v1 = {}, p0, p1 = {};
    u32x4 k1b, v0b, v1b; u32x2 k2b;
    f32x16 sn;
#define EXP2(a) __builtin_amdgcn_exp2f(a)
#define GRP(MF, SC, i, P, j) do { MF; SBAR(); { const float e0 = EXP2(SC[i]), e1 = EXP2(SC[i + 1]), e2 = EXP2(SC[i + 2]), e3 = EXP2(SC[i + 3]); P[j] = pk2(e0, e1); P[j + 1] = pk2(e2, e3); } PIN(P); SBAR(); } while (0)
#define TILE(SC, SN, K1, K2, V0, V1, K1N, K2N, V0N, V1N, PK1, PK2, PV0, PV1) do { \
      WAITL(); \
      K2N = lds64(PK2); K1N = lds128(PK1); V0N = lds128(PV0); V1N = lds128(PV1); SBAR(); \
      GRP(SN = MFMA8(asbf4(K2), asbf4(q2), cb), SC, 0, p0, 0); \
      GRP(o = MFMA16(asbf8(V1), asbf8(p1), o), SC, 4, p0, 2); \
      GRP(SN = MFMA16(asbf8(K1), asbf8(q1), SN), SC, 8, p1, 0); \
      GRP(o = MFMA16(asbf8(V0), asbf8(p0), o), SC, 12, p1, 2); } while (0)
#define TA(T) TILE(sc, sn, k1, k2, v0, v1, k1b, k2b, v0b, v1b, aK1 + ((T) + 2) * KV_TILE, aK2 + ((T) + 2) * KV_TILE, aV + ((T) + 1) * KV_TILE, aV + (T) * KV_TILE + 2 * KV_VS)
#define TB(T) TILE(sn, sc, k1b, k2b, v0b, v1b, k1, k2, v0, v1, aK1 + ((T) + 2) * KV_TILE, aK2 + ((T) + 2) * KV_TILE, aV + ((T) + 1) * KV_TILE, aV + (T) * KV_TILE + 2 * KV_VS)
#define ADV() do { aK1 += 8 * KV_TILE; aK2 += 8 * KV_TILE; aV += 8 * KV_TILE; } while (0)
#define BODY8() do { TA(0); TB(1); TA(2); TB(3); TA(4); TB(5); TA(6); TB(7); ADV(); } while (0)
    if (th == 1) {
      __builtin_amdgcn_s_setprio(2);
      dma3(3); SBAR(); TA(0); dma3(4); SBAR(); TB(1); dma3(5); SBAR(); TA(2); dma3(6); SBAR(); TB(3);
      dma3(7); SBAR(); TA(4); dma3(8); SBAR(); TB(5); dma3(9); SBAR(); TA(6); dma3(10); SBAR(); TB(7); ADV();
      if (lane == 0) lds_add1(lds + L2_CP);
      WAITV(6); if (lane == 0) lds_add1(lds + L2_CS);
      lds_wait_ge(lds + L2_CS, 4u);
      dma3(11); SBAR(); TA(0); dma3(12); SBAR(); TB(1); TA(2); TB(3); TA(4); TB(5); TA(6); TB(7); ADV();
      WAITV(0); if (lane == 0) lds_add1(lds + L2_CS + 16);
#pragma nounroll
      for (int m = 2; m < 7; ++m) {
        if (m == 3) { if (attempt == 0) lds_wait_ge(lds + L2_CR, 4u); dma3(13); dma3(14); }
        if (m == 4) { WAITV(0); if (lane == 0) lds_add1(lds + L2_CS + 32); lds_wait_ge(lds + L2_CS + 16, 4u); }
        if (m == 5) { lds_wait_ge(lds + L2_CP, 8u); dma3(15); }
        if (m == 6) { WAITV(0); if (lane == 0) lds_add1(lds + L2_CS + 48); lds_wait_ge(lds + L2_CS + 32, 4u); }
        if (m == PRIO_M) __builtin_amdgcn_s_setprio(0);
        BODY8();
      }
    } else {
      __builtin_amdgcn_s_setprio(1);
      TA(0); TB(1); TA(2); TB(3);
      if (attempt == 0) retile();
      TA(4); TB(5); TA(6); TB(7); ADV();
      if (attempt == 0) {
        WAITV(0);
        unsigned old = 0; if (lane == 0) old = __hip_atomic_fetch_add((LAS unsigned*)(lds + L2_CR), 1u, __ATOMIC_RELAXED, __HIP_MEMORY_SCOPE_WORKGROUP);
        if (lane == 0 && old == 3u) __hip_atomic_store((GAS unsigned*)(p.ws + W_FRT) + b, epoch, RLX_AGENT);
      }
#pragma nounroll
      for (int m = 1; m < 7; ++m) {
        if (m == 1) { if (lane == 0) lds_add1(lds + L2_CP); lds_wait_ge(lds + L2_CS, 4u); }
        if (m == 3 && attempt == 0) {
          wait_flags<4>(p.ws, W_FRT, epoch, lane);
          if (b == 0 && w == 0 && lane == 0) __hip_atomic_store((GAS unsigned*)(p.ws + W_EPOCH), epoch, RLX_AGENT);
          const int ngl = rk & 3, u0 = 27 * (xcd & 3) + 7 * ngl, part = (rk >> 2) * 4 + hh;
          const char* src = p.ws + W_WGU + (size_t)u0 * 40960;
          const int nl = (ngl == 3 ? 6 : 7) * 10;
          touch_sc1((part * nl + lane) * 128, src, lds0 + L2_DUMP);
          if (lane < nl - 64) touch_sc1((part * nl + 64 + lane) * 128, src, lds0 + L2_DUMP);
          const int ln = ((rk >> 3) * 4 + hh) * 80 + lane; const char* wsrc = p.wo + ((hg * 160) & ~127);
          touch((ln >> 1) * 1280 + (ln & 1) * 128, wsrc, lds0 + L2_DUMP);
          if (lane < 16) touch(((ln + 64) >> 1) * 1280 + (ln & 1) * 128, wsrc, lds0 + L2_DUMP);
        }
        if (m == 4) lds_wait_ge(lds + L2_CS + 16, 4u);
        if (m == 6) lds_wait_ge(lds + L2_CS + 32, 4u);
        BODY8();
      }
    }
    lds_wait_ge(lds + L2_CS + 48, 4u);
    {
      lptr zK1 = lds + oK1; lptr zK2 = lds + oK2; lptr zV = lds + oV;
      TA(0); TB(1);
      TILE(sc, sn, k1, k2, v0, v1, k1b, k2b, v0b, v1b, zK1, zK2, aV + 3 * KV_TILE, aV + 2 * KV_TILE + 2 * KV_VS);
      TILE(sn, sc, k1b, k2b, v0b, v1b, k1, k2, v0, v1, zK1 + KV_TILE, zK2 + KV_TILE, zV, aV + 3 * KV_TILE + 2 * KV_VS);
      aK1 = zK1; aK2 = zK2; aV = zV;
      TA(0); TB(1); TA(2); TB(3);
    }
    WAITL();
    o = MFMA16(asbf8(v1), asbf8(p1), o);
    __builtin_amdgcn_s_setprio(0);
    STAMPX(10);
    const float lsum = __uint_as_float(__builtin_amdgcn_permlane32_swap(__float_as_uint(o[8]), __float_as_uint(o[8]), false, false)[1]);
    if (attempt == 1) { LBAR(); break; }
    const bool bad = !(lsum >= 5.421010862427522e-20f);
    if (__any(bad)) *(LAS unsigned*)(lds + L2_FLAG) = 1u;
    LBAR();
    if (*(LAS unsigned*)(lds + L2_FLAG) == 0u) break;
    {
      float mx = -3.0e38f;
      for (int t = 0; t < 64; ++t) {
        const unsigned base = W_KV + t * KV_TILE;
        const u32x4 a1 = bld128<AUX_SC1>(ws, base + l16);
        const u32x2 a2 = bld64<AUX_SC1>(ws, base + KV_K2 + r32 * 8);
        f32x16 s = MFMA8(asbf4(a2), asbf4(q2), f32x16{}); s = MFMA16(asbf8(a1), asbf8(q1), s);
#pragma unroll
        for (int j = 0; j < 16; ++j) mx = fmaxf(mx, s[j]);
      }
      ref = swap32_max(mx);
      if (tid < 5) *(LAS unsigned*)(lds + L2_CP + tid * 16) = 0u;
      stage_first();
    }
  }
  {
    const float lsum = __uint_as_float(__builtin_amdgcn_permlane32_swap(__float_as_uint(o[8]), __float_as_uint(o[8]), false, false)[1]);
    const float inv = 1.0f / lsum;
    const u32x4 oa = {pk2(o[0] * inv, o[1] * inv), pk2(o[2] * inv, o[3] * inv), pk2(o[4] * inv, o[5] * inv), pk2(o[6] * inv, o[7] * inv)};
    const u32x2 ob = {pk2(o[8] * inv, o[9] * inv), pk2(o[10] * inv, o[11] * inv)};
    const unsigned tile16 = tg * 4 + th * 2 + (r32 >> 4), g0 = 10 * hg + 5 * (hh >> 1);
    bst128<AUX_SC1>(ws, W_OF + ((tile16 * 80 + g0 + 2 * (hh & 1) + h) * 16 + (r32 & 15)) * 16, oa);
    if (h == 0) bst64<AUX_SC1>(ws, W_OF + ((tile16 * 80 + g0 + 4) * 16 + (r32 & 15)) * 16 + 8 * (hh & 1), ob);
  }
  {
    const unsigned vo = (lane / 10) * 1280 + (lane % 10) * 16; const char* src = p.wo + hg * 160;
#pragma unroll
    for (int i = 0; i < 14; ++i) { int n = w + 8 * i; if (n > 106) n = 105;
      if (n == 106) { if (lane < 40) glds(vo, src + (size_t)n * 7680, rfl(lds0 + L3_WO + n * 960)); } else glds(vo, src + (size_t)n * 7680, rfl(lds0 + L3_WO + n * 960)); }
  }
  WAITV(14);
  LBAR();
  if (tid == 0) (void)__hip_atomic_fetch_add((GAS unsigned*)(p.ws + W_CNTO + tg * 256), 1u, RLX_AGENT);
}

DI void phase_oproj(const Params& p, lptr lds, rsrc_t ws, int b, u32x2 (&hres)[5], unsigned epoch) {
  const int tid = threadIdx.x, lane = tid & 63, w = rfl(tid >> 6);
  const int xcd = b & 7, rk = b >> 3, tg = 4 * xcd + (rk >> 3), cg = rk & 7;
  const int mt = w & 3, kh = w >> 2, t16 = lane & 15, g = lane >> 4, tq = t16 >> 2, tp = t16 & 3;
  const int tok = tg * 64 + mt * 16 + t16;
  u32x2 xr[5] = {};
  if (kh == 0) {
#pragma unroll
    for (int nt = 0; nt < 5; ++nt) xr[nt] = *(const GAS u32x2*)(p.x + (size_t)tok * 1280 + cg * 160 + nt * 32 + g * 8);
  }
  unsigned r1[10], r2[10];
#pragma unroll
  for (int i = 0; i < 10; ++i) {
    const int t = 4 * i + g, a5 = (t * 13) >> 6, m = t - 5 * a5, pair = 8 * kh + a5;
    const int last = m >> 2, ra = pair * 40 + (m >> 1) * 20 + (m & 1) * 4 - last * 24;
    r1[i] = L3_WO + (ra + tq) * 160 + tp * 8; r2[i] = r1[i] + (8 + 12 * last) * 160;
  }
  WAITV(0);
  wait_cnt2(p.ws, W_CNTO + tg * 256, W_CNTO + tg * 256, 8u, epoch);
  STAMPX(13);
  bf16x8 bf[2][5];
  u32x4 a[10];
#pragma unroll
  for (int i = 0; i < 10; ++i) a[i] = bld128<AUX_SC1>(ws, W_OF + ((tg * 4 + mt) * 80 + 4 * (10 * kh + i)) * 256 + lane * 16);
#pragma unroll
  for (int nt = 0; nt < 5; ++nt) bf[0][nt] = cat8(trread(lds + r1[0] + nt * 32), trread(lds + r2[0] + nt * 32));
  f32x4 acc[5] = {};
#pragma unroll
  for (int i = 0; i < 10; ++i) {
    if (i + 1 < 10) {
#pragma unroll
      for (int nt = 0; nt < 5; ++nt) bf[(i + 1) & 1][nt] = cat8(trread(lds + r1[i + 1] + nt * 32), trread(lds + r2[i + 1] + nt * 32));
    }
    SBAR();
#pragma unroll
    for (int nt = 0; nt < 5; ++nt) acc[nt] = MFMAS(bf[i & 1][nt], asbf8(a[i]), acc[nt]);
    SBAR();
  }
  STAMPX(14);
  LAS f32x4* red = (LAS f32x4*)(lds + L3_RED) + mt * 320 + lane;
  if (kh == 1) {
#pragma unroll
    for (int nt = 0; nt < 5; ++nt) red[nt * 64] = acc[nt];
  }
  LBAR();
  if (kh == 0) {
    float s1 = 0.f, s2 = 0.f;
#pragma unroll
    for (int nt = 0; nt < 5; ++nt) {
      const f32x4 o = red[nt * 64];
      const float h0 = bflo(xr[nt][0]) + acc[nt][0] + o[0], h1 = bfhi(xr[nt][0]) + acc[nt][1] + o[1], h2 = bflo(xr[nt][1]) + acc[nt][2] + o[2], h3 = bfhi(xr[nt][1]) + acc[nt][3] + o[3];
      const u32x2 hv = {pk2(h0, h1), pk2(h2, h3)};
      hres[nt] = hv;
      s1 = dot2(hv[0], ONES2, s1); s1 = dot2(hv[1], ONES2, s1); s2 = dot2(hv[0], hv[0], s2); s2 = dot2(hv[1], hv[1], s2);
      const int gam = 10 * cg + 2 * nt + (g >> 1);
      bst64<AUX_SC1>(ws, W_HF + (((tg * 4 + mt) * 20 + (gam >> 2)) * 64 + (gam & 3) * 16 + t16) * 16 + (g & 1) * 8, hv);
    }
    s1 = swap32_sum(swap16_sum(s1)); s2 = swap32_sum(swap16_sum(s2));
    if (g == 0) bst64<AUX_SC1>(ws, W_HST + ((tg * 8 + cg) * 64 + mt * 16 + t16) * 8, u32x2{__float_as_uint(s1), __float_as_uint(s2)});
  }
  signal_cnt(p.ws, W_CNTH + tg * 256);
}

constexpr unsigned L4_S = 0  , L4_ST = 122880  , L4_DUMP = 163584;
DI constexpr unsigned l5_slot(int c) { return (c & 3) == 0 ? 123904u : ((c & 3) == 1 ? 40960u : ((c & 3) == 2 ? 71680u : 0u)); }
constexpr unsigned L5_RED = 102400;
static_assert(123904 + 30720 <= L4_DUMP && L4_DUMP + 256 <= 163840, "lds");
DI void dma_wd_chunk(const char* wd, unsigned lds0, int w, unsigned l16, int c) {
#pragma unroll
  for (int i = 0; i < 4; ++i) { int n = w + 8 * i; if (n >= 30) n -= 8; const int k2 = n >= 15, j = n - 15 * k2;
    glds_sc1(l16, wd + (size_t)(864 * k2 + 96 * c) * 160 + j * 1024, rfl(lds0 + l5_slot(c) + n * 1024)); }
}
constexpr int GWIN = 5;
DI void phase_gateup(const Params& p, lptr lds, rsrc_t ws, int b, unsigned epoch) {
  const int tid = threadIdx.x, lane = tid & 63, w = rfl(tid >> 6);
  const int xcd = b & 7, rk = b >> 3, mb = 8 * (xcd >> 2) + (rk >> 2), ngl = rk & 3, u0 = 27 * (xcd & 3) + 7 * ngl, nu = ngl == 3 ? 6 : 7;
  const int t16 = lane & 15, g = lane >> 4;
  const unsigned lds0 = (unsigned)(uintptr_t)lds, l16 = lane * 16;
  const char* wd = p.ws + W_WD + (size_t)(rk & 7) * FF * 160;
  const bool act = w < nu;
  const int u = u0 + (act ? w : 0);
  const unsigned wbase = W_WGU + u * 40960 + l16;
  u32x4 ag[GWIN], au[GWIN];
  if (act) {
#pragma unroll
    for (int i = 0; i < GWIN; ++i) { ag[i] = bld128<AUX_SC1>(ws, wbase + i * 2048); au[i] = bld128<AUX_SC1>(ws, wbase + i * 2048 + 1024); }
  } else if (w == 7) {
#pragma unroll
    for (int i = 0; i < 9; ++i) { const int ln = i * 64 + lane; touch_sc1((ln < 540 ? ln : 539) * 128, wd + (rk >> 3) * 69120, rfl(lds0 + L4_DUMP)); }
  }
  dma_wd_chunk(wd, lds0, w, l16, 0);
  SBAR();
  wait_cnt2(p.ws, W_CNTH + (2 * mb) * 256, W_CNTH + (2 * mb + 1) * 256, 8u, epoch);
  STAMPX(16);
  u32x2 sv[8];
  if (tid < 128) {
#pragma unroll
    for (int c = 0; c < 8; ++c) sv[c] = bld64<AUX_SC1>(ws, W_HST + (((2 * mb + (tid >> 6)) * 8 + c) * 64 + (tid & 63)) * 8);
  }
  u32x4 st[4][5];
#pragma unroll
  for (int j = 0; j < 4; ++j)
#pragma unroll
    for (int i = 0; i < 5; ++i) st[j][i] = bld128<AUX_SC1>(ws, W_HF + ((mb * 8 + w) * 20 + 5 * j + i) * 1024 + l16);
  SBAR();
  if (tid < 128) {
    float s1 = 0.f, s2 = 0.f;
#pragma unroll
    for (int c = 0; c < 8; ++c) { s1 += __uint_as_float(sv[c][0]); s2 += __uint_as_float(sv[c][1]); }
    const float mean = s1 * (1.f / DM), rstd = rsqrtf(fmaxf(s2 * (1.f / DM) - mean * mean, 0.f) + LN_EPS);
    *(LAS f32x2*)(lds + L4_ST + tid * 8) = f32x2{mean, rstd};
  }
  lptr hw = lds + L4_S + w * 1024 + l16;
#pragma unroll
  for (int i = 0; i < 5; ++i) *(LAS u32x4*)(hw + i * 8192) = st[0][i];
  LBAR();
  STAMPX(17);
#pragma unroll
  for (int j = 1; j < 3; ++j)
#pragma unroll
    for (int i = 0; i < 5; ++i) *(LAS u32x4*)(hw + j * 40960 + i * 8192) = st[j][i];
  if (act) {
    f32x4 aG[8] = {}, aU[8] = {}; float csg[4] = {}, csu[4] = {};
    lptr hb = lds + L4_S + l16;
    u32x4 bb[2][4];
#pragma unroll
    for (int t = 0; t < 4; ++t) bb[0][t] = lds128(hb + t * 1024);
#pragma unroll
    for (int s = 0; s < 20; ++s) {
      const int slot = (s / 5) % 3, nslot = ((s + 1) / 5) % 3;
      if (s == 4) { asm volatile("" ::: "memory"); WAITL(); asm volatile("" ::: "memory"); BAR(); }
      if (s == 5) { BAR();
#pragma unroll
        for (int i = 0; i < 5; ++i) *(LAS u32x4*)(hw + i * 8192) = st[3][i]; }
      if (s == 14) BAR();
      if (s == 15) { BAR(); dma_wd_chunk(wd, lds0, w, l16, 1); dma_wd_chunk(wd, lds0, w, l16, 2); }
      const u32x4 cg_ = ag[s % GWIN], cu_ = au[s % GWIN];
      if (s + GWIN < 20) { ag[s % GWIN] = bld128<AUX_SC1>(ws, wbase + (s + GWIN) * 2048); au[s % GWIN] = bld128<AUX_SC1>(ws, wbase + (s + GWIN) * 2048 + 1024); }
#pragma unroll
      for (int t = 0; t < 4; ++t) bb[1][t] = lds128(hb + slot * 40960 + (s % 5) * 8192 + (4 + t) * 1024);
      SBAR();
#pragma unroll
      for (int t = 0; t < 4; ++t) { aG[t] = MFMAS(asbf8(cg_), asbf8(bb[0][t]), aG[t]); aU[t] = MFMAS(asbf8(cu_), asbf8(bb[0][t]), aU[t]); csg[t] = dot2(cg_[t], ONES2, csg[t]); }
      SBAR();
      if (s + 1 < 20) {
#pragma unroll
        for (int t = 0; t < 4; ++t) bb[0][t] = lds128(hb + nslot * 40960 + ((s + 1) % 5) * 8192 + t * 1024);
      }
      SBAR();
#pragma unroll
      for (int t = 0; t < 4; ++t) { aG[4 + t] = MFMAS(asbf8(cg_), asbf8(bb[1][t]), aG[4 + t]); aU[4 + t] = MFMAS(asbf8(cu_), asbf8(bb[1][t]), aU[4 + t]); csu[t] = dot2(cu_[t], ONES2, csu[t]); }
      SBAR();
    }
    STAMPX(18);
    const float sg = swap32_sum(swap16_sum((csg[0] + csg[1]) + (csg[2] + csg[3]))), su = swap32_sum(swap16_sum((csu[0] + csu[1]) + (csu[2] + csu[3])));
    float cg4[4], cu4[4];
#pragma unroll
    for (int j = 0; j < 4; ++j) { cg4[j] = __shfl(sg, 4 * g + j); cu4[j] = __shfl(su, 4 * g + j); }
#pragma unroll
    for (int i = 0; i < 4; ++i) {
      u32x2 pk[2];
#pragma unroll
      for (int e = 0; e < 2; ++e) {
        const int t = 2 * i + e;
        const f32x2 mr = *(LAS f32x2*)(lds + L4_ST + (t * 16 + t16) * 8);
        const float r2 = mr[1] * mr[1];
        float hv[4];
#pragma unroll
        for (int j = 0; j < 4; ++j) hv[j] = (aG[t][j] - mr[0] * cg4[j]) * (aU[t][j] - mr[0] * cu4[j]) * r2;
        pk[e] = u32x2{pk2(hv[0], hv[1]), pk2(hv[2], hv[3])};
      }
      const auto s0 = __builtin_amdgcn_permlane16_swap(pk[0][0], pk[1][0], false, false), s1 = __builtin_amdgcn_permlane16_swap(pk[0][1], pk[1][1], false, false);
      bst128<AUX_SC1>(ws, W_GF + (((mb * 8 + 2 * i + (g & 1)) * 54 + (u >> 1)) * 64 + (2 * (u & 1) + (g >> 1)) * 16 + t16) * 16, u32x4{s0[0], s1[0], s0[1], s1[1]});
    }
  } else {
    asm volatile("" ::: "memory"); WAITL(); asm volatile("" ::: "memory"); BAR();
    BAR();
#pragma unroll
    for (int i = 0; i < 5; ++i) *(LAS u32x4*)(hw + i * 8192) = st[3][i];
    asm volatile("" ::: "memory"); WAITL(); asm volatile("" ::: "memory");
    BAR();
    BAR(); dma_wd_chunk(wd, lds0, w, l16, 1); dma_wd_chunk(wd, lds0, w, l16, 2);
  }
  STAMPX(21);
  signal_cnt(p.ws, W_CNTG + mb * 256);
}

DI void gld128_sc1(u32x4& d, unsigned voff, const char* sbase) { asm volatile("global_load_dwordx4 %0, %1, %2 sc1" : "=v"(d) : "v"(voff), "s"(sbase) : "memory"); }
DI void phase_down(const Params& p, lptr lds, rsrc_t ws, int b, const u32x2 (&hres)[5], unsigned epoch) {
  const int tid = threadIdx.x, lane = tid & 63, w = rfl(tid >> 6);
  const int xcd = b & 7, rk = b >> 3, tg = 4 * xcd + (rk >> 3), cg = rk & 7;
  const int mt = w & 3, kh = w >> 2, t16 = lane & 15, g = lane >> 4, tq = t16 >> 2, tp = t16 & 3;
  const unsigned lds0 = (unsigned)(uintptr_t)lds, l16 = lane * 16;
  const char* wd = p.ws + W_WD + (size_t)cg * FF * 160;
  wait_cnt2(p.ws, W_CNTG + (tg >> 1) * 256, W_CNTG + (tg >> 1) * 256, 16u, epoch);
  STAMPX(20);
  const char* abase = p.ws + W_GF + (size_t)((tg * 4 + mt) * 54 + 27 * kh) * 1024;
  u32x4 a[4][3];
#pragma unroll
  for (int c = 0; c < 3; ++c)
#pragma unroll
    for (int k = 0; k < 3; ++k) gld128_sc1(a[c][k], l16, abase + (3 * c + k) * 1024);
  f32x4 acc[5] = {};
  const unsigned brow = kh * 15360 + (16 * (g >> 1) + 4 * (g & 1) + tq) * 160 + tp * 8;
#define DOWN_WAIT(N, c) asm volatile("s_waitcnt vmcnt(" #N ")\n\ts_barrier" : "+v"(a[(c) & 3][0]), "+v"(a[(c) & 3][1]), "+v"(a[(c) & 3][2]) :: "memory")
  bf16x8 bf[2][5];
  { lptr bb = lds + l5_slot(0) + brow;
#pragma unroll
    for (int nt = 0; nt < 5; ++nt) bf[0][nt] = cat8(trread(bb + nt * 32), trread(bb + nt * 32 + 1280)); }
#pragma unroll
  for (int c = 0; c < 9; ++c) {
    asm volatile("" ::: "memory"); WAITL(); asm volatile("" ::: "memory");
    if (c == 0) DOWN_WAIT(6, c); else if (c == 1) DOWN_WAIT(10, c); else if (c <= 5) DOWN_WAIT(10, c); else if (c == 6) DOWN_WAIT(10, c); else if (c == 7) DOWN_WAIT(3, c); else DOWN_WAIT(0, c);
    if (c + 3 < 9) { dma_wd_chunk(wd, lds0, w, l16, c + 3);
#pragma unroll
      for (int k = 0; k < 3; ++k) gld128_sc1(a[(c + 3) & 3][k], l16, abase + (3 * (c + 3) + k) * 1024); }
#pragma unroll
    for (int k = 0; k < 3; ++k) {
      const int sn = 3 * c + k + 1;
      if (sn < 27) { lptr bb = lds + l5_slot(sn / 3) + brow + (sn % 3) * 5120;
#pragma unroll
        for (int nt = 0; nt < 5; ++nt) bf[sn & 1][nt] = cat8(trread(bb + nt * 32), trread(bb + nt * 32 + 1280)); }
      SBAR();
#pragma unroll
      for (int nt = 0; nt < 5; ++nt) acc[nt] = MFMAS(bf[(3 * c + k) & 1][nt], asbf8(a[c & 3][k]), acc[nt]);
      SBAR();
    }
  }
  STAMPX(22);
  LAS f32x4* red = (LAS f32x4*)(lds + L5_RED) + mt * 320 + lane;
  if (kh == 1) {
#pragma unroll
    for (int nt = 0; nt < 5; ++nt) red[nt * 64] = acc[nt];
  }
  LBAR();
  if (kh == 0) {
    const int tok = tg * 64 + mt * 16 + t16;
#pragma unroll
    for (int nt = 0; nt < 5; ++nt) {
      const f32x4 o = red[nt * 64];
      const u32x2 r = {pk2(bflo(hres[nt][0]) + acc[nt][0] + o[0], bfhi(hres[nt][0]) + acc[nt][1] + o[1]), pk2(bflo(hres[nt][1]) + acc[nt][2] + o[2], bfhi(hres[nt][1]) + acc[nt][3] + o[3])};
      *(GAS u32x2*)(p.out + (size_t)tok * 1280 + cg * 160 + nt * 32 + g * 8) = r;
    }
  }
}

__global__ void __launch_bounds__(NTHR, 2) k_block(Params p) {
  extern __shared__ __attribute__((aligned(16))) char smem[];
  lptr lds = (lptr)smem;
  const rsrc_t ws = __builtin_amdgcn_make_buffer_rsrc((void*)p.ws, 0, (int)W_END, 0x00020000);
  const int b = blockIdx.x;
  u32x4 q1; u32x2 q2; unsigned epoch;
  u32x2 hres[5] = {};
  {
    unsigned long long pc; asm volatile("s_getpc_b64 %0" : "=s"(pc));
    if (threadIdx.x >= 448) { const int ln = (b >> 3) * 16 + (threadIdx.x & 15); if ((threadIdx.x & 63) < 16 && ln * 128 < CODEPF)
      touch(ln * 128, (const char*)(pc & ~127ull), (unsigned)(uintptr_t)smem + 163584); }
  }
  STAMPX(0);
  phase1(p, lds, ws, b, q1, q2, epoch);
  STAMPX(5);
  phase_attn(p, lds, ws, b, q1, q2, epoch);
  STAMPX(12);
  phase_oproj(p, lds, ws, b, hres, epoch);
  STAMPX(15);
  phase_gateup(p, lds, ws, b, epoch);
  STAMPX(19);
  phase_down(p, lds, ws, b, hres, epoch);
  STAMPX(23);
}

extern "C" void kernel_launch(void* const* d_in, const int* in_sizes, int n_in, void* d_out, int out_size, void* d_ws, size_t ws_size, hipStream_t stream) {
  Params p;
  p.x = (const char*)d_in[0]; p.wq = (const char*)d_in[1]; p.wk = (const char*)d_in[2]; p.wv = (const char*)d_in[3]; p.wo = (const char*)d_in[4];
  p.wg = (const char*)d_in[6]; p.wu = (const char*)d_in[7]; p.wd = (const char*)d_in[8];
  p.out = (char*)d_out; p.ws = (char*)d_ws;
  constexpr int LDS_BYTES = 163840;
  (void)hipFuncSetAttribute((const void*)k_block, hipFuncAttributeMaxDynamicSharedMemorySize, LDS_BYTES);
  k_block<<<NWG, NTHR, LDS_BYTES, stream>>>(p);
}
```

```cpp
#include <hip/hip_runtime.h>
#include <stdint.h>
#define CODEPF 55296
#define TLBPF 2097152

#define DI __device__ __forceinline__
#define LAS __attribute__((address_space(3)))
#define GAS __attribute__((address_space(1)))
typedef short bf16x8 __attribute__((ext_vector_type(8)));
typedef short bf16x4 __attribute__((ext_vector_type(4)));
typedef __bf16 bfv2 __attribute__((ext_vector_type(2)));
typedef float f32x2 __attribute__((ext_vector_type(2)));
typedef float f32x4 __attribute__((ext_vector_type(4)));
typedef float f32x16 __attribute__((ext_vector_type(16)));
typedef unsigned u32x2 __attribute__((ext_vector_type(2)));
typedef unsigned u32x4 __attribute__((ext_vector_type(4)));
typedef LAS char* lptr;

constexpr int SEQ = 2048, DM = 640, FF = 1728;
constexpr int NWG = 256, NTHR = 512;
constexpr float LN_EPS = 1e-5f;
constexpr float QSCALE = 0.22360679774997896f * 1.4426950408889634f;
constexpr unsigned KV_TILE = 2688, KV_K2 = 1024, KV_V = 1280, KV_VS = 352;

constexpr unsigned W_EPOCH = 0, W_TMO = 256;
constexpr unsigned W_FKV = 1024  , W_FRT = 2048  , W_KMAX = 4096  ;
constexpr unsigned W_CNTO = 8192, W_CNTH = 16384, W_CNTG = 24576;
constexpr unsigned W_KV = 65536;
constexpr unsigned W_OF = W_KV + 64 * KV_TILE;
constexpr unsigned W_HF = W_OF + SEQ * DM * 2;
constexpr unsigned W_HST = W_HF + SEQ * DM * 2;
constexpr unsigned W_GF = W_HST + 32 * 8 * 64 * 8;
constexpr unsigned W_WGU = W_GF + 128 * 54 * 1024;
constexpr unsigned W_WD = W_WGU + 108 * 40 * 1024;
constexpr unsigned W_END = W_WD + 8 * FF * 160;

#define STAMPW() do { } while (0)
#define STAMP(i) do { } while (0)
#define STW 0
#define STAMPX(i) do { STAMP(i); if (STW == (i)) STAMPW(); } while (0)

struct Params {
  const char* x; const char* wq; const char* wk; const char* wv; const char* wo;
  const char* wg; const char* wu; const char* wd;
  char* out; char* ws;
};

constexpr int AUX_SC1 = 16;
#define MFMA16(a, b, c) __builtin_amdgcn_mfma_f32_32x32x16_bf16((a), (b), (c), 0, 0, 0)
#define MFMA8(a, b, c) __builtin_amdgcn_mfma_f32_32x32x8bf16_1k((a), (b), (c), 0, 0, 0)
#define MFMAS(a, b, c) __builtin_amdgcn_mfma_f32_16x16x32_bf16((a), (b), (c), 0, 0, 0)
#define SBAR() __builtin_amdgcn_sched_barrier(0)
#define WAITV(n) asm volatile("s_waitcnt vmcnt(" #n ")" ::: "memory")
#define BAR() asm volatile("s_barrier" ::: "memory")
#define WAITL() __builtin_amdgcn_s_waitcnt(0xC07F)
#define LBAR() do { asm volatile("" ::: "memory"); WAITL(); BAR(); } while (0)
#define PIN(x) asm volatile("" : "+v"(x))

DI unsigned pk2(float lo, float hi) { f32x2 v = {lo, hi}; return __builtin_bit_cast(unsigned, __builtin_convertvector(v, bfv2)); }
DI float bflo(unsigned v) { return __uint_as_float(v << 16); }
DI float bfhi(unsigned v) { return __uint_as_float(v & 0xffff0000u); }
DI float dot2(unsigned a, unsigned b, float c) { return __builtin_amdgcn_fdot2_f32_bf16(__builtin_bit_cast(bfv2, a), __builtin_bit_cast(bfv2, b), c, false); }
constexpr unsigned ONES2 = 0x3f803f80u;
DI float swap32_sum(float v) { auto r = __builtin_amdgcn_permlane32_swap(__float_as_uint(v), __float_as_uint(v), false, false); return __uint_as_float(r[0]) + __uint_as_float(r[1]); }
DI float swap32_max(float v) { auto r = __builtin_amdgcn_permlane32_swap(__float_as_uint(v), __float_as_uint(v), false, false); return fmaxf(__uint_as_float(r[0]), __uint_as_float(r[1])); }
DI float swap16_sum(float v) { auto r = __builtin_amdgcn_permlane16_swap(__float_as_uint(v), __float_as_uint(v), false, false); return __uint_as_float(r[0]) + __uint_as_float(r[1]); }
template <int CTRL> DI float dppf(float v) { return __uint_as_float(__builtin_amdgcn_update_dpp(0u, __float_as_uint(v), CTRL, 0xf, 0xf, true)); }
DI float sum8(float v) { v += dppf<0xB1>(v); v += dppf<0x4E>(v); return v + dppf<0x141>(v); }
DI float max16(float v) { v = fmaxf(v, dppf<0x128>(v)); v = fmaxf(v, dppf<0x124>(v)); v = fmaxf(v, dppf<0x122>(v)); return fmaxf(v, dppf<0x121>(v)); }
DI float max64(float v) { v = max16(v); auto r = __builtin_amdgcn_permlane16_swap(__float_as_uint(v), __float_as_uint(v), false, false);
  return swap32_max(fmaxf(__uint_as_float(r[0]), __uint_as_float(r[1]))); }
typedef short v4i16_t __attribute__((ext_vector_type(4)));
DI bf16x4 trread(lptr p) { return __builtin_bit_cast(bf16x4, __builtin_amdgcn_ds_read_tr16_b64_v4i16((LAS v4i16_t*)p)); }
DI bf16x8 cat8(bf16x4 a, bf16x4 b) { return __builtin_shufflevector(a, b, 0, 1, 2, 3, 4, 5, 6, 7); }
DI u32x4 lds128(lptr p) { return *(LAS u32x4*)p; }
DI u32x2 lds64(lptr p) { return *(LAS u32x2*)p; }
DI bf16x8 asbf8(u32x4 v) { return __builtin_bit_cast(bf16x8, v); }
DI bf16x4 asbf4(u32x2 v) { return __builtin_bit_cast(bf16x4, v); }
DI u32x4 asu4(bf16x8 v) { return __builtin_bit_cast(u32x4, v); }
DI unsigned rfl(unsigned v) { return __builtin_amdgcn_readfirstlane(v); }
typedef __amdgpu_buffer_rsrc_t rsrc_t;
template <int AUX> DI u32x4 bld128(rsrc_t r, unsigned off) { return __builtin_amdgcn_raw_buffer_load_b128(r, off, 0, AUX); }
template <int AUX> DI u32x2 bld64(rsrc_t r, unsigned off) { return __builtin_amdgcn_raw_buffer_load_b64(r, off, 0, AUX); }
template <int AUX> DI unsigned bld32(rsrc_t r, unsigned off) { return __builtin_amdgcn_raw_buffer_load_b32(r, off, 0, AUX); }
template <int AUX> DI void bst128(rsrc_t r, unsigned off, u32x4 v) { __builtin_amdgcn_raw_buffer_store_b128(v, r, off, 0, AUX); }
template <int AUX> DI void bst64(rsrc_t r, unsigned off, u32x2 v) { __builtin_amdgcn_raw_buffer_store_b64(v, r, off, 0, AUX); }
template <int AUX> DI void bst32(rsrc_t r, unsigned off, unsigned v) { __builtin_amdgcn_raw_buffer_store_b32(v, r, off, 0, AUX); }
DI void glds(unsigned voff, const char* sbase, unsigned lds_base) {
  unsigned sv; lds_base = rfl(lds_base);
  asm volatile("s_mov_b32 %0, m0\n\ts_mov_b32 m0, %3\n\ts_nop 0\n\tglobal_load_lds_dwordx4 %1, %2\n\ts_mov_b32 m0, %0" : "=&s"(sv) : "v"(voff), "s"(sbase), "s"(lds_base) : "memory");
}
DI void glds_sc1(unsigned voff, const char* sbase, unsigned lds_base) {
  unsigned sv; lds_base = rfl(lds_base);
  asm volatile("s_mov_b32 %0, m0\n\ts_mov_b32 m0, %3\n\ts_nop 0\n\tglobal_load_lds_dwordx4 %1, %2 sc1\n\ts_mov_b32 m0, %0" : "=&s"(sv) : "v"(voff), "s"(sbase), "s"(lds_base) : "memory");
}
DI void touch(unsigned voff, const char* sbase, unsigned lds_dump) {
  unsigned sv; lds_dump = rfl(lds_dump);
  asm volatile("s_mov_b32 %0, m0\n\ts_mov_b32 m0, %3\n\ts_nop 0\n\tglobal_load_lds_dword %1, %2\n\ts_mov_b32 m0, %0" : "=&s"(sv) : "v"(voff), "s"(sbase), "s"(lds_dump) : "memory");
}
DI void touch_sc1(unsigned voff, const char* sbase, unsigned lds_dump) {
  unsigned sv; lds_dump = rfl(lds_dump);
  asm volatile("s_mov_b32 %0, m0\n\ts_mov_b32 m0, %3\n\ts_nop 0\n\tglobal_load_lds_dword %1, %2 sc1\n\ts_mov_b32 m0, %0" : "=&s"(sv) : "v"(voff), "s"(sbase), "s"(lds_dump) : "memory");
}

#define RLX_AGENT __ATOMIC_RELAXED, __HIP_MEMORY_SCOPE_AGENT
constexpr unsigned SPIN_CAP = 1u << 17;
DI bool spin_fail(unsigned& spins, char* wsp, unsigned epoch) {
  __builtin_amdgcn_s_sleep(1);
  GAS unsigned* tmo = (GAS unsigned*)(wsp + W_TMO);
  if (++spins > SPIN_CAP) { __hip_atomic_store(tmo, epoch, RLX_AGENT); return true; }
  return (spins & 255u) == 0u && rfl(__hip_atomic_load(tmo, RLX_AGENT)) == epoch;
}
template <int NW> DI void wait_flags(char* wsp, unsigned off, unsigned epoch, int lane) {
  for (unsigned spins = 0;;) {
    bool ok = true;
#pragma unroll
    for (int i = 0; i < NW; ++i) ok &= __hip_atomic_load((GAS unsigned*)(wsp + off) + i * 64 + lane, RLX_AGENT) == epoch;
    if (__all(ok)) break;
    if (spin_fail(spins, wsp, epoch)) break;
  }
  __builtin_amdgcn_fence(__ATOMIC_ACQUIRE, "wavefront");
}
DI void wait_cnt2(char* wsp, unsigned off0, unsigned off1, unsigned want, unsigned epoch) {
  if (threadIdx.x < 64) {
    GAS unsigned* word = (GAS unsigned*)(wsp + ((threadIdx.x & 1) ? off1 : off0));
    for (unsigned spins = 0;;) {
      if (__all(__hip_atomic_load(word, RLX_AGENT) >= want)) break;
      if (spin_fail(spins, wsp, epoch)) break;
    }
    __builtin_amdgcn_fence(__ATOMIC_ACQUIRE, "wavefront");
  }
  LBAR();
}
DI void signal_cnt(char* wsp, unsigned off) {
  WAITV(0);
  LBAR();
  if (threadIdx.x == 0) (void)__hip_atomic_fetch_add((GAS unsigned*)(wsp + off), 1u, RLX_AGENT);
}
DI void lds_add1(lptr p) { (void)__hip_atomic_fetch_add((LAS unsigned*)p, 1u, __ATOMIC_RELAXED, __HIP_MEMORY_SCOPE_WORKGROUP); }
DI void lds_wait_ge(lptr p, unsigned want) {
  while ((unsigned)rfl(__hip_atomic_load((LAS unsigned*)p, __ATOMIC_RELAXED, __HIP_MEMORY_SCOPE_WORKGROUP)) < want) __builtin_amdgcn_s_sleep(2);
  asm volatile("" ::: "memory");
}

constexpr unsigned L1_X = 0, L1_S = 81920, L1_WK = L1_S + 4 * 10240, L1_PART = L1_WK + 25600;
constexpr unsigned L1_CS = L1_PART + 12672  , L1_ST = L1_CS + 1024  , L1_KC = L1_ST + 512  , L1_DUMP = L1_KC + 128;
static_assert(L1_DUMP + 256 <= 163840, "lds");

DI void phase1(const Params& p, lptr lds, rsrc_t ws, int b, u32x4& q1, u32x2& q2, unsigned& epoch) {
  const int tid = threadIdx.x, lane = tid & 63, w = rfl(tid >> 6);
  const int r32 = lane & 31, h = lane >> 5;
  const int xcd = b & 7, rk = b >> 3, tg = 4 * xcd + (rk >> 3), hg = rk & 7;
  const unsigned lds0 = (unsigned)(uintptr_t)lds, l16 = lane * 16;
  unsigned ep = __hip_atomic_load((GAS unsigned*)(p.ws + W_EPOCH), RLX_AGENT);
  {
    const char* xb = p.x + (size_t)(tg * 64 + 8 * w) * 1280;
    const int tl = lane >> 4;
    unsigned v0 = tl * 1280 + ((lane ^ (8 * w + tl)) & 15) * 16, v1 = (4 + tl) * 1280 + ((lane ^ (8 * w + 4 + tl)) & 15) * 16, xd = lds0 + L1_X + 8 * w * 256;
#pragma unroll 1
    for (int a = 0; a < 5; ++a) { glds(v0, xb, xd); glds(v1, xb, xd + 1024); v0 += 256; v1 += 256; xd += 16384; }
  }
  if (hg < 4) {
    const char* W = (hg < 2 ? p.wk : p.wv) + w * 1024;
#pragma unroll
    for (int i = 0; i < 3; ++i) glds(l16 + i * 8192, W, lds0 + L1_WK + w * 1024 + i * 8192);
    if (w == 0) glds(l16 + 3 * 8192, W, lds0 + L1_WK + 3 * 8192);
  }
  BAR();
  const char* wqb = p.wq + hg * 160;
  unsigned vq[3], dq[3];
#pragma unroll
  for (int i = 0; i < 3; ++i) {
    const int n = i < 2 ? w + 8 * i : (w < 6 ? w + 16 : w + 8), cc = n >= 11, pc = n - 11 * cc;
    const int slot = 6 * pc + lane / 10, row = (slot & ~7) | ((slot & 1) << 2) | ((slot >> 1) & 3);
    vq[i] = (64 * cc + row) * 1280 + (lane % 10) * 16; dq[i] = cc * 10240 + pc * 960;
  }
  const bool m1 = w != 2 || lane < 40, m2 = w < 6 ? (w != 5 || lane < 40) : lane == 0;
  auto dma_pair = [&](const char* src, unsigned dst) {
    glds(vq[0], src, dst + dq[0]);
    if (m1) glds(vq[1], src, dst + dq[1]);
    if (m2) glds(vq[2], src, dst + dq[2]);
  };
  dma_pair(wqb, lds0 + L1_S); dma_pair(wqb + 128 * 1280, lds0 + L1_S + 20480);
  SBAR();
  STAMPX(8);
  const int i16 = lane & 15, tq = i16 >> 2, tp = i16 & 3, grp = (lane >> 4) & 1;
  WAITV(6);
  LBAR();
  PIN(ep); epoch = rfl(ep) + 1u;
  STAMPX(1);
  const int kth = hg & 1; const bool isV = hg >= 2;
  f32x16 kacc = {}; float kcs = 0.f;
  if (w >= 4) {
#pragma unroll
    for (int ps = 0; ps < 2; ++ps) {
      const int t = (ps ? 32 - 32 * kth : 32 * kth) + 8 * (w - 4) + (lane >> 3), e = lane & 7;
      u32x4 v[10];
#pragma unroll
      for (int i = 0; i < 10; ++i) v[i] = lds128(lds + L1_X + (i >> 1) * 16384 + t * 256 + ((e + 8 * (i & 1) + 8 * ((t >> 1) & 1)) & 15) * 16);
      float a4[4] = {}, b4[4] = {};
#pragma unroll
      for (int i = 0; i < 10; ++i)
#pragma unroll
        for (int j = 0; j < 4; ++j) { a4[j] = dot2(v[i][j], v[i][j], a4[j]); b4[j] = dot2(v[i][j], ONES2, b4[j]); }
      const float sq = sum8((a4[0] + a4[1]) + (a4[2] + a4[3])), sm = sum8((b4[0] + b4[1]) + (b4[2] + b4[3]));
      const float mean = sm * (1.f / DM), rstd = rsqrtf(fmaxf(sq * (1.f / DM) - mean * mean, 0.f) + LN_EPS);
      if (e == 0) { ((LAS float*)(lds + L1_ST))[t] = mean; ((LAS float*)(lds + L1_ST))[64 + t] = rstd; }
      if (ps == 0) {
        LBAR(); dma_pair(wqb + 256 * 1280, lds0 + L1_WK);
        const int ln = ((rk >> 3) * 4 + (w - 4)) * 32 + r32;
        if (h == 0) touch((ln >> 1) * 1280 + (ln & 1) * 128, wqb + 384 * 1280 - ((hg * 160) & 127), lds0 + L1_DUMP);
      }
    }
  } else if (hg < 4) {
    lptr wb = lds + L1_WK + (160 * w + 8 * h + tq) * 40 + grp * 32 + 8 * tp;
    const int tok = kth * 32 + r32;
    u32x4 xf[10]; bf16x8 wf[10];
#pragma unroll
    for (int i = 0; i < 10; ++i) { const int c = 20 * w + 2 * i + h;
      xf[i] = lds128(lds + L1_X + (c >> 4) * 16384 + tok * 256 + ((c ^ tok) & 15) * 16); wf[i] = cat8(trread(wb + i * 640), trread(wb + i * 640 + 160)); }
    if (isV) {
#pragma unroll
      for (int i = 0; i < 10; ++i) kacc = MFMA16(asbf8(xf[i]), wf[i], kacc);
    } else {
#pragma unroll
      for (int i = 0; i < 10; ++i) kacc = MFMA16(wf[i], asbf8(xf[i]), kacc);
    }
    float c4[4] = {};
#pragma unroll
    for (int i = 0; i < 10; ++i) { const u32x4 wu = asu4(wf[i]);
#pragma unroll
      for (int j = 0; j < 4; ++j) c4[j] = dot2(wu[j], ONES2, c4[j]); }
    kcs = swap32_sum((c4[0] + c4[1]) + (c4[2] + c4[3]));
    if (w > 0) {
      LAS f32x4* pa = (LAS f32x4*)(lds + L1_PART + (w - 1) * 4224) + lane;
#pragma unroll
      for (int g = 0; g < 4; ++g) pa[g * 64] = f32x4{kacc[4 * g], kacc[4 * g + 1], kacc[4 * g + 2], kacc[4 * g + 3]};
      if (h == 0) ((LAS float*)(lds + L1_PART + (w - 1) * 4224 + 4096))[r32] = kcs;
    }
  }
  if (w < 4) {
    LBAR();
    dma_pair(wqb + 256 * 1280, lds0 + L1_WK);
  }
  if (hg < 4 && w == 0) {
#pragma unroll
    for (int v = 0; v < 3; ++v) {
      LAS f32x4* pa = (LAS f32x4*)(lds + L1_PART + v * 4224) + lane;
#pragma unroll
      for (int g = 0; g < 4; ++g) { const f32x4 t = pa[g * 64]; kacc[4 * g] += t[0]; kacc[4 * g + 1] += t[1]; kacc[4 * g + 2] += t[2]; kacc[4 * g + 3] += t[3]; }
      kcs += ((LAS float*)(lds + L1_PART + v * 4224 + 4096))[r32];
    }
    const int tile = tg * 2 + kth;
    const LAS float* st = (const LAS float*)(lds + L1_ST) + kth * 32;
    if (lane == 0 && hg == 0) { bst32<AUX_SC1>(ws, W_CNTO + tg * 256, 0u); bst32<AUX_SC1>(ws, W_CNTH + tg * 256, 0u); }
    if (lane == 0 && hg == 1 && !(tg & 1)) bst32<AUX_SC1>(ws, W_CNTG + (tg >> 1) * 256, 0u);
    if (!isV) {
      LAS float* tab = (LAS float*)(lds + L1_KC);
      if (h == 0) tab[r32] = kcs;
      const float mean = st[r32], rstd = st[64 + r32];
      asm volatile("" ::: "memory"); WAITL(); asm volatile("" ::: "memory");
      float kv[12];
#pragma unroll
      for (int g = 0; g < 3; ++g) { const f32x4 c4 = *(LAS f32x4*)(tab + 8 * g + 4 * h);
#pragma unroll
        for (int j = 0; j < 4; ++j) kv[4 * g + j] = (kacc[4 * g + j] - mean * c4[j]) * rstd; }
      const u32x4 k1 = {pk2(kv[0], kv[1]), pk2(kv[2], kv[3]), pk2(kv[4], kv[5]), pk2(kv[6], kv[7])};
      const u32x2 k2 = {pk2(kv[8], kv[9]), pk2(kv[10], kv[11])};
      bst128<AUX_SC1>(ws, W_KV + tile * KV_TILE + l16, k1);
      if (h == 0) bst64<AUX_SC1>(ws, W_KV + tile * KV_TILE + KV_K2 + r32 * 8, k2);
      float n2 = 0.f;
#pragma unroll
      for (int j = 0; j < 4; ++j) n2 = dot2(k1[j], k1[j], n2);
      if (h == 0) { n2 = dot2(k2[0], k2[0], n2); n2 = dot2(k2[1], k2[1], n2); }
      n2 = max64(swap32_sum(n2));
      if (lane == 0) bst32<AUX_SC1>(ws, W_KMAX + tile * 4, __float_as_uint(n2));
    } else {
      const unsigned cv = r32 == 20 ? ONES2 : 0u;
#pragma unroll
      for (int s = 0; s < 2; ++s) {
        float vv[8];
#pragma unroll
        for (int g = 0; g < 2; ++g) { const f32x4 m4 = *(const LAS f32x4*)(st + 16 * s + 8 * g + 4 * h), r4 = *(const LAS f32x4*)(st + 64 + 16 * s + 8 * g + 4 * h);
#pragma unroll
          for (int j = 0; j < 4; ++j) vv[4 * g + j] = (kacc[8 * s + 4 * g + j] - m4[j] * kcs) * r4[j]; }
        u32x4 o = {pk2(vv[0], vv[1]), pk2(vv[2], vv[3]), pk2(vv[4], vv[5]), pk2(vv[6], vv[7])};
        if (r32 >= 20) o = u32x4{cv, cv, cv, cv};
        if (r32 < 22) bst128<AUX_SC1>(ws, W_KV + tile * KV_TILE + KV_V + (2 * s + h) * KV_VS + r32 * 16, o);
      }
    }
  }
  if (w >= 4) WAITV(4); else if (hg < 4 && w == 0) WAITV(5); else WAITV(3);
  LBAR();
  STAMPX(2);
  {
    const int hh = w & 3, th = w >> 2, tok = th * 32 + r32;
    f32x16 acc = {}; float c4[4] = {};
    const unsigned wrow = (8 * h + 2 * tq) * 160 + hh * 40 + grp * 32 + 8 * tp;
    unsigned xo[8];
#pragma unroll
    for (int i = 0; i < 8; ++i) xo[i] = L1_X + tok * 256 + (((2 * i + h) ^ tok) & 15) * 16;
    u32x4 xf[2][4]; bf16x8 wf[2][4];
    unsigned xa = 0, wa = L1_S + wrow;
#define Q_RD1(e, ks, XA, WA) do { xf[e][ks] = lds128(lds + xo[4 * (e) + (ks)] + (XA)); lptr a_ = lds + (WA) + (e) * 10240 + (ks) * 2560; wf[e][ks] = cat8(trread(a_), trread(a_ + 160)); } while (0)
#define Q_STEP(e, XA, WA) do { _Pragma("unroll") for (int ks = 0; ks < 4; ++ks) { acc = MFMA16(wf[e][ks], asbf8(xf[e][ks]), acc); Q_RD1(1 - (e), ks, XA, WA); SBAR(); } } while (0)
#define Q_CS(e) do { _Pragma("unroll") for (int ks = 0; ks < 4; ++ks) { const u32x4 wu = asu4(wf[e][ks]); _Pragma("unroll") for (int j = 0; j < 4; ++j) c4[j] = dot2(wu[j], ONES2, c4[j]); } } while (0)
#pragma unroll
    for (int ks = 0; ks < 4; ++ks) Q_RD1(0, ks, xa, wa);
#pragma unroll 1
    for (int pr = 0; pr < 5; ++pr) {
      Q_STEP(0, xa, wa);
      if (pr == 1 || pr == 2) WAITV(3); else if (pr == 3) WAITV(0);
      if (pr == 1 && w == 0 && hg < 4 && lane == 0)
        __hip_atomic_store((GAS unsigned*)(p.ws + W_FKV) + tg * 4 + hg, epoch, RLX_AGENT);
      LBAR();
      const unsigned wn = pr == 1 ? L1_WK + wrow : (pr == 2 || pr == 4 ? L1_S + wrow : wa + 20480);
      if (pr < 2) dma_pair(wqb + (size_t)(pr + 3) * 128 * 1280, lds0 + L1_S + pr * 20480);
      if (th == 0) Q_CS(0); else Q_CS(1);
      SBAR();
      xa = pr < 4 ? xa + 16384 : 0;
      Q_STEP(1, xa, wn);
      wa = wn;
    }
#undef Q_RD1
#undef Q_STEP
#undef Q_CS
    const float cs = swap32_sum((c4[0] + c4[1]) + (c4[2] + c4[3]));
    if (h == 0) ((LAS float*)(lds + L1_CS))[w * 32 + r32] = cs;
    const float mean = ((LAS float*)(lds + L1_ST))[tok], rstd = ((LAS float*)(lds + L1_ST))[64 + tok] * QSCALE;
    LBAR();
    float qv[12];
#pragma unroll
    for (int g = 0; g < 3; ++g) {
      const f32x4 ca = *(LAS f32x4*)(lds + L1_CS + (hh * 32 + 8 * g + 4 * h) * 4), cb = *(LAS f32x4*)(lds + L1_CS + ((hh + 4) * 32 + 8 * g + 4 * h) * 4);
#pragma unroll
      for (int j = 0; j < 4; ++j) qv[4 * g + j] = (acc[4 * g + j] - mean * (ca[j] + cb[j])) * rstd;
    }
    q1 = u32x4{pk2(qv[0], qv[1]), pk2(qv[2], qv[3]), pk2(qv[4], qv[5]), pk2(qv[6], qv[7])};
    q2 = h ? u32x2{0u, 0u} : u32x2{pk2(qv[8], qv[9]), pk2(qv[10], qv[11])};
  }
  STAMPX(3);
}

constexpr unsigned L2_FLAG = 60 * KV_TILE, L2_CR = L2_FLAG + 16, L2_CP = L2_FLAG + 32, L2_CS = L2_FLAG + 48, L2_DUMP = L2_FLAG + 256;
constexpr unsigned L2_RT = 52 * KV_TILE;
constexpr unsigned L3_WO = 0, L3_RED = 102400;
#define PRIO_M 6

DI void phase_attn(const Params& p, lptr lds, rsrc_t ws, int b, const u32x4 q1, const u32x2 q2, unsigned epoch) {
  const int tid = threadIdx.x, lane = tid & 63, w = rfl(tid >> 6);
  const int r32 = lane & 31, h = lane >> 5;
  const int xcd = b & 7, rk = b >> 3, tg = 4 * xcd + (rk >> 3), hg = rk & 7;
  const int hh = w & 3, th = w >> 2;
  const unsigned lds0 = (unsigned)(uintptr_t)lds, l16 = lane * 16;
  const char* kvg = p.ws + W_KV;
  auto dma3 = [&](int c) {
    const unsigned dst = lds0 + (c == 15 ? 0 : c) * 4 * KV_TILE; const char* src = kvg + c * 4 * KV_TILE;
    glds_sc1(l16, src + hh * 1024, rfl(dst + hh * 1024));
    glds_sc1(l16, src + (hh + 4) * 1024, rfl(dst + (hh + 4) * 1024));
    const int n = hh == 3 ? 7 : hh + 8;
    if (hh == 2) { if (lane < 32) glds_sc1(l16, src + n * 1024, rfl(dst + n * 1024)); } else glds_sc1(l16, src + n * 1024, rfl(dst + n * 1024));
  };
  auto stage_first = [&]() { if (th == 0) { dma3(0); dma3(2); } else dma3(1); };
  u32x4 rg[2][4], rd[3];
  const int ga = b * 4 + hh, dj0 = (ga - 304) * 3;
  if (th == 0) {
#pragma unroll
    for (int e = 0; e < 2; ++e) { const int gj = ga + 1024 * e;
      if (gj < 1080) { const int cgf = gj / 40, r = gj % 40; const char* W = (r & 1) ? p.wu : p.wg;
#pragma unroll
        for (int i = 0; i < 4; ++i) rg[e][i] = *(const GAS u32x4*)(W + (size_t)(32 * (r >> 1) + 8 * i + (lane >> 3)) * (FF * 2) + cgf * 128 + (lane & 7) * 16); } }
    if (ga >= 304) {
#pragma unroll
      for (int i = 0; i < 3; ++i) rd[i] = *(const GAS u32x4*)(p.wd + (size_t)(dj0 + i) * 1024 + l16);
    }
  }
  auto retile = [&]() {
    const int i16 = lane & 15, tq = i16 >> 2, tp = i16 & 3, g4 = lane >> 4;
    lptr sc = lds + L2_RT + hh * 4096;
#pragma unroll
    for (int e = 0; e < 2; ++e) { const int gj = ga + 1024 * e;
      if (gj < 1080) { const int cgf = gj / 40, r = gj % 40;
#pragma unroll
        for (int i = 0; i < 4; ++i) *(LAS u32x4*)(sc + i * 1024 + l16) = rg[e][i];
#pragma unroll
        for (int u = 0; u < 4; ++u) { lptr a = sc + (8 * g4 + tq) * 128 + u * 32 + tp * 8;
          bst128<AUX_SC1>(ws, W_WGU + (((cgf * 4 + u) * 20 + (r >> 1)) * 2 + (r & 1)) * 1024 + l16, asu4(cat8(trread(a), trread(a + 512)))); } } }
    if (ga >= 304) {
#pragma unroll
      for (int i = 0; i < 3; ++i) { const int a5 = (dj0 + i) * 4 + (lane >> 4), f = (a5 * 52429) >> 18, c = (a5 - 5 * f) * 16 + (lane & 15), cg = (c * 6554) >> 16, cc = c - cg * 10;
        bst128<AUX_SC1>(ws, W_WD + (cg * FF + ((f & ~12) | ((f & 4) << 1) | ((f & 8) >> 1))) * 160 + cc * 16, rd[i]); } }
  };
  if (w == 0) wait_flags<2>(p.ws, W_FKV, epoch, lane);
  if (tid < 7) *(LAS unsigned*)(lds + L2_FLAG + tid * 16) = 0u;
  LBAR();
  STAMPX(4);
  float kmx = __uint_as_float(bld32<AUX_SC1>(ws, W_KMAX + lane * 4));
  stage_first();
  float ref;
  {
    float n2 = 0.f;
#pragma unroll
    for (int j = 0; j < 4; ++j) n2 = dot2(q1[j], q1[j], n2);
    n2 = dot2(q2[0], q2[0], n2); n2 = dot2(q2[1], q2[1], n2);
    n2 = swap32_sum(n2);
    kmx = max64(kmx);
    ref = sqrtf(n2 * kmx) * 1.001f;
  }
  const int rv = r32 < 21 ? r32 : 21;
  const unsigned oK1 = l16, oK2 = KV_K2 + r32 * 8, oV = KV_V + h * KV_VS + rv * 16;
  f32x16 o;
  for (int attempt = 0;; ++attempt) {
    o = f32x16{};
    f32x16 cb;
#pragma unroll
    for (int j = 0; j < 16; ++j) cb[j] = -ref;
    PIN(cb);
    WAITV(0);
    LBAR();
    STAMPX(9);
    lptr aK1 = lds + oK1; lptr aK2 = lds + oK2; lptr aV = lds + oV;
    u32x4 k1 = lds128(aK1); u32x2 k2 = lds64(aK2);
    f32x16 sc = MFMA8(asbf4(k2), asbf4(q2), cb); sc = MFMA16(asbf8(k1), asbf8(q1), sc);
    k1 = lds128(aK1 + KV_TILE); k2 = lds64(aK2 + KV_TILE);
    u32x4 v0 = lds128(aV), v1 = {}, p0, p1 = {};
    u32x4 k1b, v0b, v1b; u32x2 k2b;
    f32x16 sn;
#define EXP2(a) __builtin_amdgcn_exp2f(a)
#define GRP(MF, SC, i, P, j) do { MF; SBAR(); { const float e0 = EXP2(SC[i]), e1 = EXP2(SC[i + 1]), e2 = EXP2(SC[i + 2]), e3 = EXP2(SC[i + 3]); P[j] = pk2(e0, e1); P[j + 1] = pk2(e2, e3); } PIN(P); SBAR(); } while (0)
#define TILE(SC, SN, K1, K2, V0, V1, K1N, K2N, V0N, V1N, PK1, PK2, PV0, PV1) do { \
      WAITL(); \
      K2N = lds64(PK2); K1N = lds128(PK1); V0N = lds128(PV0); V1N = lds128(PV1); SBAR(); \
      GRP(SN = MFMA8(asbf4(K2), asbf4(q2), cb), SC, 0, p0, 0); \
      GRP(o = MFMA16(asbf8(V1), asbf8(p1), o), SC, 4, p0, 2); \
      GRP(SN = MFMA16(asbf8(K1), asbf8(q1), SN), SC, 8, p1, 0); \
      GRP(o = MFMA16(asbf8(V0), asbf8(p0), o), SC, 12, p1, 2); } while (0)
#define TA(T) TILE(sc, sn, k1, k2, v0, v1, k1b, k2b, v0b, v1b, aK1 + ((T) + 2) * KV_TILE, aK2 + ((T) + 2) * KV_TILE, aV + ((T) + 1) * KV_TILE, aV + (T) * KV_TILE + 2 * KV_VS)
#define TB(T) TILE(sn, sc, k1b, k2b, v0b, v1b, k1, k2, v0, v1, aK1 + ((T) + 2) * KV_TILE, aK2 + ((T) + 2) * KV_TILE, aV + ((T) + 1) * KV_TILE, aV + (T) * KV_TILE + 2 * KV_VS)
#define ADV() do { aK1 += 8 * KV_TILE; aK2 += 8 * KV_TILE; aV += 8 * KV_TILE; } while (0)
#define BODY8() do { TA(0); TB(1); TA(2); TB(3); TA(4); TB(5); TA(6); TB(7); ADV(); } while (0)
    if (th == 1) {
      __builtin_amdgcn_s_setprio(2);
      dma3(3); SBAR(); TA(0); dma3(4); SBAR(); TB(1); dma3(5); SBAR(); TA(2); dma3(6); SBAR(); TB(3);
      dma3(7); SBAR(); TA(4); dma3(8); SBAR(); TB(5); dma3(9); SBAR(); TA(6); dma3(10); SBAR(); TB(7); ADV();
      if (lane == 0) lds_add1(lds + L2_CP);
      WAITV(6); if (lane == 0) lds_add1(lds + L2_CS);
      lds_wait_ge(lds + L2_CS, 4u);
      dma3(11); SBAR(); TA(0); dma3(12); SBAR(); TB(1); TA(2); TB(3); TA(4); TB(5); TA(6); TB(7); ADV();
      WAITV(0); if (lane == 0) lds_add1(lds + L2_CS + 16);
#pragma nounroll
      for (int m = 2; m < 7; ++m) {
        if (m == 3) { if (attempt == 0) lds_wait_ge(lds + L2_CR, 4u); dma3(13); dma3(14); }
        if (m == 4) { WAITV(0); if (lane == 0) lds_add1(lds + L2_CS + 32); lds_wait_ge(lds + L2_CS + 16, 4u); }
        if (m == 5) { lds_wait_ge(lds + L2_CP, 8u); dma3(15); }
        if (m == 6) { WAITV(0); if (lane == 0) lds_add1(lds + L2_CS + 48); lds_wait_ge(lds + L2_CS + 32, 4u); }
        if (m == PRIO_M) __builtin_amdgcn_s_setprio(0);
        BODY8();
      }
    } else {
      __builtin_amdgcn_s_setprio(1);
      TA(0); TB(1); TA(2); TB(3);
      if (attempt == 0) retile();
      TA(4); TB(5); TA(6); TB(7); ADV();
      if (attempt == 0) {
        WAITV(0);
        unsigned old = 0; if (lane == 0) old = __hip_atomic_fetch_add((LAS unsigned*)(lds + L2_CR), 1u, __ATOMIC_RELAXED, __HIP_MEMORY_SCOPE_WORKGROUP);
        if (lane == 0 && old == 3u) __hip_atomic_store((GAS unsigned*)(p.ws + W_FRT) + b, epoch, RLX_AGENT);
      }
#pragma nounroll
      for (int m = 1; m < 7; ++m) {
        if (m == 1) { if (lane == 0) lds_add1(lds + L2_CP); lds_wait_ge(lds + L2_CS, 4u); }
        if (m == 3 && attempt == 0) {
          wait_flags<4>(p.ws, W_FRT, epoch, lane);
          if (b == 0 && w == 0 && lane == 0) __hip_atomic_store((GAS unsigned*)(p.ws + W_EPOCH), epoch, RLX_AGENT);
          const int ngl = rk & 3, u0 = 27 * (xcd & 3) + 7 * ngl, part = (rk >> 2) * 4 + hh;
          const char* src = p.ws + W_WGU + (size_t)u0 * 40960;
          const int nl = (ngl == 3 ? 6 : 7) * 10;
          touch_sc1((part * nl + lane) * 128, src, lds0 + L2_DUMP);
          if (lane < nl - 64) touch_sc1((part * nl + 64 + lane) * 128, src, lds0 + L2_DUMP);
          const int ln = ((rk >> 3) * 4 + hh) * 80 + lane; const char* wsrc = p.wo + ((hg * 160) & ~127);
          touch((ln >> 1) * 1280 + (ln & 1) * 128, wsrc, lds0 + L2_DUMP);
          if (lane < 16) touch(((ln + 64) >> 1) * 1280 + (ln & 1) * 128, wsrc, lds0 + L2_DUMP);
        }
        if (m == 4) lds_wait_ge(lds + L2_CS + 16, 4u);
        if (m == 6) lds_wait_ge(lds + L2_CS + 32, 4u);
        BODY8();
      }
    }
    lds_wait_ge(lds + L2_CS + 48, 4u);
    {
      lptr zK1 = lds + oK1; lptr zK2 = lds + oK2; lptr zV = lds + oV;
      TA(0); TB(1);
      TILE(sc, sn, k1, k2, v0, v1, k1b, k2b, v0b, v1b, zK1, zK2, aV + 3 * KV_TILE, aV + 2 * KV_TILE + 2 * KV_VS);
      TILE(sn, sc, k1b, k2b, v0b, v1b, k1, k2, v0, v1, zK1 + KV_TILE, zK2 + KV_TILE, zV, aV + 3 * KV_TILE + 2 * KV_VS);
      aK1 = zK1; aK2 = zK2; aV = zV;
      TA(0); TB(1); TA(2); TB(3);
    }
    WAITL();
    o = MFMA16(asbf8(v1), asbf8(p1), o);
    __builtin_amdgcn_s_setprio(0);
    STAMPX(10);
    const float lsum = __uint_as_float(__builtin_amdgcn_permlane32_swap(__float_as_uint(o[8]), __float_as_uint(o[8]), false, false)[1]);
    if (attempt == 1) { LBAR(); break; }
    const bool bad = !(lsum >= 5.421010862427522e-20f);
    if (__any(bad)) *(LAS unsigned*)(lds + L2_FLAG) = 1u;
    LBAR();
    if (*(LAS unsigned*)(lds + L2_FLAG) == 0u) break;
    {
      float mx = -3.0e38f;
      for (int t = 0; t < 64; ++t) {
        const unsigned base = W_KV + t * KV_TILE;
        const u32x4 a1 = bld128<AUX_SC1>(ws, base + l16);
        const u32x2 a2 = bld64<AUX_SC1>(ws, base + KV_K2 + r32 * 8);
        f32x16 s = MFMA8(asbf4(a2), asbf4(q2), f32x16{}); s = MFMA16(asbf8(a1), asbf8(q1), s);
#pragma unroll
        for (int j = 0; j < 16; ++j) mx = fmaxf(mx, s[j]);
      }
      ref = swap32_max(mx);
      if (tid < 5) *(LAS unsigned*)(lds + L2_CP + tid * 16) = 0u;
      stage_first();
    }
  }
  {
    const float lsum = __uint_as_float(__builtin_amdgcn_permlane32_swap(__float_as_uint(o[8]), __float_as_uint(o[8]), false, false)[1]);
    const float inv = 1.0f / lsum;
    const u32x4 oa = {pk2(o[0] * inv, o[1] * inv), pk2(o[2] * inv, o[3] * inv), pk2(o[4] * inv, o[5] * inv), pk2(o[6] * inv, o[7] * inv)};
    const u32x2 ob = {pk2(o[8] * inv, o[9] * inv), pk2(o[10] * inv, o[11] * inv)};
    const unsigned tile16 = tg * 4 + th * 2 + (r32 >> 4), g0 = 10 * hg + 5 * (hh >> 1);
    bst128<AUX_SC1>(ws, W_OF + ((tile16 * 80 + g0 + 2 * (hh & 1) + h) * 16 + (r32 & 15)) * 16, oa);
    if (h == 0) bst64<AUX_SC1>(ws, W_OF + ((tile16 * 80 + g0 + 4) * 16 + (r32 & 15)) * 16 + 8 * (hh & 1), ob);
  }
  {
    const unsigned vo = (lane / 10) * 1280 + (lane % 10) * 16; const char* src = p.wo + hg * 160;
#pragma unroll
    for (int i = 0; i < 14; ++i) { int n = w + 8 * i; if (n > 106) n = 105;
      if (n == 106) { if (lane < 40) glds(vo, src + (size_t)n * 7680, rfl(lds0 + L3_WO + n * 960)); } else glds(vo, src + (size_t)n * 7680, rfl(lds0 + L3_WO + n * 960)); }
  }
  WAITV(14);
  LBAR();
  if (tid == 0) (void)__hip_atomic_fetch_add((GAS unsigned*)(p.ws + W_CNTO + tg * 256), 1u, RLX_AGENT);
}

DI void phase_oproj(const Params& p, lptr lds, rsrc_t ws, int b, u32x2 (&hres)[5], unsigned epoch) {
  const int tid = threadIdx.x, lane = tid & 63, w = rfl(tid >> 6);
  const int xcd = b & 7, rk = b >> 3, tg = 4 * xcd + (rk >> 3), cg = rk & 7;
  const int mt = w & 3, kh = w >> 2, t16 = lane & 15, g = lane >> 4, tq = t16 >> 2, tp = t16 & 3;
  const int tok = tg * 64 + mt * 16 + t16;
  u32x2 xr[5] = {};
  if (kh == 0) {
#pragma unroll
    for (int nt = 0; nt < 5; ++nt) xr[nt] = *(const GAS u32x2*)(p.x + (size_t)tok * 1280 + cg * 160 + nt * 32 + g * 8);
  }
  unsigned r1[10], r2[10];
#pragma unroll
  for (int i = 0; i < 10; ++i) {
    const int t = 4 * i + g, a5 = (t * 13) >> 6, m = t - 5 * a5, pair = 8 * kh + a5;
    const int last = m >> 2, ra = pair * 40 + (m >> 1) * 20 + (m & 1) * 4 - last * 24;
    r1[i] = L3_WO + (ra + tq) * 160 + tp * 8; r2[i] = r1[i] + (8 + 12 * last) * 160;
  }
  WAITV(0);
  wait_cnt2(p.ws, W_CNTO + tg * 256, W_CNTO + tg * 256, 8u, epoch);
  STAMPX(13);
  bf16x8 bf[2][5];
  u32x4 a[10];
#pragma unroll
  for (int i = 0; i < 10; ++i) a[i] = bld128<AUX_SC1>(ws, W_OF + ((tg * 4 + mt) * 80 + 4 * (10 * kh + i)) * 256 + lane * 16);
#pragma unroll
  for (int nt = 0; nt < 5; ++nt) bf[0][nt] = cat8(trread(lds + r1[0] + nt * 32), trread(lds + r2[0] + nt * 32));
  f32x4 acc[5] = {};
#pragma unroll
  for (int i = 0; i < 10; ++i) {
    if (i + 1 < 10) {
#pragma unroll
      for (int nt = 0; nt < 5; ++nt) bf[(i + 1) & 1][nt] = cat8(trread(lds + r1[i + 1] + nt * 32), trread(lds + r2[i + 1] + nt * 32));
    }
    SBAR();
#pragma unroll
    for (int nt = 0; nt < 5; ++nt) acc[nt] = MFMAS(bf[i & 1][nt], asbf8(a[i]), acc[nt]);
    SBAR();
  }
  STAMPX(14);
  LAS f32x4* red = (LAS f32x4*)(lds + L3_RED) + mt * 320 + lane;
  if (kh == 1) {
#pragma unroll
    for (int nt = 0; nt < 5; ++nt) red[nt * 64] = acc[nt];
  }
  LBAR();
  if (kh == 0) {
    float s1 = 0.f, s2 = 0.f;
#pragma unroll
    for (int nt = 0; nt < 5; ++nt) {
      const f32x4 o = red[nt * 64];
      const float h0 = bflo(xr[nt][0]) + acc[nt][0] + o[0], h1 = bfhi(xr[nt][0]) + acc[nt][1] + o[1], h2 = bflo(xr[nt][1]) + acc[nt][2] + o[2], h3 = bfhi(xr[nt][1]) + acc[nt][3] + o[3];
      const u32x2 hv = {pk2(h0, h1), pk2(h2, h3)};
      hres[nt] = hv;
      s1 = dot2(hv[0], ONES2, s1); s1 = dot2(hv[1], ONES2, s1); s2 = dot2(hv[0], hv[0], s2); s2 = dot2(hv[1], hv[1], s2);
      const int gam = 10 * cg + 2 * nt + (g >> 1);
      bst64<AUX_SC1>(ws, W_HF + (((tg * 4 + mt) * 20 + (gam >> 2)) * 64 + (gam & 3) * 16 + t16) * 16 + (g & 1) * 8, hv);
    }
    s1 = swap32_sum(swap16_sum(s1)); s2 = swap32_sum(swap16_sum(s2));
    if (g == 0) bst64<AUX_SC1>(ws, W_HST + ((tg * 8 + cg) * 64 + mt * 16 + t16) * 8, u32x2{__float_as_uint(s1), __float_as_uint(s2)});
  }
  signal_cnt(p.ws, W_CNTH + tg * 256);
}

constexpr unsigned L4_S = 0  , L4_ST = 122880  , L4_DUMP = 163584;
DI constexpr unsigned l5_slot(int c) { return (c & 3) == 0 ? 123904u : ((c & 3) == 1 ? 40960u : ((c & 3) == 2 ? 71680u : 0u)); }
constexpr unsigned L5_RED = 102400;
static_assert(123904 + 30720 <= L4_DUMP && L4_DUMP + 256 <= 163840, "lds");
DI void dma_wd_chunk(const char* wd, unsigned lds0, int w, unsigned l16, int c) {
#pragma unroll
  for (int i = 0; i < 4; ++i) { int n = w + 8 * i; if (n >= 30) n -= 8; const int k2 = n >= 15, j = n - 15 * k2;
    glds_sc1(l16, wd + (size_t)(864 * k2 + 96 * c) * 160 + j * 1024, rfl(lds0 + l5_slot(c) + n * 1024)); }
}
constexpr int GWIN = 5;
DI void phase_gateup(const Params& p, lptr lds, rsrc_t ws, int b, unsigned epoch) {
  const int tid = threadIdx.x, lane = tid & 63, w = rfl(tid >> 6);
  const int xcd = b & 7, rk = b >> 3, mb = 8 * (xcd >> 2) + (rk >> 2), ngl = rk & 3, u0 = 27 * (xcd & 3) + 7 * ngl, nu = ngl == 3 ? 6 : 7;
  const int t16 = lane & 15, g = lane >> 4;
  const unsigned lds0 = (unsigned)(uintptr_t)lds, l16 = lane * 16;
  const char* wd = p.ws + W_WD + (size_t)(rk & 7) * FF * 160;
  const bool act = w < nu;
  const int u = u0 + (act ? w : 0);
  const unsigned wbase = W_WGU + u * 40960 + l16;
  u32x4 ag[GWIN], au[GWIN];
  if (act) {
#pragma unroll
    for (int i = 0; i < GWIN; ++i) { ag[i] = bld128<AUX_SC1>(ws, wbase + i * 2048); au[i] = bld128<AUX_SC1>(ws, wbase + i * 2048 + 1024); }
  } else if (w == 7) {
#pragma unroll
    for (int i = 0; i < 9; ++i) { const int ln = i * 64 + lane; touch_sc1((ln < 540 ? ln : 539) * 128, wd + (rk >> 3) * 69120, rfl(lds0 + L4_DUMP)); }
  }
  dma_wd_chunk(wd, lds0, w, l16, 0);
  SBAR();
  wait_cnt2(p.ws, W_CNTH + (2 * mb) * 256, W_CNTH + (2 * mb + 1) * 256, 8u, epoch);
  STAMPX(16);
  u32x2 sv[8];
  if (tid < 128) {
#pragma unroll
    for (int c = 0; c < 8; ++c) sv[c] = bld64<AUX_SC1>(ws, W_HST + (((2 * mb + (tid >> 6)) * 8 + c) * 64 + (tid & 63)) * 8);
  }
  u32x4 st[4][5];
#pragma unroll
  for (int j = 0; j < 4; ++j)
#pragma unroll
    for (int i = 0; i < 5; ++i) st[j][i] = bld128<AUX_SC1>(ws, W_HF + ((mb * 8 + w) * 20 + 5 * j + i) * 1024 + l16);
  SBAR();
  if (tid < 128) {
    float s1 = 0.f, s2 = 0.f;
#pragma unroll
    for (int c = 0; c < 8; ++c) { s1 += __uint_as_float(sv[c][0]); s2 += __uint_as_float(sv[c][1]); }
    const float mean = s1 * (1.f / DM), rstd = rsqrtf(fmaxf(s2 * (1.f / DM) - mean * mean, 0.f) + LN_EPS);
    *(LAS f32x2*)(lds + L4_ST + tid * 8) = f32x2{mean, rstd};
  }
  lptr hw = lds + L4_S + w * 1024 + l16;
#pragma unroll
  for (int i = 0; i < 5; ++i) *(LAS u32x4*)(hw + i * 8192) = st[0][i];
  LBAR();
  STAMPX(17);
#pragma unroll
  for (int j = 1; j < 3; ++j)
#pragma unroll
    for (int i = 0; i < 5; ++i) *(LAS u32x4*)(hw + j * 40960 + i * 8192) = st[j][i];
  if (act) {
    f32x4 aG[8] = {}, aU[8] = {}; float csg[4] = {}, csu[4] = {};
    lptr hb = lds + L4_S + l16;
    u32x4 bb[2][4];
#pragma unroll
    for (int t = 0; t < 4; ++t) bb[0][t] = lds128(hb + t * 1024);
#pragma unroll
    for (int s = 0; s < 20; ++s) {
      const int slot = (s / 5) % 3, nslot = ((s + 1) / 5) % 3;
      if (s == 4) { asm volatile("" ::: "memory"); WAITL(); asm volatile("" ::: "memory"); BAR(); }
      if (s == 5) { BAR();
#pragma unroll
        for (int i = 0; i < 5; ++i) *(LAS u32x4*)(hw + i * 8192) = st[3][i]; }
      if (s == 14) BAR();
      if (s == 15) { BAR(); dma_wd_chunk(wd, lds0, w, l16, 1); dma_wd_chunk(wd, lds0, w, l16, 2); }
      const u32x4 cg_ = ag[s % GWIN], cu_ = au[s % GWIN];
      if (s + GWIN < 20) { ag[s % GWIN] = bld128<AUX_SC1>(ws, wbase + (s + GWIN) * 2048); au[s % GWIN] = bld128<AUX_SC1>(ws, wbase + (s + GWIN) * 2048 + 1024); }
#pragma unroll
      for (int t = 0; t < 4; ++t) bb[1][t] = lds128(hb + slot * 40960 + (s % 5) * 8192 + (4 + t) * 1024);
      SBAR();
#pragma unroll
      for (int t = 0; t < 4; ++t) { aG[t] = MFMAS(asbf8(cg_), asbf8(bb[0][t]), aG[t]); aU[t] = MFMAS(asbf8(cu_), asbf8(bb[0][t]), aU[t]); csg[t] = dot2(cg_[t], ONES2, csg[t]); }
      SBAR();
      if (s + 1 < 20) {
#pragma unroll
        for (int t = 0; t < 4; ++t) bb[0][t] = lds128(hb + nslot * 40960 + ((s + 1) % 5) * 8192 + t * 1024);
      }
      SBAR();
#pragma unroll
      for (int t = 0; t < 4; ++t) { aG[4 + t] = MFMAS(asbf8(cg_), asbf8(bb[1][t]), aG[4 + t]); aU[4 + t] = MFMAS(asbf8(cu_), asbf8(bb[1][t]), aU[4 + t]); csu[t] = dot2(cu_[t], ONES2, csu[t]); }
      SBAR();
    }
    STAMPX(18);
    const float sg = swap32_sum(swap16_sum((csg[0] + csg[1]) + (csg[2] + csg[3]))), su = swap32_sum(swap16_sum((csu[0] + csu[1]) + (csu[2] + csu[3])));
    float cg4[4], cu4[4];
#pragma unroll
    for (int j = 0; j < 4; ++j) { cg4[j] = __shfl(sg, 4 * g + j); cu4[j] = __shfl(su, 4 * g + j); }
#pragma unroll
    for (int i = 0; i < 4; ++i) {
      u32x2 pk[2];
#pragma unroll
      for (int e = 0; e < 2; ++e) {
        const int t = 2 * i + e;
        const f32x2 mr = *(LAS f32x2*)(lds + L4_ST + (t * 16 + t16) * 8);
        const float r2 = mr[1] * mr[1];
        float hv[4];
#pragma unroll
        for (int j = 0; j < 4; ++j) hv[j] = (aG[t][j] - mr[0] * cg4[j]) * (aU[t][j] - mr[0] * cu4[j]) * r2;
        pk[e] = u32x2{pk2(hv[0], hv[1]), pk2(hv[2], hv[3])};
      }
      const auto s0 = __builtin_amdgcn_permlane16_swap(pk[0][0], pk[1][0], false, false), s1 = __builtin_amdgcn_permlane16_swap(pk[0][1], pk[1][1], false, false);
      bst128<AUX_SC1>(ws, W_GF + (((mb * 8 + 2 * i + (g & 1)) * 54 + (u >> 1)) * 64 + (2 * (u & 1) + (g >> 1)) * 16 + t16) * 16, u32x4{s0[0], s1[0], s0[1], s1[1]});
    }
  } else {
    asm volatile("" ::: "memory"); WAITL(); asm volatile("" ::: "memory"); BAR();
    BAR();
#pragma unroll
    for (int i = 0; i < 5; ++i) *(LAS u32x4*)(hw + i * 8192) = st[3][i];
    asm volatile("" ::: "memory"); WAITL(); asm volatile("" ::: "memory");
    BAR();
    BAR(); dma_wd_chunk(wd, lds0, w, l16, 1); dma_wd_chunk(wd, lds0, w, l16, 2);
  }
  STAMPX(21);
  signal_cnt(p.ws, W_CNTG + mb * 256);
}

DI void gld128_sc1(u32x4& d, unsigned voff, const char* sbase) { asm volatile("global_load_dwordx4 %0, %1, %2 sc1" : "=v"(d) : "v"(voff), "s"(sbase) : "memory"); }
DI void phase_down(const Params& p, lptr lds, rsrc_t ws, int b, const u32x2 (&hres)[5], unsigned epoch) {
  const int tid = threadIdx.x, lane = tid & 63, w = rfl(tid >> 6);
  const int xcd = b & 7, rk = b >> 3, tg = 4 * xcd + (rk >> 3), cg = rk & 7;
  const int mt = w & 3, kh = w >> 2, t16 = lane & 15, g = lane >> 4, tq = t16 >> 2, tp = t16 & 3;
  const unsigned lds0 = (unsigned)(uintptr_t)lds, l16 = lane * 16;
  const char* wd = p.ws + W_WD + (size_t)cg * FF * 160;
  wait_cnt2(p.ws, W_CNTG + (tg >> 1) * 256, W_CNTG + (tg >> 1) * 256, 16u, epoch);
  STAMPX(20);
  const char* abase = p.ws + W_GF + (size_t)((tg * 4 + mt) * 54 + 27 * kh) * 1024;
  u32x4 a[4][3];
#pragma unroll
  for (int c = 0; c < 3; ++c)
#pragma unroll
    for (int k = 0; k < 3; ++k) gld128_sc1(a[c][k], l16, abase + (3 * c + k) * 1024);
  f32x4 acc[5] = {};
  const unsigned brow = kh * 15360 + (16 * (g >> 1) + 4 * (g & 1) + tq) * 160 + tp * 8;
#define DOWN_WAIT(N, c) asm volatile("s_waitcnt vmcnt(" #N ")\n\ts_barrier" : "+v"(a[(c) & 3][0]), "+v"(a[(c) & 3][1]), "+v"(a[(c) & 3][2]) :: "memory")
  bf16x8 bf[2][5];
  { lptr bb = lds + l5_slot(0) + brow;
#pragma unroll
    for (int nt = 0; nt < 5; ++nt) bf[0][nt] = cat8(trread(bb + nt * 32), trread(bb + nt * 32 + 1280)); }
#pragma unroll
  for (int c = 0; c < 9; ++c) {
    asm volatile("" ::: "memory"); WAITL(); asm volatile("" ::: "memory");
    if (c == 0) DOWN_WAIT(6, c); else if (c == 1) DOWN_WAIT(10, c); else if (c <= 5) DOWN_WAIT(10, c); else if (c == 6) DOWN_WAIT(10, c); else if (c == 7) DOWN_WAIT(3, c); else DOWN_WAIT(0, c);
    if (c + 3 < 9) { dma_wd_chunk(wd, lds0, w, l16, c + 3);
#pragma unroll
      for (int k = 0; k < 3; ++k) gld128_sc1(a[(c + 3) & 3][k], l16, abase + (3 * (c + 3) + k) * 1024); }
#pragma unroll
    for (int k = 0; k < 3; ++k) {
      const int sn = 3 * c + k + 1;
      if (sn < 27) { lptr bb = lds + l5_slot(sn / 3) + brow + (sn % 3) * 5120;
#pragma unroll
        for (int nt = 0; nt < 5; ++nt) bf[sn & 1][nt] = cat8(trread(bb + nt * 32), trread(bb + nt * 32 + 1280)); }
      SBAR();
#pragma unroll
      for (int nt = 0; nt < 5; ++nt) acc[nt] = MFMAS(bf[(3 * c + k) & 1][nt], asbf8(a[c & 3][k]), acc[nt]);
      SBAR();
    }
  }
  STAMPX(22);
  LAS f32x4* red = (LAS f32x4*)(lds + L5_RED) + mt * 320 + lane;
  if (kh == 1) {
#pragma unroll
    for (int nt = 0; nt < 5; ++nt) red[nt * 64] = acc[nt];
  }
  LBAR();
  if (kh == 0) {
    const int tok = tg * 64 + mt * 16 + t16;
#pragma unroll
    for (int nt = 0; nt < 5; ++nt) {
      const f32x4 o = red[nt * 64];
      const u32x2 r = {pk2(bflo(hres[nt][0]) + acc[nt][0] + o[0], bfhi(hres[nt][0]) + acc[nt][1] + o[1]), pk2(bflo(hres[nt][1]) + acc[nt][2] + o[2], bfhi(hres[nt][1]) + acc[nt][3] + o[3])};
      *(GAS u32x2*)(p.out + (size_t)tok * 1280 + cg * 160 + nt * 32 + g * 8) = r;
    }
  }
}

__global__ void __launch_bounds__(NTHR, 2) k_block(Params p) {
  extern __shared__ __attribute__((aligned(16))) char smem[];
  lptr lds = (lptr)smem;
  const rsrc_t ws = __builtin_amdgcn_make_buffer_rsrc((void*)p.ws, 0, (int)W_END, 0x00020000);
  const int b = blockIdx.x;
  u32x4 q1; u32x2 q2; unsigned epoch;
  u32x2 hres[5] = {};
  {
    unsigned long long pc; asm volatile("s_getpc_b64 %0" : "=s"(pc));
    if (threadIdx.x >= 448) { const int ln = (b >> 3) * 16 + (threadIdx.x & 15); if ((threadIdx.x & 63) < 16 && ln * 128 < CODEPF)
      touch(ln * 128, (const char*)(pc & ~127ull), (unsigned)(uintptr_t)smem + 163584); }
  }
  if (threadIdx.x >= 384 && threadIdx.x < 448) {
    const int l = threadIdx.x & 63; const unsigned dump = (unsigned)(uintptr_t)smem + 163584;
    if (l * (TLBPF) < (int)W_END) touch(l * (TLBPF), p.ws, dump);
    if (l < 2) { const unsigned o = l * 2000000u; touch(o, p.wg, dump); touch(o, p.wu, dump); touch(o, p.wd, dump); touch(o, p.out, dump); }
    if (l == 0) touch(0, p.wo, dump);
  }
  STAMPX(0);
  phase1(p, lds, ws, b, q1, q2, epoch);
  STAMPX(5);
  phase_attn(p, lds, ws, b, q1, q2, epoch);
  STAMPX(12);
  phase_oproj(p, lds, ws, b, hres, epoch);
  STAMPX(15);
  phase_gateup(p, lds, ws, b, epoch);
  STAMPX(19);
  phase_down(p, lds, ws, b, hres, epoch);
  STAMPX(23);
}

extern "C" void kernel_launch(void* const* d_in, const int* in_sizes, int n_in, void* d_out, int out_size, void* d_ws, size_t ws_size, hipStream_t stream) {
  Params p;
  p.x = (const char*)d_in[0]; p.wq = (const char*)d_in[1]; p.wk = (const char*)d_in[2]; p.wv = (const char*)d_in[3]; p.wo = (const char*)d_in[4];
  p.wg = (const char*)d_in[6]; p.wu = (const char*)d_in[7]; p.wd = (const char*)d_in[8];
  p.out = (char*)d_out; p.ws = (char*)d_ws;
  constexpr int LDS_BYTES = 163840;
  (void)hipFuncSetAttribute((const void*)k_block, hipFuncAttributeMaxDynamicSharedMemorySize, LDS_BYTES);
  k_block<<<NWG, NTHR, LDS_BYTES, stream>>>(p);
}
```

```cpp
#include <hip/hip_runtime.h>
#include <stdint.h>

#define DI __device__ __forceinline__
#define LAS __attribute__((address_space(3)))
#define GAS __attribute__((address_space(1)))
typedef short bf16x8 __attribute__((ext_vector_type(8)));
typedef short bf16x4 __attribute__((ext_vector_type(4)));
typedef __bf16 bfv2 __attribute__((ext_vector_type(2)));
typedef float f32x2 __attribute__((ext_vector_type(2)));
typedef float f32x4 __attribute__((ext_vector_type(4)));
typedef float f32x16 __attribute__((ext_vector_type(16)));
typedef unsigned u32x2 __attribute__((ext_vector_type(2)));
typedef unsigned u32x4 __attribute__((ext_vector_type(4)));
typedef LAS char* lptr;

constexpr int SEQ = 2048, DM = 640, FF = 1728;
constexpr int NWG = 256, NTHR = 512;
constexpr float LN_EPS = 1e-5f;
constexpr float QSCALE = 0.22360679774997896f * 1.4426950408889634f;
constexpr unsigned KV_TILE = 2688, KV_K2 = 1024, KV_V = 1280, KV_VS = 352;

constexpr unsigned W_EPOCH = 0, W_TMO = 256;
constexpr unsigned W_FKV = 1024  , W_FRT = 2048  ;
constexpr unsigned W_CNTO = 8192, W_CNTH = 16384, W_CNTG = 24576;
constexpr unsigned W_KV = 65536;
constexpr unsigned W_OF = W_KV + 64 * KV_TILE;
constexpr unsigned W_HF = W_OF + SEQ * DM * 2;
constexpr unsigned W_HST = W_HF + SEQ * DM * 2;
constexpr unsigned W_GF = W_HST + 32 * 8 * 64 * 8;
constexpr unsigned W_WGU = W_GF + 128 * 54 * 1024;
constexpr unsigned W_WD = W_WGU + 108 * 40 * 1024;
constexpr unsigned W_WO = W_WD + 8 * FF * 160;
constexpr unsigned W_CSP = W_WO + 8 * DM * 160;
constexpr unsigned W_END = W_CSP + 108 * 2 * 20 * 64;

#define STAMPW() do { } while (0)
#define STAMP(i) do { } while (0)
#define STW 0
#define STAMPX(i) do { STAMP(i); if (STW == (i)) STAMPW(); } while (0)

struct Params {
  const char* x; const char* wq; const char* wk; const char* wv; const char* wo;
  const char* wg; const char* wu; const char* wd;
  char* out; char* ws;
};

typedef const __attribute__((address_space(4))) Params* kargs_t;
DI kargs_t kargs() { kargs_t a = (kargs_t)__builtin_amdgcn_kernarg_segment_ptr(); asm volatile("" : "+s"(a)); return a; }
constexpr int AUX_SC1 = 16;
#define MFMA16(a, b, c) __builtin_amdgcn_mfma_f32_32x32x16_bf16((a), (b), (c), 0, 0, 0)
#define MFMA8(a, b, c) __builtin_amdgcn_mfma_f32_32x32x8bf16_1k((a), (b), (c), 0, 0, 0)
#define MFMAS(a, b, c) __builtin_amdgcn_mfma_f32_16x16x32_bf16((a), (b), (c), 0, 0, 0)
#define SBAR() __builtin_amdgcn_sched_barrier(0)
#define WAITV(n) asm volatile("s_waitcnt vmcnt(" #n ")" ::: "memory")
#define BAR() asm volatile("s_barrier" ::: "memory")
#define WAITL() __builtin_amdgcn_s_waitcnt(0xC07F)
#define LBAR() do { asm volatile("" ::: "memory"); WAITL(); BAR(); } while (0)
#define PIN(x) asm volatile("" : "+v"(x))

DI unsigned pk2(float lo, float hi) { f32x2 v = {lo, hi}; return __builtin_bit_cast(unsigned, __builtin_convertvector(v, bfv2)); }
DI float bflo(unsigned v) { return __uint_as_float(v << 16); }
DI float bfhi(unsigned v) { return __uint_as_float(v & 0xffff0000u); }
DI float dot2(unsigned a, unsigned b, float c) { return __builtin_amdgcn_fdot2_f32_bf16(__builtin_bit_cast(bfv2, a), __builtin_bit_cast(bfv2, b), c, false); }
constexpr unsigned ONES2 = 0x3f803f80u;
DI float swap32_sum(float v) { auto r = __builtin_amdgcn_permlane32_swap(__float_as_uint(v), __float_as_uint(v), false, false); return __uint_as_float(r[0]) + __uint_as_float(r[1]); }
DI float swap32_max(float v) { auto r = __builtin_amdgcn_permlane32_swap(__float_as_uint(v), __float_as_uint(v), false, false); return fmaxf(__uint_as_float(r[0]), __uint_as_float(r[1])); }
DI float swap16_sum(float v) { auto r = __builtin_amdgcn_permlane16_swap(__float_as_uint(v), __float_as_uint(v), false, false); return __uint_as_float(r[0]) + __uint_as_float(r[1]); }
template <int CTRL> DI float dppf(float v) { return __uint_as_float(__builtin_amdgcn_update_dpp(0u, __float_as_uint(v), CTRL, 0xf, 0xf, true)); }
DI float sum8(float v) { v += dppf<0xB1>(v); v += dppf<0x4E>(v); return v + dppf<0x141>(v); }
DI float max16(float v) { v = fmaxf(v, dppf<0x128>(v)); v = fmaxf(v, dppf<0x124>(v)); v = fmaxf(v, dppf<0x122>(v)); return fmaxf(v, dppf<0x121>(v)); }
DI float max64(float v) { v = max16(v); auto r = __builtin_amdgcn_permlane16_swap(__float_as_uint(v), __float_as_uint(v), false, false);
  return swap32_max(fmaxf(__uint_as_float(r[0]), __uint_as_float(r[1]))); }
typedef short v4i16_t __attribute__((ext_vector_type(4)));
DI bf16x4 trread(lptr p) { return __builtin_bit_cast(bf16x4, __builtin_amdgcn_ds_read_tr16_b64_v4i16((LAS v4i16_t*)p)); }
DI bf16x8 cat8(bf16x4 a, bf16x4 b) { return __builtin_shufflevector(a, b, 0, 1, 2, 3, 4, 5, 6, 7); }
DI u32x4 lds128(lptr p) { return *(LAS u32x4*)p; }
DI u32x2 lds64(lptr p) { return *(LAS u32x2*)p; }
DI bf16x8 asbf8(u32x4 v) { return __builtin_bit_cast(bf16x8, v); }
DI bf16x4 asbf4(u32x2 v) { return __builtin_bit_cast(bf16x4, v); }
DI u32x4 asu4(bf16x8 v) { return __builtin_bit_cast(u32x4, v); }
DI unsigned rfl(unsigned v) { return __builtin_amdgcn_readfirstlane(v); }
typedef __amdgpu_buffer_rsrc_t rsrc_t;
template <int AUX> DI u32x4 bld128(rsrc_t r, unsigned off) { return __builtin_amdgcn_raw_buffer_load_b128(r, off, 0, AUX); }
template <int AUX> DI u32x2 bld64(rsrc_t r, unsigned off) { return __builtin_amdgcn_raw_buffer_load_b64(r, off, 0, AUX); }
template <int AUX> DI unsigned bld32(rsrc_t r, unsigned off) { return __builtin_amdgcn_raw_buffer_load_b32(r, off, 0, AUX); }
template <int AUX> DI void bst128(rsrc_t r, unsigned off, u32x4 v) { __builtin_amdgcn_raw_buffer_store_b128(v, r, off, 0, AUX); }
template <int AUX> DI void bst64(rsrc_t r, unsigned off, u32x2 v) { __builtin_amdgcn_raw_buffer_store_b64(v, r, off, 0, AUX); }
template <int AUX> DI void bst32(rsrc_t r, unsigned off, unsigned v) { __builtin_amdgcn_raw_buffer_store_b32(v, r, off, 0, AUX); }
DI void glds(unsigned voff, const char* sbase, unsigned lds_base) {
  asm volatile("s_mov_b32 m0, %2\n\ts_nop 0\n\tglobal_load_lds_dwordx4 %0, %1" :: "v"(voff), "s"(sbase), "s"(rfl(lds_base)) : "memory");
}
DI void glds_sc1(unsigned voff, const char* sbase, unsigned lds_base) {
  asm volatile("s_mov_b32 m0, %2\n\ts_nop 0\n\tglobal_load_lds_dwordx4 %0, %1 sc1" :: "v"(voff), "s"(sbase), "s"(rfl(lds_base)) : "memory");
}
DI void touch(unsigned voff, const char* sbase, unsigned lds_dump) {
  asm volatile("s_mov_b32 m0, %2\n\ts_nop 0\n\tglobal_load_lds_dword %0, %1" :: "v"(voff), "s"(sbase), "s"(rfl(lds_dump)) : "memory");
}
DI void touch_sc1(unsigned voff, const char* sbase, unsigned lds_dump) {
  asm volatile("s_mov_b32 m0, %2\n\ts_nop 0\n\tglobal_load_lds_dword %0, %1 sc1" :: "v"(voff), "s"(sbase), "s"(rfl(lds_dump)) : "memory");
}

#define RLX_AGENT __ATOMIC_RELAXED, __HIP_MEMORY_SCOPE_AGENT
constexpr unsigned SPIN_CAP = 1u << 17;
DI bool spin_fail(unsigned& spins, char* wsp, unsigned epoch) {
  __builtin_amdgcn_s_sleep(1);
  GAS unsigned* tmo = (GAS unsigned*)(wsp + W_TMO);
  if (++spins > SPIN_CAP) { __hip_atomic_store(tmo, epoch, RLX_AGENT); return true; }
  return (spins & 255u) == 0u && rfl(__hip_atomic_load(tmo, RLX_AGENT)) == epoch;
}
template <int NW> DI void wait_flags(char* wsp, unsigned off, unsigned epoch, int lane) {
  for (unsigned spins = 0;;) {
    bool ok = true;
#pragma unroll
    for (int i = 0; i < NW; ++i) ok &= __hip_atomic_load((GAS unsigned*)(wsp + off) + i * 64 + lane, RLX_AGENT) == epoch;
    if (__all(ok)) break;
    if (spin_fail(spins, wsp, epoch)) break;
  }
  __builtin_amdgcn_fence(__ATOMIC_ACQUIRE, "wavefront");
}
DI float wait_granules(char* wsp, unsigned off, unsigned epoch, int lane) {
  unsigned mx;
  for (unsigned spins = 0;;) {
    const unsigned long long a = __hip_atomic_load((GAS unsigned long long*)(wsp + off) + lane, RLX_AGENT), b = __hip_atomic_load((GAS unsigned long long*)(wsp + off) + 64 + lane, RLX_AGENT);
    mx = max((unsigned)a, (unsigned)b);
    if (__all((unsigned)(a >> 32) == epoch && (unsigned)(b >> 32) == epoch)) break;
    if (spin_fail(spins, wsp, epoch)) break;
  }
  __builtin_amdgcn_fence(__ATOMIC_ACQUIRE, "wavefront");
  return __uint_as_float(mx);
}
DI void wait_cnt2(char* wsp, unsigned off0, unsigned off1, unsigned want, unsigned epoch) {
  if (threadIdx.x < 64) {
    GAS unsigned* word = (GAS unsigned*)(wsp + ((threadIdx.x & 1) ? off1 : off0));
    for (unsigned spins = 0;;) {
      if (__all(__hip_atomic_load(word, RLX_AGENT) >= want)) break;
      if (spin_fail(spins, wsp, epoch)) break;
    }
    __builtin_amdgcn_fence(__ATOMIC_ACQUIRE, "wavefront");
  }
  LBAR();
}
DI void signal_cnt(char* wsp, unsigned off) {
  WAITV(0);
  LBAR();
  if (threadIdx.x == 0) (void)__hip_atomic_fetch_add((GAS unsigned*)(wsp + off), 1u, RLX_AGENT);
}
DI void lds_add1(lptr p) { (void)__hip_atomic_fetch_add((LAS unsigned*)p, 1u, __ATOMIC_RELAXED, __HIP_MEMORY_SCOPE_WORKGROUP); }
DI void lds_wait_ge(lptr p, unsigned want) {
  while ((unsigned)rfl(__hip_atomic_load((LAS unsigned*)p, __ATOMIC_RELAXED, __HIP_MEMORY_SCOPE_WORKGROUP)) < want) __builtin_amdgcn_s_sleep(2);
  asm volatile("" ::: "memory");
}

DI void dma_kv3(const char* kvg, unsigned lds0, int c, int j, int lane) {
  const unsigned dst = lds0 + (c == 15 ? 0 : c) * 4 * KV_TILE, l16 = lane * 16; const char* src = kvg + c * 4 * KV_TILE;
  glds_sc1(l16, src + j * 1024, dst + j * 1024);
  glds_sc1(l16, src + (j + 4) * 1024, dst + (j + 4) * 1024);
  const int n = j == 3 ? 7 : j + 8;
  if (j != 2 || lane < 32) glds_sc1(l16, src + n * 1024, dst + n * 1024);
}

constexpr unsigned L1_X = 0, L1_S = 81920, L1_WK = L1_S + 4 * 10240, L1_PART = L1_WK + 25600;
constexpr unsigned L1_CS = L1_PART  ;
constexpr unsigned L1_ST = 161792  , L1_KC = L1_ST + 512  , L1_KM = L1_KC + 128, L1_DUMP = L1_KM + 16;
static_assert(L1_PART + 12672 <= 161280 && L1_DUMP + 256 <= 163584, "lds");

DI void phase1(lptr lds, rsrc_t ws, int b, u32x4& q1, u32x2& q2, unsigned& epoch, float& kmax2) {
  kargs_t pa = kargs();
  const int tid = threadIdx.x, lane = tid & 63, w = rfl(tid >> 6);
  const int r32 = lane & 31, h = lane >> 5;
  const int xcd = b & 7, rk = b >> 3, tg = 4 * xcd + (rk >> 3), hg = rk & 7;
  const unsigned lds0 = (unsigned)(uintptr_t)lds, l16 = lane * 16;
  unsigned ep = __hip_atomic_load((GAS unsigned*)(pa->ws + W_EPOCH), RLX_AGENT);
  {
    const char* xb = pa->x + (size_t)(tg * 64 + 8 * w) * 1280;
    const int tl = lane >> 4;
    unsigned v0 = tl * 1280 + ((lane ^ (8 * w + tl)) & 15) * 16, v1 = (4 + tl) * 1280 + ((lane ^ (8 * w + 4 + tl)) & 15) * 16, xd = lds0 + L1_X + 8 * w * 256;
#pragma unroll 1
    for (int a = 0; a < 5; ++a) { glds(v0, xb, xd); glds(v1, xb, xd + 1024); v0 += 256; v1 += 256; xd += 16384; }
  }
  if (hg < 4) {
    const char* W = (hg < 2 ? pa->wk : pa->wv) + w * 1024;
#pragma unroll
    for (int i = 0; i < 3; ++i) glds(l16 + i * 8192, W, lds0 + L1_WK + w * 1024 + i * 8192);
    if (w == 0) glds(l16 + 3 * 8192, W, lds0 + L1_WK + 3 * 8192);
  }
  const char* wqb = pa->wq + hg * 160;
  unsigned vq[3], dq[3];
#pragma unroll
  for (int i = 0; i < 3; ++i) {
    const int n = i < 2 ? w + 8 * i : (w < 6 ? w + 16 : w + 8);
    const int slot = 6 * n + lane / 10, row = (slot & ~7) | ((slot & 1) << 2) | ((slot >> 1) & 3);
    vq[i] = row * 1280 + (lane % 10) * 16; dq[i] = n * 960;
  }
  const bool m3 = w < 5 || (w == 5 ? lane < 20 : lane == 0);
  auto dma_pair = [&](const char* src, unsigned dst) {
    glds(vq[0], src, dst + dq[0]);
    glds(vq[1], src, dst + dq[1]);
    if (m3) glds(vq[2], src, dst + dq[2]);
  };
  SBAR();
  STAMPX(8);
  const int i16 = lane & 15, tq = i16 >> 2, tp = i16 & 3, grp = (lane >> 4) & 1;
  WAITV(0);
  LBAR();
  PIN(ep); epoch = rfl(ep) + 1u;
  STAMPX(1);
  const int kth = hg & 1; const bool isV = hg >= 2;
  f32x16 kacc = {}; float kcs = 0.f; unsigned kn2 = 0u;
  auto dma_first = [&]() { dma_pair(wqb, lds0 + L1_S); dma_pair(wqb + 128 * 1280, lds0 + L1_S + 20480); };
  if (w >= 4) {
    dma_first();
#pragma unroll
    for (int ps = 0; ps < 2; ++ps) {
      const int t = (ps ? 32 - 32 * kth : 32 * kth) + 8 * (w - 4) + (lane >> 3), e = lane & 7;
      u32x4 v[10];
#pragma unroll
      for (int i = 0; i < 10; ++i) v[i] = lds128(lds + L1_X + (i >> 1) * 16384 + t * 256 + ((e + 8 * (i & 1) + 8 * ((t >> 1) & 1)) & 15) * 16);
      float a4[4] = {}, b4[4] = {};
#pragma unroll
      for (int i = 0; i < 10; ++i)
#pragma unroll
        for (int j = 0; j < 4; ++j) { a4[j] = dot2(v[i][j], v[i][j], a4[j]); b4[j] = dot2(v[i][j], ONES2, b4[j]); }
      const float sq = sum8((a4[0] + a4[1]) + (a4[2] + a4[3])), sm = sum8((b4[0] + b4[1]) + (b4[2] + b4[3]));
      const float mean = sm * (1.f / DM), rstd = rsqrtf(fmaxf(sq * (1.f / DM) - mean * mean, 0.f) + LN_EPS);
      if (e == 0) { ((LAS float*)(lds + L1_ST))[t] = mean; ((LAS float*)(lds + L1_ST))[64 + t] = rstd; }
      if (ps == 0) {
        LBAR(); dma_pair(wqb + 256 * 1280, lds0 + L1_WK);
        const int ln = ((rk >> 3) * 4 + (w - 4)) * 32 + r32;
        if (h == 0) touch((ln >> 1) * 1280 + (ln & 1) * 128, wqb + 384 * 1280 - ((hg * 160) & 127), lds0 + L1_DUMP);
      }
    }
  } else if (hg < 4) {
    lptr wb = lds + L1_WK + (160 * w + 8 * h + tq) * 40 + grp * 32 + 8 * tp;
    const int tok = kth * 32 + r32;
    u32x4 xf[10]; bf16x8 wf[10];
#pragma unroll
    for (int i = 0; i < 10; ++i) { const int c = 20 * w + 2 * i + h;
      xf[i] = lds128(lds + L1_X + (c >> 4) * 16384 + tok * 256 + ((c ^ tok) & 15) * 16); wf[i] = cat8(trread(wb + i * 640), trread(wb + i * 640 + 160)); }
    SBAR();
    if (w != 0) dma_first();
    if (isV) {
#pragma unroll
      for (int i = 0; i < 10; ++i) kacc = MFMA16(asbf8(xf[i]), wf[i], kacc);
    } else {
#pragma unroll
      for (int i = 0; i < 10; ++i) kacc = MFMA16(wf[i], asbf8(xf[i]), kacc);
    }
    float c4[4] = {};
#pragma unroll
    for (int i = 0; i < 10; ++i) { const u32x4 wu = asu4(wf[i]);
#pragma unroll
      for (int j = 0; j < 4; ++j) c4[j] = dot2(wu[j], ONES2, c4[j]); }
    kcs = swap32_sum((c4[0] + c4[1]) + (c4[2] + c4[3]));
    if (w > 0) {
      LAS f32x4* pa = (LAS f32x4*)(lds + L1_PART + (w - 1) * 4224) + lane;
#pragma unroll
      for (int g = 0; g < 4; ++g) pa[g * 64] = f32x4{kacc[4 * g], kacc[4 * g + 1], kacc[4 * g + 2], kacc[4 * g + 3]};
      if (h == 0) ((LAS float*)(lds + L1_PART + (w - 1) * 4224 + 4096))[r32] = kcs;
    }
  } else dma_first();
  if (w < 4) {
    LBAR();
    if (w != 0 || hg >= 4) dma_pair(wqb + 256 * 1280, lds0 + L1_WK);
  }
  if (hg < 4 && w == 0) {
#pragma unroll
    for (int v = 0; v < 3; ++v) {
      LAS f32x4* pa = (LAS f32x4*)(lds + L1_PART + v * 4224) + lane;
#pragma unroll
      for (int g = 0; g < 4; ++g) { const f32x4 t = pa[g * 64]; kacc[4 * g] += t[0]; kacc[4 * g + 1] += t[1]; kacc[4 * g + 2] += t[2]; kacc[4 * g + 3] += t[3]; }
      kcs += ((LAS float*)(lds + L1_PART + v * 4224 + 4096))[r32];
    }
    const int tile = tg * 2 + kth;
    const LAS float* st = (const LAS float*)(lds + L1_ST) + kth * 32;
    if (lane == 0 && hg == 0) { bst32<AUX_SC1>(ws, W_CNTO + tg * 256, 0u); bst32<AUX_SC1>(ws, W_CNTH + tg * 256, 0u); }
    if (lane == 0 && hg == 1 && !(tg & 1)) bst32<AUX_SC1>(ws, W_CNTG + (tg >> 1) * 256, 0u);
    if (!isV) {
      LAS float* tab = (LAS float*)(lds + L1_KC);
      if (h == 0) tab[r32] = kcs;
      const float mean = st[r32], rstd = st[64 + r32];
      asm volatile("" ::: "memory"); WAITL(); asm volatile("" ::: "memory");
      float kv[12];
#pragma unroll
      for (int g = 0; g < 3; ++g) { const f32x4 c4 = *(LAS f32x4*)(tab + 8 * g + 4 * h);
#pragma unroll
        for (int j = 0; j < 4; ++j) kv[4 * g + j] = (kacc[4 * g + j] - mean * c4[j]) * rstd; }
      const u32x4 k1 = {pk2(kv[0], kv[1]), pk2(kv[2], kv[3]), pk2(kv[4], kv[5]), pk2(kv[6], kv[7])};
      const u32x2 k2 = {pk2(kv[8], kv[9]), pk2(kv[10], kv[11])};
      bst128<AUX_SC1>(ws, W_KV + tile * KV_TILE + l16, k1);
      if (h == 0) bst64<AUX_SC1>(ws, W_KV + tile * KV_TILE + KV_K2 + r32 * 8, k2);
      float n2 = 0.f;
#pragma unroll
      for (int j = 0; j < 4; ++j) n2 = dot2(k1[j], k1[j], n2);
      if (h == 0) { n2 = dot2(k2[0], k2[0], n2); n2 = dot2(k2[1], k2[1], n2); }
      kn2 = __float_as_uint(max64(swap32_sum(n2)));
    } else {
      const unsigned cv = r32 == 20 ? ONES2 : 0u;
#pragma unroll
      for (int s = 0; s < 2; ++s) {
        float vv[8];
#pragma unroll
        for (int g = 0; g < 2; ++g) { const f32x4 m4 = *(const LAS f32x4*)(st + 16 * s + 8 * g + 4 * h), r4 = *(const LAS f32x4*)(st + 64 + 16 * s + 8 * g + 4 * h);
#pragma unroll
          for (int j = 0; j < 4; ++j) vv[4 * g + j] = (kacc[8 * s + 4 * g + j] - m4[j] * kcs) * r4[j]; }
        u32x4 o = {pk2(vv[0], vv[1]), pk2(vv[2], vv[3]), pk2(vv[4], vv[5]), pk2(vv[6], vv[7])};
        if (r32 >= 20) o = u32x4{cv, cv, cv, cv};
        if (r32 < 22) bst128<AUX_SC1>(ws, W_KV + tile * KV_TILE + KV_V + (2 * s + h) * KV_VS + r32 * 16, o);
      }
    }
  }
  if (hg < 4 && w == 0) {
    dma_first(); dma_pair(wqb + 256 * 1280, lds0 + L1_WK);
    WAITV(9);
    if (lane == 0) __hip_atomic_store((GAS unsigned long long*)(pa->ws + W_FKV) + tg * 4 + hg, ((unsigned long long)epoch << 32) | kn2, RLX_AGENT);
    WAITV(4);
  } else if (w >= 4) WAITV(4); else WAITV(3);
  LBAR();
  STAMPX(2);
  {
    const int hh = w & 3, th = w >> 2, tok = th * 32 + r32;
    f32x16 acc = {}; float c4[4] = {};
    const unsigned wrow = (8 * h + 2 * tq) * 160 + hh * 40 + grp * 32 + 8 * tp;
    unsigned xo[8];
#pragma unroll
    for (int i = 0; i < 8; ++i) xo[i] = L1_X + tok * 256 + (((2 * i + h) ^ tok) & 15) * 16;
    u32x4 xf[2][4]; bf16x8 wf[2][4];
    unsigned xa = 0, wa = L1_S + wrow;
#define Q_RD1(e, ks, XA, WA) do { xf[e][ks] = lds128(lds + xo[4 * (e) + (ks)] + (XA)); lptr a_ = lds + (WA) + (e) * 10240 + (ks) * 2560; wf[e][ks] = cat8(trread(a_), trread(a_ + 160)); } while (0)
#define Q_STEP(e, XA, WA) do { _Pragma("unroll") for (int ks = 0; ks < 4; ++ks) { acc = MFMA16(wf[e][ks], asbf8(xf[e][ks]), acc); Q_RD1(1 - (e), ks, XA, WA); SBAR(); } } while (0)
#define Q_CS(e) do { _Pragma("unroll") for (int ks = 0; ks < 4; ++ks) { const u32x4 wu = asu4(wf[e][ks]); _Pragma("unroll") for (int j = 0; j < 4; ++j) c4[j] = dot2(wu[j], ONES2, c4[j]); } } while (0)
#pragma unroll
    for (int ks = 0; ks < 4; ++ks) Q_RD1(0, ks, xa, wa);
#pragma unroll 1
    for (int pr = 0; pr < 5; ++pr) {
      Q_STEP(0, xa, wa);
      if (pr == 1 || pr == 2) WAITV(3); else if (pr == 3) WAITV(0);
      if (pr == 2 && w == 0) {
        const float km = max64(wait_granules(pa->ws, W_FKV, epoch, lane));
        if (lane == 0) *(LAS float*)(lds + L1_KM) = km;
      }
      LBAR();
      if (pr == 2) {
        kmax2 = *(LAS float*)(lds + L1_KM);
        if (th == 0) { dma_kv3(pa->ws + W_KV, lds0, 0, hh, lane); dma_kv3(pa->ws + W_KV, lds0, 2, hh, lane); } else dma_kv3(pa->ws + W_KV, lds0, 1, hh, lane);
      }
      const unsigned wn = pr == 1 ? L1_WK + wrow : (pr == 2 || pr == 4 ? L1_S + wrow : wa + 20480);
      if (pr < 2) dma_pair(wqb + (size_t)(pr + 3) * 128 * 1280, lds0 + L1_S + pr * 20480);
      if (th == 0) Q_CS(0); else Q_CS(1);
      SBAR();
      xa = pr < 4 ? xa + 16384 : 0;
      Q_STEP(1, xa, wn);
      wa = wn;
    }
#undef Q_RD1
#undef Q_STEP
#undef Q_CS
    const float cs = swap32_sum((c4[0] + c4[1]) + (c4[2] + c4[3]));
    if (h == 0) ((LAS float*)(lds + L1_CS))[w * 32 + r32] = cs;
    const float mean = ((LAS float*)(lds + L1_ST))[tok], rstd = ((LAS float*)(lds + L1_ST))[64 + tok] * QSCALE;
    LBAR();
    float qv[12];
#pragma unroll
    for (int g = 0; g < 3; ++g) {
      const f32x4 ca = *(LAS f32x4*)(lds + L1_CS + (hh * 32 + 8 * g + 4 * h) * 4), cb = *(LAS f32x4*)(lds + L1_CS + ((hh + 4) * 32 + 8 * g + 4 * h) * 4);
#pragma unroll
      for (int j = 0; j < 4; ++j) qv[4 * g + j] = (acc[4 * g + j] - mean * (ca[j] + cb[j])) * rstd;
    }
    q1 = u32x4{pk2(qv[0], qv[1]), pk2(qv[2], qv[3]), pk2(qv[4], qv[5]), pk2(qv[6], qv[7])};
    q2 = h ? u32x2{0u, 0u} : u32x2{pk2(qv[8], qv[9]), pk2(qv[10], qv[11])};
  }
  STAMPX(3);
}

constexpr unsigned L2_CO = 60 * KV_TILE, L2_CR = L2_CO + 16, L2_CP = L2_CO + 32, L2_CS = L2_CO + 48, L2_CD = L2_CO + 112, L2_DUMP = L2_CO + 256;
constexpr unsigned L2_RT = 52 * KV_TILE;
constexpr unsigned L3_WO = 0, L3_RED = 102400;
#define PRIO_M 3
#define EXP2(a) __builtin_amdgcn_exp2f(a)

DI f32x16 attn_exact(rsrc_t ws, u32x4 q1, u32x2 q2, int lane) {
  const int r32 = lane & 31, h = lane >> 5, rv = r32 < 21 ? r32 : 21;
  float mx = -3.0e38f;
#pragma unroll 1
  for (int t = 0; t < 64; ++t) {
    const unsigned base = W_KV + t * KV_TILE;
    f32x16 s = MFMA8(asbf4(bld64<AUX_SC1>(ws, base + KV_K2 + r32 * 8)), asbf4(q2), f32x16{});
    s = MFMA16(asbf8(bld128<AUX_SC1>(ws, base + lane * 16)), asbf8(q1), s);
#pragma unroll
    for (int j = 0; j < 16; ++j) mx = fmaxf(mx, s[j]);
  }
  mx = swap32_max(mx);
  f32x16 o = {};
#pragma unroll 1
  for (int t = 0; t < 64; ++t) {
    const unsigned base = W_KV + t * KV_TILE;
    f32x16 s = MFMA8(asbf4(bld64<AUX_SC1>(ws, base + KV_K2 + r32 * 8)), asbf4(q2), f32x16{});
    s = MFMA16(asbf8(bld128<AUX_SC1>(ws, base + lane * 16)), asbf8(q1), s);
    u32x4 pp[2];
#pragma unroll
    for (int j = 0; j < 8; ++j) pp[j >> 2][j & 3] = pk2(EXP2(s[2 * j] - mx), EXP2(s[2 * j + 1] - mx));
#pragma unroll
    for (int e = 0; e < 2; ++e) o = MFMA16(asbf8(bld128<AUX_SC1>(ws, base + KV_V + (2 * e + h) * KV_VS + rv * 16)), asbf8(pp[e]), o);
  }
  return o;
}

DI void phase_attn(lptr lds, rsrc_t ws, int b, const u32x4 q1, const u32x2 q2, unsigned epoch, float kmax2) {
  const int tid = threadIdx.x, lane = tid & 63, w = rfl(tid >> 6);
  const int r32 = lane & 31, h = lane >> 5;
  const int xcd = b & 7, rk = b >> 3, tg = 4 * xcd + (rk >> 3), hg = rk & 7;
  const int hh = w & 3, th = w >> 2;
  const unsigned lds0 = (unsigned)(uintptr_t)lds, l16 = lane * 16;
  if (tid < 8) *(LAS unsigned*)(lds + L2_CO + tid * 16) = 0u;
  f32x16 cb;
  {
    float n2 = 0.f;
#pragma unroll
    for (int j = 0; j < 4; ++j) n2 = dot2(q1[j], q1[j], n2);
    n2 = dot2(q2[0], q2[0], n2); n2 = dot2(q2[1], q2[1], n2);
    const float ref = sqrtf(swap32_sum(n2) * kmax2) * 1.001f;
#pragma unroll
    for (int j = 0; j < 16; ++j) cb[j] = -ref;
    PIN(cb);
  }
  const int rv = r32 < 21 ? r32 : 21;
  const unsigned oK1 = l16, oK2 = KV_K2 + r32 * 8, oV = KV_V + h * KV_VS + rv * 16;
  f32x16 o = {};
  LBAR();
  STAMPX(9);
  {
    lptr aK1 = lds + oK1; lptr aK2 = lds + oK2; lptr aV = lds + oV;
    u32x4 k1 = lds128(aK1); u32x2 k2 = lds64(aK2);
    f32x16 sc = MFMA8(asbf4(k2), asbf4(q2), cb); sc = MFMA16(asbf8(k1), asbf8(q1), sc);
    k1 = lds128(aK1 + KV_TILE); k2 = lds64(aK2 + KV_TILE);
    u32x4 v0 = lds128(aV), v1 = {}, p0, p1 = {};
    u32x4 k1b, v0b, v1b; u32x2 k2b;
    f32x16 sn;
#define GRP(MF, SC, i, P, j) do { MF; SBAR(); { const float e0 = EXP2(SC[i]), e1 = EXP2(SC[i + 1]), e2 = EXP2(SC[i + 2]), e3 = EXP2(SC[i + 3]); P[j] = pk2(e0, e1); P[j + 1] = pk2(e2, e3); } PIN(P); SBAR(); } while (0)
#define TILE(SC, SN, K1, K2, V0, V1, K1N, K2N, V0N, V1N, PK1, PK2, PV0, PV1) do { \
      WAITL(); \
      K2N = lds64(PK2); K1N = lds128(PK1); V0N = lds128(PV0); V1N = lds128(PV1); SBAR(); \
      GRP(SN = MFMA8(asbf4(K2), asbf4(q2), cb), SC, 0, p0, 0); \
      GRP(o = MFMA16(asbf8(V1), asbf8(p1), o), SC, 4, p0, 2); \
      GRP(SN = MFMA16(asbf8(K1), asbf8(q1), SN), SC, 8, p1, 0); \
      GRP(o = MFMA16(asbf8(V0), asbf8(p0), o), SC, 12, p1, 2); } while (0)
#define TA(T) TILE(sc, sn, k1, k2, v0, v1, k1b, k2b, v0b, v1b, aK1 + ((T) + 2) * KV_TILE, aK2 + ((T) + 2) * KV_TILE, aV + ((T) + 1) * KV_TILE, aV + (T) * KV_TILE + 2 * KV_VS)
#define TB(T) TILE(sn, sc, k1b, k2b, v0b, v1b, k1, k2, v0, v1, aK1 + ((T) + 2) * KV_TILE, aK2 + ((T) + 2) * KV_TILE, aV + ((T) + 1) * KV_TILE, aV + (T) * KV_TILE + 2 * KV_VS)
#define ADV() do { aK1 += 8 * KV_TILE; aK2 += 8 * KV_TILE; aV += 8 * KV_TILE; } while (0)
#define BODY8() do { TA(0); TB(1); TA(2); TB(3); TA(4); TB(5); TA(6); TB(7); ADV(); } while (0)
    if (th == 1) {
      const char* kvg = kargs()->ws + W_KV;
#define DMA3(c) do { dma_kv3(kvg, lds0, c, hh, lane); SBAR(); } while (0)
      __builtin_amdgcn_s_setprio(2);
      PIN(sc);
      DMA3(3); TA(0); DMA3(4); TB(1); DMA3(5); TA(2); DMA3(6); TB(3);
      DMA3(7); TA(4); DMA3(8); TB(5); DMA3(9); TA(6); DMA3(10); TB(7); ADV();
      if (lane == 0) lds_add1(lds + L2_CP);
      WAITV(6); if (lane == 0) lds_add1(lds + L2_CS);
      lds_wait_ge(lds + L2_CS, 4u);
      DMA3(11); TA(0); DMA3(12); TB(1); TA(2); TB(3); TA(4); TB(5); TA(6); TB(7); ADV();
      WAITV(0); if (lane == 0) lds_add1(lds + L2_CS + 16);
#pragma nounroll
      for (int m = 2; m < 7; ++m) {
        if (m == 4) { lds_wait_ge(lds + L2_CS + 16, 4u); lds_wait_ge(lds + L2_CR, 4u); DMA3(13); DMA3(14); }
        if (m == 5) { WAITV(0); if (lane == 0) lds_add1(lds + L2_CS + 32); lds_wait_ge(lds + L2_CP, 8u); DMA3(15); }
        if (m == 6) { WAITV(0); if (lane == 0) lds_add1(lds + L2_CS + 48); lds_wait_ge(lds + L2_CS + 32, 4u); }
        if (m == PRIO_M) __builtin_amdgcn_s_setprio(0);
        BODY8();
      }
#undef DMA3
    } else {
      __builtin_amdgcn_s_setprio(1);
      const int ga = b * 4 + hh;
      u32x4 rg[4];
      auto gu_load = [&](int gj) {
        kargs_t pa = kargs(); const int cgf = gj / 40, r = gj % 40;
        const rsrc_t wr = __builtin_amdgcn_make_buffer_rsrc((void*)((r & 1) ? pa->wu : pa->wg), 0, DM * FF * 2, 0x00020000);
        const unsigned vo = (32 * (r >> 1) + (lane >> 3)) * (FF * 2) + cgf * 128 + (lane & 7) * 16;
#pragma unroll
        for (int i = 0; i < 4; ++i) rg[i] = __builtin_amdgcn_raw_buffer_load_b128(wr, vo, i * 8 * FF * 2, 0);
      };
      auto gu_store = [&](int gj) {
        const int cgf = gj / 40, r = gj % 40, i16 = lane & 15, tq = i16 >> 2, tp = i16 & 3, g4 = lane >> 4;
        lptr scr = lds + L2_RT + hh * 4096;
#pragma unroll
        for (int i = 0; i < 4; ++i) *(LAS u32x4*)(scr + i * 1024 + l16) = rg[i];
#pragma unroll
        for (int u = 0; u < 4; ++u) { lptr a = scr + (8 * g4 + tq) * 128 + u * 32 + tp * 8;
          const u32x4 f = asu4(cat8(trread(a), trread(a + 512)));
          bst128<AUX_SC1>(ws, W_WGU + (((cgf * 4 + u) * 20 + (r >> 1)) * 2 + (r & 1)) * 1024 + l16, f);
          float cs = ((bflo(f[0]) + bfhi(f[0])) + (bflo(f[1]) + bfhi(f[1]))) + ((bflo(f[2]) + bfhi(f[2])) + (bflo(f[3]) + bfhi(f[3])));
          cs = swap32_sum(swap16_sum(cs));
          if (lane < 16) bst32<AUX_SC1>(ws, W_CSP + (((cgf * 4 + u) * 2 + (r & 1)) * 20 + (r >> 1)) * 64 + lane * 4, __float_as_uint(cs)); }
      };
      {
        kargs_t pa = kargs();
#pragma unroll
        for (int e = 0; e < 2; ++e) { const int gj = ga + 1024 * e;
          if (e == 0 || gj < 1080) { const int cgf = gj / 40, r = gj % 40; if (lane < 32) touch((32 * (r >> 1) + lane) * (FF * 2) + cgf * 128, (r & 1) ? pa->wu : pa->wg, lds0 + L2_DUMP); } }
        const int nl = ga < 986 ? 24 : (ga == 986 ? 16 : 0);
        if (lane < nl) touch(lane * 128, ga < 720 ? pa->wd + (size_t)ga * 3072 : pa->wo + (size_t)(ga - 720) * 3072, lds0 + L2_DUMP);
      }
      PIN(sc);
#pragma nounroll
      for (int m = 0; m < 7; ++m) {
        if (m == 1) {
          if (lane == 0) lds_add1(lds + L2_CP);
          u32x4 rd[3];
          gu_load(ga);
          {
            kargs_t pa = kargs();
#pragma unroll
            for (int i = 0; i < 3; ++i) { const int j = ga * 3 + i;
              const char* src = j < 2160 ? pa->wd + (size_t)j * 1024 : pa->wo + (size_t)(j < 2960 ? j - 2160 : 0) * 1024;
              rd[i] = *(const GAS u32x4*)(src + l16); }
          }
          gu_store(ga);
#pragma unroll
          for (int i = 0; i < 3; ++i) { const int j = ga * 3 + i;
            if (j < 2960) { const bool dn = j < 2160; const int a5 = (dn ? j : j - 2160) * 4 + (lane >> 4), f = (a5 * 52429) >> 18, c = (a5 - 5 * f) * 16 + (lane & 15), cg = (c * 6554) >> 16, cc = c - cg * 10;
              bst128<AUX_SC1>(ws, dn ? W_WD + (cg * FF + ((f & ~12) | ((f & 4) << 1) | ((f & 8) >> 1))) * 160 + cc * 16 : W_WO + (cg * DM + f) * 160 + cc * 16, rd[i]); } }
          if (ga < 56) { gu_load(ga + 1024); gu_store(ga + 1024); }
          WAITL(); if (lane == 0) lds_add1(lds + L2_CR);
          lds_wait_ge(lds + L2_CS, 4u);
        }
        if (m == 2) {
          WAITV(0);
          unsigned old = 0; if (lane == 0) old = __hip_atomic_fetch_add((LAS unsigned*)(lds + L2_CD), 1u, __ATOMIC_RELAXED, __HIP_MEMORY_SCOPE_WORKGROUP);
          if (lane == 0 && old == 3u) __hip_atomic_store((GAS unsigned*)(kargs()->ws + W_FRT) + b, epoch, RLX_AGENT);
        }
        if (m == 4) {
          char* wsp = kargs()->ws;
          wait_flags<4>(wsp, W_FRT, epoch, lane);
          if (b == 0 && w == 0 && lane == 0) __hip_atomic_store((GAS unsigned*)(wsp + W_EPOCH), epoch, RLX_AGENT);
          const int ngl = rk & 3, u0 = 27 * (xcd & 3) + 7 * ngl, part = (rk >> 2) * 4 + hh;
          const int nl = (ngl == 3 ? 6 : 7) * 10;
          touch_sc1((part * nl + lane) * 128, wsp + W_WGU + (size_t)u0 * 40960, lds0 + L2_DUMP);
          if (lane < nl - 64) touch_sc1((part * nl + 64 + lane) * 128, wsp + W_WGU + (size_t)u0 * 40960, lds0 + L2_DUMP);
          if (lane < 50) touch_sc1((((rk >> 3) * 4 + hh) * 50 + lane) * 128, wsp + W_WO + hg * DM * 160, lds0 + L2_DUMP);
          lds_wait_ge(lds + L2_CS + 16, 4u);
        }
        if (m == 6) lds_wait_ge(lds + L2_CS + 32, 4u);
        BODY8();
      }
    }
    lds_wait_ge(lds + L2_CS + 48, 4u);
    {
      lptr zK1 = lds + oK1; lptr zK2 = lds + oK2; lptr zV = lds + oV;
      TA(0); TB(1);
      TILE(sc, sn, k1, k2, v0, v1, k1b, k2b, v0b, v1b, zK1, zK2, aV + 3 * KV_TILE, aV + 2 * KV_TILE + 2 * KV_VS);
      TILE(sn, sc, k1b, k2b, v0b, v1b, k1, k2, v0, v1, zK1 + KV_TILE, zK2 + KV_TILE, zV, aV + 3 * KV_TILE + 2 * KV_VS);
      aK1 = zK1; aK2 = zK2; aV = zV;
      TA(0); TB(1); TA(2); TB(3);
    }
    WAITL();
    o = MFMA16(asbf8(v1), asbf8(p1), o);
    __builtin_amdgcn_s_setprio(0);
  }
  STAMPX(10);
  float lsum = __uint_as_float(__builtin_amdgcn_permlane32_swap(__float_as_uint(o[8]), __float_as_uint(o[8]), false, false)[1]);
  if (__builtin_expect(__any(!(lsum >= 5.421010862427522e-20f)), 0)) {
    o = attn_exact(ws, q1, q2, lane);
    lsum = __uint_as_float(__builtin_amdgcn_permlane32_swap(__float_as_uint(o[8]), __float_as_uint(o[8]), false, false)[1]);
  }
  {
    const float inv = __builtin_amdgcn_rcpf(lsum);
    const u32x4 oa = {pk2(o[0] * inv, o[1] * inv), pk2(o[2] * inv, o[3] * inv), pk2(o[4] * inv, o[5] * inv), pk2(o[6] * inv, o[7] * inv)};
    const u32x2 ob = {pk2(o[8] * inv, o[9] * inv), pk2(o[10] * inv, o[11] * inv)};
    const unsigned tile16 = tg * 4 + th * 2 + (r32 >> 4), g0 = 10 * hg + 5 * (hh >> 1);
    bst128<AUX_SC1>(ws, W_OF + ((tile16 * 80 + g0 + 2 * (hh & 1) + h) * 16 + (r32 & 15)) * 16, oa);
    if (h == 0) bst64<AUX_SC1>(ws, W_OF + ((tile16 * 80 + g0 + 4) * 16 + (r32 & 15)) * 16 + 8 * (hh & 1), ob);
  }
  BAR();
  {
    char* wsp = kargs()->ws;
    const char* src = wsp + W_WO + hg * DM * 160 + w * 1024; unsigned dst = lds0 + L3_WO + w * 1024, vo = l16;
#pragma unroll
    for (int i = 0; i < 4; ++i) { glds_sc1(vo, src, dst); vo += 8192; dst += 8192; }
    WAITV(4);
    unsigned old = 0; if (lane == 0) old = __hip_atomic_fetch_add((LAS unsigned*)(lds + L2_CO), 1u, __ATOMIC_RELAXED, __HIP_MEMORY_SCOPE_WORKGROUP);
    if (lane == 0 && old == 7u) (void)__hip_atomic_fetch_add((GAS unsigned*)(wsp + W_CNTO + tg * 256), 1u, RLX_AGENT);
#pragma unroll 1
    for (int i = 4; i < 12; ++i) { glds_sc1(vo, src, dst); vo += 8192; dst += 8192; }
    if (w < 4) glds_sc1(vo, src, dst);
  }
}

DI void phase_oproj(lptr lds, rsrc_t ws, int b, u32x2 (&hres)[5], unsigned epoch) {
  kargs_t pa = kargs();
  const int tid = threadIdx.x, lane = tid & 63, w = rfl(tid >> 6);
  const int xcd = b & 7, rk = b >> 3, tg = 4 * xcd + (rk >> 3), cg = rk & 7;
  const int mt = w & 3, kh = w >> 2, t16 = lane & 15, g = lane >> 4, tq = t16 >> 2, tp = t16 & 3;
  const int tok = tg * 64 + mt * 16 + t16;
  u32x2 xr[5] = {};
  if (kh == 0) {
#pragma unroll
    for (int nt = 0; nt < 5; ++nt) xr[nt] = *(const GAS u32x2*)(pa->x + (size_t)tok * 1280 + cg * 160 + nt * 32 + g * 8);
  }
  unsigned r1[10], r2[10];
#pragma unroll
  for (int i = 0; i < 10; ++i) {
    const int t = 4 * i + g, a5 = (t * 13) >> 6, m = t - 5 * a5, pair = 8 * kh + a5;
    const int last = m >> 2, ra = pair * 40 + (m >> 1) * 20 + (m & 1) * 4 - last * 24;
    r1[i] = L3_WO + (ra + tq) * 160 + tp * 8; r2[i] = r1[i] + (8 + 12 * last) * 160;
  }
  WAITV(0);
  wait_cnt2(pa->ws, W_CNTO + tg * 256, W_CNTO + tg * 256, 8u, epoch);
  STAMPX(13);
  bf16x8 bf[2][5];
  u32x4 a[10];
#pragma unroll
  for (int i = 0; i < 10; ++i) a[i] = bld128<AUX_SC1>(ws, W_OF + ((tg * 4 + mt) * 80 + 4 * (10 * kh + i)) * 256 + lane * 16);
#pragma unroll
  for (int nt = 0; nt < 5; ++nt) bf[0][nt] = cat8(trread(lds + r1[0] + nt * 32), trread(lds + r2[0] + nt * 32));
  f32x4 acc[5] = {};
#pragma unroll
  for (int i = 0; i < 10; ++i) {
    if (i + 1 < 10) {
#pragma unroll
      for (int nt = 0; nt < 5; ++nt) bf[(i + 1) & 1][nt] = cat8(trread(lds + r1[i + 1] + nt * 32), trread(lds + r2[i + 1] + nt * 32));
    }
    SBAR();
#pragma unroll
    for (int nt = 0; nt < 5; ++nt) acc[nt] = MFMAS(bf[i & 1][nt], asbf8(a[i]), acc[nt]);
    SBAR();
  }
  STAMPX(14);
  LAS f32x4* red = (LAS f32x4*)(lds + L3_RED) + mt * 320 + lane;
  if (kh == 1) {
#pragma unroll
    for (int nt = 0; nt < 5; ++nt) red[nt * 64] = acc[nt];
  }
  LBAR();
  if (kh == 0) {
    float s1 = 0.f, s2 = 0.f;
#pragma unroll
    for (int nt = 0; nt < 5; ++nt) {
      const f32x4 o = red[nt * 64];
      const float h0 = bflo(xr[nt][0]) + acc[nt][0] + o[0], h1 = bfhi(xr[nt][0]) + acc[nt][1] + o[1], h2 = bflo(xr[nt][1]) + acc[nt][2] + o[2], h3 = bfhi(xr[nt][1]) + acc[nt][3] + o[3];
      const u32x2 hv = {pk2(h0, h1), pk2(h2, h3)};
      hres[nt] = hv;
      s1 = dot2(hv[0], ONES2, s1); s1 = dot2(hv[1], ONES2, s1); s2 = dot2(hv[0], hv[0], s2); s2 = dot2(hv[1], hv[1], s2);
      const int gam = 10 * cg + 2 * nt + (g >> 1);
      bst64<AUX_SC1>(ws, W_HF + (((tg * 4 + mt) * 20 + (gam >> 2)) * 64 + (gam & 3) * 16 + t16) * 16 + (g & 1) * 8, hv);
    }
    s1 = swap32_sum(swap16_sum(s1)); s2 = swap32_sum(swap16_sum(s2));
    if (g == 0) bst64<AUX_SC1>(ws, W_HST + ((tg * 8 + cg) * 64 + mt * 16 + t16) * 8, u32x2{__float_as_uint(s1), __float_as_uint(s2)});
  }
  signal_cnt(kargs()->ws, W_CNTH + tg * 256);
}

constexpr unsigned L4_S = 0  , L4_ST = 122880  , L4_CO = 163568;
DI constexpr unsigned l5_slot(int c) { return c % 5 == 0 ? 123904u : (unsigned)(c % 5 - 1) * 30720u; }
constexpr unsigned L5_RED = 92160;
static_assert(123904 + 30720 <= L4_CO, "lds");
DI void dma_wd_chunk(const char* wd, unsigned lds0, int w, int lane, int c) {
  const unsigned l16 = lane * 16;
#pragma unroll
  for (int i = 0; i < 4; ++i) { int n = w + 8 * i; const bool dup = i == 3 && n >= 30; if (dup) n -= 8; const int k2 = n >= 15, j = n - 15 * k2;
    if (i < 3 || !dup || lane == 0) glds_sc1(l16 + j * 1024 + k2 * (864 * 160), wd + (size_t)c * (96 * 160), lds0 + l5_slot(c) + n * 1024); }
}
constexpr int GWIN = 10;
DI void phase_gateup(lptr lds, rsrc_t ws, int b, unsigned epoch) {
  char* wsp = kargs()->ws;
  const int tid = threadIdx.x, lane = tid & 63, w = rfl(tid >> 6);
  const int xcd = b & 7, rk = b >> 3, mb = 8 * (xcd >> 2) + (rk >> 2), ngl = rk & 3, u0 = 27 * (xcd & 3) + 7 * ngl, nu = ngl == 3 ? 6 : 7;
  const int t16 = lane & 15, g = lane >> 4;
  const unsigned lds0 = (unsigned)(uintptr_t)lds, l16 = lane * 16;
  const char* wd = wsp + W_WD + (size_t)(rk & 7) * FF * 160;
  const bool act = w < nu;
  const int u = u0 + (act ? w : 0);
  const unsigned wbase = W_WGU + u * 40960 + l16;
  u32x4 ag[GWIN], au[GWIN];
  if (act) {
#pragma unroll
    for (int i = 0; i < GWIN; ++i) { ag[i] = bld128<AUX_SC1>(ws, wbase + i * 2048); au[i] = bld128<AUX_SC1>(ws, wbase + i * 2048 + 1024); }
  }
  float csp[2][5];
#pragma unroll
  for (int m = 0; m < 2; ++m)
#pragma unroll
    for (int i = 0; i < 5; ++i) csp[m][i] = __uint_as_float(bld32<AUX_SC1>(ws, W_CSP + ((u * 2 + m) * 20 + 5 * g + i) * 64 + t16 * 4));
  dma_wd_chunk(wd, lds0, w, lane, 0);
  if (tid == 0) *(LAS unsigned*)(lds + L4_CO) = 0u;
  SBAR();
  wait_cnt2(wsp, W_CNTH + (2 * mb) * 256, W_CNTH + (2 * mb + 1) * 256, 8u, epoch);
  STAMPX(16);
  const char* hsrc = wsp + W_HF + (size_t)(mb * 8 + w) * 20 * 1024;
  auto dma_h = [&](int sc, int slot) {
#pragma unroll
    for (int i = 0; i < 5; ++i) glds_sc1(l16 + (5 * sc + i) * 1024, hsrc, lds0 + L4_S + slot * 40960 + i * 8192 + w * 1024);
  };
  dma_h(0, 0);
  u32x2 sv[8];
  if (tid < 128) {
#pragma unroll
    for (int c = 0; c < 8; ++c) sv[c] = bld64<AUX_SC1>(ws, W_HST + (((2 * mb + (tid >> 6)) * 8 + c) * 64 + (tid & 63)) * 8);
  }
  SBAR();
  dma_h(1, 1); dma_h(2, 2);
  if (tid < 128) {
    float s1 = 0.f, s2 = 0.f;
#pragma unroll
    for (int c = 0; c < 8; ++c) { s1 += __uint_as_float(sv[c][0]); s2 += __uint_as_float(sv[c][1]); }
    const float mean = s1 * (1.f / DM), rstd = rsqrtf(fmaxf(s2 * (1.f / DM) - mean * mean, 0.f) + LN_EPS);
    *(LAS f32x2*)(lds + L4_ST + tid * 8) = f32x2{mean, rstd};
  }
  WAITV(10);
  LBAR();
  STAMPX(17);
  if (act) {
    f32x4 aG[8] = {}, aU[8] = {};
    lptr hb = lds + L4_S + l16;
    u32x4 bb[2][4];
#pragma unroll
    for (int t = 0; t < 4; ++t) bb[0][t] = lds128(hb + t * 1024);
#pragma unroll
    for (int s = 0; s < 20; ++s) {
      const int slot = (s / 5) % 3, nslot = ((s + 1) / 5) % 3;
      if (s == 4) { WAITV(13); LBAR(); }
      if (s == 9) { WAITV(18); LBAR(); dma_h(3, 0); }
      if (s == 14) { WAITV(2); LBAR(); }
      const u32x4 cg_ = ag[s % GWIN], cu_ = au[s % GWIN];
      if (s + GWIN < 20) { ag[s % GWIN] = bld128<AUX_SC1>(ws, wbase + (s + GWIN) * 2048); au[s % GWIN] = bld128<AUX_SC1>(ws, wbase + (s + GWIN) * 2048 + 1024); }
#pragma unroll
      for (int t = 0; t < 4; ++t) bb[1][t] = lds128(hb + slot * 40960 + (s % 5) * 8192 + (4 + t) * 1024);
      SBAR();
#pragma unroll
      for (int t = 0; t < 4; ++t) { aG[t] = MFMAS(asbf8(cg_), asbf8(bb[0][t]), aG[t]); aU[t] = MFMAS(asbf8(cu_), asbf8(bb[0][t]), aU[t]); }
      SBAR();
      if (s + 1 < 20) {
#pragma unroll
        for (int t = 0; t < 4; ++t) bb[0][t] = lds128(hb + nslot * 40960 + ((s + 1) % 5) * 8192 + t * 1024);
      }
      SBAR();
#pragma unroll
      for (int t = 0; t < 4; ++t) { aG[4 + t] = MFMAS(asbf8(cg_), asbf8(bb[1][t]), aG[4 + t]); aU[4 + t] = MFMAS(asbf8(cu_), asbf8(bb[1][t]), aU[4 + t]); }
      SBAR();
    }
    STAMPX(18);
    const float sg = swap32_sum(swap16_sum(((csp[0][0] + csp[0][1]) + (csp[0][2] + csp[0][3])) + csp[0][4])), su = swap32_sum(swap16_sum(((csp[1][0] + csp[1][1]) + (csp[1][2] + csp[1][3])) + csp[1][4]));
    float cg4[4], cu4[4];
#pragma unroll
    for (int j = 0; j < 4; ++j) { cg4[j] = __shfl(sg, 4 * g + j); cu4[j] = __shfl(su, 4 * g + j); }
#pragma unroll
    for (int i = 0; i < 4; ++i) {
      u32x2 pk[2];
#pragma unroll
      for (int e = 0; e < 2; ++e) {
        const int t = 2 * i + e;
        const f32x2 mr = *(LAS f32x2*)(lds + L4_ST + (t * 16 + t16) * 8);
        const float r2 = mr[1] * mr[1];
        float hv[4];
#pragma unroll
        for (int j = 0; j < 4; ++j) hv[j] = (aG[t][j] - mr[0] * cg4[j]) * (aU[t][j] - mr[0] * cu4[j]) * r2;
        pk[e] = u32x2{pk2(hv[0], hv[1]), pk2(hv[2], hv[3])};
      }
      const auto s0 = __builtin_amdgcn_permlane16_swap(pk[0][0], pk[1][0], false, false), s1 = __builtin_amdgcn_permlane16_swap(pk[0][1], pk[1][1], false, false);
      bst128<AUX_SC1>(ws, W_GF + (((mb * 8 + 2 * i + (g & 1)) * 54 + (u >> 1)) * 64 + (2 * (u & 1) + (g >> 1)) * 16 + t16) * 16, u32x4{s0[0], s1[0], s0[1], s1[1]});
    }
  } else {
    WAITV(5); LBAR();
    WAITV(0); LBAR(); dma_h(3, 0);
    WAITV(0); LBAR();
  }
  STAMPX(21);
  BAR();
  dma_wd_chunk(wd, lds0, w, lane, 1);
  WAITV(4);
  unsigned old = 0; if (lane == 0) old = __hip_atomic_fetch_add((LAS unsigned*)(lds + L4_CO), 1u, __ATOMIC_RELAXED, __HIP_MEMORY_SCOPE_WORKGROUP);
  if (lane == 0 && old == 7u) (void)__hip_atomic_fetch_add((GAS unsigned*)(wsp + W_CNTG + mb * 256), 1u, RLX_AGENT);
#pragma unroll 1
  for (int c = 2; c < 5; ++c) dma_wd_chunk(wd, lds0, w, lane, c);
}

DI void gld128_sc1(u32x4& d, unsigned voff, const char* sbase) { asm volatile("global_load_dwordx4 %0, %1, %2 sc1" : "=v"(d) : "v"(voff), "s"(sbase) : "memory"); }
DI void phase_down(lptr lds, rsrc_t ws, int b, const u32x2 (&hres)[5], unsigned epoch) {
  char* wsp = kargs()->ws;
  const int tid = threadIdx.x, lane = tid & 63, w = rfl(tid >> 6);
  const int xcd = b & 7, rk = b >> 3, tg = 4 * xcd + (rk >> 3), cg = rk & 7;
  const int mt = w & 3, kh = w >> 2, t16 = lane & 15, g = lane >> 4, tq = t16 >> 2, tp = t16 & 3;
  const unsigned lds0 = (unsigned)(uintptr_t)lds, l16 = lane * 16;
  const char* wd = wsp + W_WD + (size_t)cg * FF * 160;
  WAITV(0);
  wait_cnt2(wsp, W_CNTG + (tg >> 1) * 256, W_CNTG + (tg >> 1) * 256, 16u, epoch);
  STAMPX(20);
  const char* abase = wsp + W_GF + (size_t)((tg * 4 + mt) * 54 + 27 * kh) * 1024;
  u32x4 a[4][3];
#pragma unroll
  for (int c = 0; c < 3; ++c)
#pragma unroll
    for (int k = 0; k < 3; ++k) gld128_sc1(a[c][k], l16 + (3 * c + k) * 1024, abase);
  f32x4 acc[5] = {};
  const unsigned brow = kh * 15360 + (16 * (g >> 1) + 4 * (g & 1) + tq) * 160 + tp * 8;
#define DOWN_WAIT(N, c) asm volatile("s_waitcnt vmcnt(" #N ")\n\ts_barrier" : "+v"(a[(c) & 3][0]), "+v"(a[(c) & 3][1]), "+v"(a[(c) & 3][2]) :: "memory")
  bf16x8 bf[2][5];
  { lptr bb = lds + l5_slot(0) + brow;
#pragma unroll
    for (int nt = 0; nt < 5; ++nt) bf[0][nt] = cat8(trread(bb + nt * 32), trread(bb + nt * 32 + 1280)); }
#pragma unroll
  for (int c = 0; c < 9; ++c) {
    asm volatile("" ::: "memory"); WAITL();
    if (c <= 1) DOWN_WAIT(6, c); else if (c == 2 || c == 6) DOWN_WAIT(10, c); else if (c <= 5) DOWN_WAIT(14, c); else if (c == 7) DOWN_WAIT(3, c); else DOWN_WAIT(0, c);
    if (c >= 1 && c + 4 < 9) dma_wd_chunk(wd, lds0, w, lane, c + 4);
    if (c + 3 < 9) {
#pragma unroll
      for (int k = 0; k < 3; ++k) gld128_sc1(a[(c + 3) & 3][k], l16 + (3 * (c + 3) + k) * 1024, abase); }
#pragma unroll
    for (int k = 0; k < 3; ++k) {
      const int sn = 3 * c + k + 1;
      if (sn < 27) { lptr bb = lds + l5_slot(sn / 3) + brow + (sn % 3) * 5120;
#pragma unroll
        for (int nt = 0; nt < 5; ++nt) bf[sn & 1][nt] = cat8(trread(bb + nt * 32), trread(bb + nt * 32 + 1280)); }
      SBAR();
#pragma unroll
      for (int nt = 0; nt < 5; ++nt) acc[nt] = MFMAS(bf[(3 * c + k) & 1][nt], asbf8(a[c & 3][k]), acc[nt]);
      SBAR();
    }
  }
  STAMPX(22);
  LAS f32x4* red = (LAS f32x4*)(lds + L5_RED) + mt * 320 + lane;
  if (kh == 1) {
#pragma unroll
    for (int nt = 0; nt < 5; ++nt) red[nt * 64] = acc[nt];
  }
  LBAR();
  if (kh == 0) {
    const int tok = tg * 64 + mt * 16 + t16; char* outp = kargs()->out;
#pragma unroll
    for (int nt = 0; nt < 5; ++nt) {
      const f32x4 o = red[nt * 64];
      const u32x2 r = {pk2(bflo(hres[nt][0]) + acc[nt][0] + o[0], bfhi(hres[nt][0]) + acc[nt][1] + o[1]), pk2(bflo(hres[nt][1]) + acc[nt][2] + o[2], bfhi(hres[nt][1]) + acc[nt][3] + o[3])};
      *(GAS u32x2*)(outp + (size_t)tok * 1280 + cg * 160 + nt * 32 + g * 8) = r;
    }
  }
}

#define CODE_BYTES 46080
__global__ void __launch_bounds__(NTHR, 2) k_block(Params p) {
  extern __shared__ __attribute__((aligned(16))) char smem[];
  lptr lds = (lptr)smem;
  const rsrc_t ws = __builtin_amdgcn_make_buffer_rsrc((void*)p.ws, 0, (int)W_END, 0x00020000);
  const int b = blockIdx.x;
  {
    unsigned long long pc; asm volatile("s_getpc_b64 %0" : "=s"(pc));
    const int ln = (b >> 3) * 16 + (threadIdx.x & 15);
    if (threadIdx.x >= 448 && (threadIdx.x & 63) < 16 && ln * 128 < CODE_BYTES) touch(ln * 128, (const char*)(pc & ~127ull), (unsigned)(uintptr_t)smem + 163584);
  }
  u32x4 q1; u32x2 q2; unsigned epoch; float kmax2 = 0.f;
  u32x2 hres[5];
  STAMPX(0);
  phase1(lds, ws, b, q1, q2, epoch, kmax2);
  STAMPX(5);
  phase_attn(lds, ws, b, q1, q2, epoch, kmax2);
  STAMPX(12);
  phase_oproj(lds, ws, b, hres, epoch);
  STAMPX(15);
  phase_gateup(lds, ws, b, epoch);
  STAMPX(19);
  phase_down(lds, ws, b, hres, epoch);
  STAMPX(23);
}

extern "C" void kernel_launch(void* const* d_in, const int* in_sizes, int n_in, void* d_out, int out_size, void* d_ws, size_t ws_size, hipStream_t stream) {
  Params p;
  p.x = (const char*)d_in[0]; p.wq = (const char*)d_in[1]; p.wk = (const char*)d_in[2]; p.wv = (const char*)d_in[3]; p.wo = (const char*)d_in[4];
  p.wg = (const char*)d_in[6]; p.wu = (const char*)d_in[7]; p.wd = (const char*)d_in[8];
  p.out = (char*)d_out; p.ws = (char*)d_ws;
  constexpr int LDS_BYTES = 163840;
  (void)hipFuncSetAttribute((const void*)k_block, hipFuncAttributeMaxDynamicSharedMemorySize, LDS_BYTES);
  k_block<<<NWG, NTHR, LDS_BYTES, stream>>>(p);
}
```

```cpp
#include <hip/hip_runtime.h>
#include <stdint.h>

#define DI __device__ __forceinline__
#define LAS __attribute__((address_space(3)))
#define GAS __attribute__((address_space(1)))
typedef short bf16x8 __attribute__((ext_vector_type(8)));
typedef short bf16x4 __attribute__((ext_vector_type(4)));
typedef __bf16 bfv2 __attribute__((ext_vector_type(2)));
typedef float f32x2 __attribute__((ext_vector_type(2)));
typedef float f32x4 __attribute__((ext_vector_type(4)));
typedef float f32x16 __attribute__((ext_vector_type(16)));
typedef unsigned u32x2 __attribute__((ext_vector_type(2)));
typedef unsigned u32x4 __attribute__((ext_vector_type(4)));
typedef LAS char* lptr;

constexpr int SEQ = 2048, DM = 640, FF = 1728;
constexpr int NWG = 256, NTHR = 512;
constexpr float LN_EPS = 1e-5f;
constexpr float QSCALE = 0.22360679774997896f * 1.4426950408889634f;
constexpr unsigned KV_TILE = 2688, KV_K2 = 1024, KV_V = 1280, KV_VS = 352;

constexpr unsigned W_EPOCH = 0, W_TMO = 256;
constexpr unsigned W_FKV = 1024  , W_FRT = 2048  ;
constexpr unsigned W_CNTO = 8192, W_CNTH = 16384, W_CNTG = 24576;
constexpr unsigned W_KV = 65536;
constexpr unsigned W_OF = W_KV + 64 * KV_TILE;
constexpr unsigned W_HF = W_OF + SEQ * DM * 2;
constexpr unsigned W_HST = W_HF + SEQ * DM * 2;
constexpr unsigned W_GF = W_HST + 32 * 8 * 64 * 8;
constexpr unsigned W_WGU = W_GF + 128 * 54 * 1024;
constexpr unsigned W_WD = W_WGU + 108 * 40 * 1024;
constexpr unsigned W_WO = W_WD + 8 * FF * 160;
constexpr unsigned W_CSP = W_WO + 8 * DM * 160;
constexpr unsigned W_END = W_CSP + 108 * 2 * 20 * 64;

#define STAMPW() do { } while (0)
#define STAMP(i) do { } while (0)
#define STW 0
#define STAMPX(i) do { STAMP(i); if (STW == (i)) STAMPW(); } while (0)

struct Params {
  const char* x; const char* wq; const char* wk; const char* wv; const char* wo;
  const char* wg; const char* wu; const char* wd;
  char* out; char* ws;
};

typedef const __attribute__((address_space(4))) Params* kargs_t;
DI kargs_t kargs() { kargs_t a = (kargs_t)__builtin_amdgcn_kernarg_segment_ptr(); asm volatile("" : "+s"(a)); return a; }
constexpr int AUX_SC1 = 16;
#define MFMA16(a, b, c) __builtin_amdgcn_mfma_f32_32x32x16_bf16((a), (b), (c), 0, 0, 0)
#define MFMA8(a, b, c) __builtin_amdgcn_mfma_f32_32x32x8bf16_1k((a), (b), (c), 0, 0, 0)
#define MFMAS(a, b, c) __builtin_amdgcn_mfma_f32_16x16x32_bf16((a), (b), (c), 0, 0, 0)
#define SBAR() __builtin_amdgcn_sched_barrier(0)
#define WAITV(n) asm volatile("s_waitcnt vmcnt(" #n ")" ::: "memory")
#define BAR() asm volatile("s_barrier" ::: "memory")
#define WAITL() __builtin_amdgcn_s_waitcnt(0xC07F)
#define LBAR() do { asm volatile("" ::: "memory"); WAITL(); BAR(); } while (0)
#define PIN(x) asm volatile("" : "+v"(x))

DI unsigned pk2(float lo, float hi) { f32x2 v = {lo, hi}; return __builtin_bit_cast(unsigned, __builtin_convertvector(v, bfv2)); }
DI float bflo(unsigned v) { return __uint_as_float(v << 16); }
DI float bfhi(unsigned v) { return __uint_as_float(v & 0xffff0000u); }
DI float dot2(unsigned a, unsigned b, float c) { return __builtin_amdgcn_fdot2_f32_bf16(__builtin_bit_cast(bfv2, a), __builtin_bit_cast(bfv2, b), c, false); }
constexpr unsigned ONES2 = 0x3f803f80u;
DI float swap32_sum(float v) { auto r = __builtin_amdgcn_permlane32_swap(__float_as_uint(v), __float_as_uint(v), false, false); return __uint_as_float(r[0]) + __uint_as_float(r[1]); }
DI float swap32_max(float v) { auto r = __builtin_amdgcn_permlane32_swap(__float_as_uint(v), __float_as_uint(v), false, false); return fmaxf(__uint_as_float(r[0]), __uint_as_float(r[1])); }
DI float swap16_sum(float v) { auto r = __builtin_amdgcn_permlane16_swap(__float_as_uint(v), __float_as_uint(v), false, false); return __uint_as_float(r[0]) + __uint_as_float(r[1]); }
template <int CTRL> DI float dppf(float v) { return __uint_as_float(__builtin_amdgcn_update_dpp(0u, __float_as_uint(v), CTRL, 0xf, 0xf, true)); }
DI float sum8(float v) { v += dppf<0xB1>(v); v += dppf<0x4E>(v); return v + dppf<0x141>(v); }
DI float max16(float v) { v = fmaxf(v, dppf<0x128>(v)); v = fmaxf(v, dppf<0x124>(v)); v = fmaxf(v, dppf<0x122>(v)); return fmaxf(v, dppf<0x121>(v)); }
DI float max64(float v) { v = max16(v); auto r = __builtin_amdgcn_permlane16_swap(__float_as_uint(v), __float_as_uint(v), false, false);
  return swap32_max(fmaxf(__uint_as_float(r[0]), __uint_as_float(r[1]))); }
typedef short v4i16_t __attribute__((ext_vector_type(4)));
DI bf16x4 trread(lptr p) { return __builtin_bit_cast(bf16x4, __builtin_amdgcn_ds_read_tr16_b64_v4i16((LAS v4i16_t*)p)); }
DI bf16x8 cat8(bf16x4 a, bf16x4 b) { return __builtin_shufflevector(a, b, 0, 1, 2, 3, 4, 5, 6, 7); }
DI u32x4 lds128(lptr p) { return *(LAS u32x4*)p; }
DI u32x2 lds64(lptr p) { return *(LAS u32x2*)p; }
DI bf16x8 asbf8(u32x4 v) { return __builtin_bit_cast(bf16x8, v); }
DI bf16x4 asbf4(u32x2 v) { return __builtin_bit_cast(bf16x4, v); }
DI u32x4 asu4(bf16x8 v) { return __builtin_bit_cast(u32x4, v); }
DI unsigned rfl(unsigned v) { return __builtin_amdgcn_readfirstlane(v); }
typedef __amdgpu_buffer_rsrc_t rsrc_t;
template <int AUX> DI u32x4 bld128(rsrc_t r, unsigned off) { return __builtin_amdgcn_raw_buffer_load_b128(r, off, 0, AUX); }
template <int AUX> DI u32x2 bld64(rsrc_t r, unsigned off) { return __builtin_amdgcn_raw_buffer_load_b64(r, off, 0, AUX); }
template <int AUX> DI unsigned bld32(rsrc_t r, unsigned off) { return __builtin_amdgcn_raw_buffer_load_b32(r, off, 0, AUX); }
template <int AUX> DI void bst128(rsrc_t r, unsigned off, u32x4 v) { __builtin_amdgcn_raw_buffer_store_b128(v, r, off, 0, AUX); }
template <int AUX> DI void bst64(rsrc_t r, unsigned off, u32x2 v) { __builtin_amdgcn_raw_buffer_store_b64(v, r, off, 0, AUX); }
template <int AUX> DI void bst32(rsrc_t r, unsigned off, unsigned v) { __builtin_amdgcn_raw_buffer_store_b32(v, r, off, 0, AUX); }
DI void glds(unsigned voff, const char* sbase, unsigned lds_base) {
  asm volatile("s_mov_b32 m0, %2\n\ts_nop 0\n\tglobal_load_lds_dwordx4 %0, %1" :: "v"(voff), "s"(sbase), "s"(rfl(lds_base)) : "memory");
}
DI void glds_sc1(unsigned voff, const char* sbase, unsigned lds_base) {
  asm volatile("s_mov_b32 m0, %2\n\ts_nop 0\n\tglobal_load_lds_dwordx4 %0, %1 sc1" :: "v"(voff), "s"(sbase), "s"(rfl(lds_base)) : "memory");
}
DI void touch(unsigned voff, const char* sbase, unsigned lds_dump) {
  asm volatile("s_mov_b32 m0, %2\n\ts_nop 0\n\tglobal_load_lds_dword %0, %1" :: "v"(voff), "s"(sbase), "s"(rfl(lds_dump)) : "memory");
}
DI void touch_sc1(unsigned voff, const char* sbase, unsigned lds_dump) {
  asm volatile("s_mov_b32 m0, %2\n\ts_nop 0\n\tglobal_load_lds_dword %0, %1 sc1" :: "v"(voff), "s"(sbase), "s"(rfl(lds_dump)) : "memory");
}

#define RLX_AGENT __ATOMIC_RELAXED, __HIP_MEMORY_SCOPE_AGENT
constexpr unsigned SPIN_CAP = 1u << 17;
DI bool spin_fail(unsigned& spins, char* wsp, unsigned epoch) {
  __builtin_amdgcn_s_sleep(1);
  GAS unsigned* tmo = (GAS unsigned*)(wsp + W_TMO);
  if (++spins > SPIN_CAP) { __hip_atomic_store(tmo, epoch, RLX_AGENT); return true; }
  return (spins & 255u) == 0u && rfl(__hip_atomic_load(tmo, RLX_AGENT)) == epoch;
}
template <int NW> DI void wait_flags(char* wsp, unsigned off, unsigned epoch, int lane) {
  for (unsigned spins = 0;;) {
    bool ok = true;
#pragma unroll
    for (int i = 0; i < NW; ++i) ok &= __hip_atomic_load((GAS unsigned*)(wsp + off) + i * 64 + lane, RLX_AGENT) == epoch;
    if (__all(ok)) break;
    if (spin_fail(spins, wsp, epoch)) break;
  }
  __builtin_amdgcn_fence(__ATOMIC_ACQUIRE, "wavefront");
}
DI float wait_granules(char* wsp, unsigned off, unsigned epoch, int lane) {
  unsigned mx;
  for (unsigned spins = 0;;) {
    const unsigned long long a = __hip_atomic_load((GAS unsigned long long*)(wsp + off) + lane, RLX_AGENT), b = __hip_atomic_load((GAS unsigned long long*)(wsp + off) + 64 + lane, RLX_AGENT);
    mx = max((unsigned)a, (unsigned)b);
    if (__all((unsigned)(a >> 32) == epoch && (unsigned)(b >> 32) == epoch)) break;
    if (spin_fail(spins, wsp, epoch)) break;
  }
  __builtin_amdgcn_fence(__ATOMIC_ACQUIRE, "wavefront");
  return __uint_as_float(mx);
}
DI void wait_cnt2(char* wsp, unsigned off0, unsigned off1, unsigned want, unsigned epoch, int poller) {
  if ((int)(threadIdx.x >> 6) == poller) {
    GAS unsigned* word = (GAS unsigned*)(wsp + ((threadIdx.x & 1) ? off1 : off0));
    for (unsigned spins = 0;;) {
      if (__all(__hip_atomic_load(word, RLX_AGENT) >= want)) break;
      if (spin_fail(spins, wsp, epoch)) break;
    }
    __builtin_amdgcn_fence(__ATOMIC_ACQUIRE, "wavefront");
  }
  LBAR();
}
DI void signal_cnt(char* wsp, unsigned off) {
  WAITV(0);
  LBAR();
  if (threadIdx.x == 0) (void)__hip_atomic_fetch_add((GAS unsigned*)(wsp + off), 1u, RLX_AGENT);
}
DI void lds_add1(lptr p) { (void)__hip_atomic_fetch_add((LAS unsigned*)p, 1u, __ATOMIC_RELAXED, __HIP_MEMORY_SCOPE_WORKGROUP); }
DI void lds_wait_ge(lptr p, unsigned want) {
  while ((unsigned)rfl(__hip_atomic_load((LAS unsigned*)p, __ATOMIC_RELAXED, __HIP_MEMORY_SCOPE_WORKGROUP)) < want) __builtin_amdgcn_s_sleep(2);
  asm volatile("" ::: "memory");
}

DI void dma_kv3(const char* kvg, unsigned lds0, int c, int j, int lane) {
  const unsigned dst = lds0 + (c == 15 ? 0 : c) * 4 * KV_TILE, l16 = lane * 16; const char* src = kvg + c * 4 * KV_TILE;
  glds_sc1(l16, src + j * 1024, dst + j * 1024);
  glds_sc1(l16, src + (j + 4) * 1024, dst + (j + 4) * 1024);
  const int n = j == 3 ? 7 : j + 8;
  if (j != 2 || lane < 32) glds_sc1(l16, src + n * 1024, dst + n * 1024);
}

constexpr unsigned L1_X = 0, L1_S = 81920, L1_WK = L1_S + 4 * 10240, L1_PART = L1_WK + 25600;
constexpr unsigned L1_CS = L1_PART  ;
constexpr unsigned L1_ST = 161792  , L1_KC = L1_ST + 512  , L1_KM = L1_KC + 128, L1_DUMP = L1_KM + 16;
static_assert(L1_PART + 12672 <= 161280 && L1_DUMP + 256 <= 163584, "lds");

DI void phase1(lptr lds, rsrc_t ws, int b, u32x4& q1, u32x2& q2, unsigned& epoch, float& kmax2) {
  kargs_t pa = kargs();
  const int tid = threadIdx.x, lane = tid & 63, w = rfl(tid >> 6);
  const int r32 = lane & 31, h = lane >> 5;
  const int xcd = b & 7, rk = b >> 3, tg = 4 * xcd + (rk >> 3), hg = rk & 7;
  const unsigned lds0 = (unsigned)(uintptr_t)lds, l16 = lane * 16;
  unsigned ep = __hip_atomic_load((GAS unsigned*)(pa->ws + W_EPOCH), RLX_AGENT);
  {
    const char* xb = pa->x + (size_t)(tg * 64 + 8 * w) * 1280;
    const int tl = lane >> 4;
    unsigned v0 = tl * 1280 + ((lane ^ (8 * w + tl)) & 15) * 16, v1 = (4 + tl) * 1280 + ((lane ^ (8 * w + 4 + tl)) & 15) * 16, xd = lds0 + L1_X + 8 * w * 256;
#pragma unroll 1
    for (int a = 0; a < 5; ++a) { glds(v0, xb, xd); glds(v1, xb, xd + 1024); v0 += 256; v1 += 256; xd += 16384; }
  }
  if (hg < 4) {
    const char* W = (hg < 2 ? pa->wk : pa->wv) + w * 1024;
#pragma unroll
    for (int i = 0; i < 3; ++i) glds(l16 + i * 8192, W, lds0 + L1_WK + w * 1024 + i * 8192);
    if (w == 0) glds(l16 + 3 * 8192, W, lds0 + L1_WK + 3 * 8192);
  }
  const char* wqb = pa->wq + hg * 160;
  unsigned vq[3], dq[3];
#pragma unroll
  for (int i = 0; i < 3; ++i) {
    const int n = i < 2 ? w + 8 * i : (w < 6 ? w + 16 : w + 8);
    const int slot = 6 * n + lane / 10, row = (slot & ~7) | ((slot & 1) << 2) | ((slot >> 1) & 3);
    vq[i] = row * 1280 + (lane % 10) * 16; dq[i] = n * 960;
  }
  const bool m3 = w < 5 || (w == 5 ? lane < 20 : lane == 0);
  auto dma_pair = [&](const char* src, unsigned dst) {
    glds(vq[0], src, dst + dq[0]);
    glds(vq[1], src, dst + dq[1]);
    if (m3) glds(vq[2], src, dst + dq[2]);
  };
  SBAR();
  STAMPX(8);
  const int i16 = lane & 15, tq = i16 >> 2, tp = i16 & 3, grp = (lane >> 4) & 1;
  WAITV(0);
  LBAR();
  PIN(ep); epoch = rfl(ep) + 1u;
  STAMPX(1);
  const int kth = hg & 1; const bool isV = hg >= 2;
  f32x16 kacc = {}; float kcs = 0.f; unsigned kn2 = 0u;
  auto dma_first = [&]() { dma_pair(wqb, lds0 + L1_S); dma_pair(wqb + 128 * 1280, lds0 + L1_S + 20480); };
  if (w >= 4) {
    dma_first();
#pragma unroll
    for (int ps = 0; ps < 2; ++ps) {
      const int t = (ps ? 32 - 32 * kth : 32 * kth) + 8 * (w - 4) + (lane >> 3), e = lane & 7;
      u32x4 v[10];
#pragma unroll
      for (int i = 0; i < 10; ++i) v[i] = lds128(lds + L1_X + (i >> 1) * 16384 + t * 256 + ((e + 8 * (i & 1) + 8 * ((t >> 1) & 1)) & 15) * 16);
      float a4[4] = {}, b4[4] = {};
#pragma unroll
      for (int i = 0; i < 10; ++i)
#pragma unroll
        for (int j = 0; j < 4; ++j) { a4[j] = dot2(v[i][j], v[i][j], a4[j]); b4[j] = dot2(v[i][j], ONES2, b4[j]); }
      const float sq = sum8((a4[0] + a4[1]) + (a4[2] + a4[3])), sm = sum8((b4[0] + b4[1]) + (b4[2] + b4[3]));
      const float mean = sm * (1.f / DM), rstd = rsqrtf(fmaxf(sq * (1.f / DM) - mean * mean, 0.f) + LN_EPS);
      if (e == 0) { ((LAS float*)(lds + L1_ST))[t] = mean; ((LAS float*)(lds + L1_ST))[64 + t] = rstd; }
      if (ps == 0) {
        LBAR(); dma_pair(wqb + 256 * 1280, lds0 + L1_WK);
        const int ln = ((rk >> 3) * 4 + (w - 4)) * 32 + r32;
        if (h == 0) touch((ln >> 1) * 1280 + (ln & 1) * 128, wqb + 384 * 1280 - ((hg * 160) & 127), lds0 + L1_DUMP);
      }
    }
  } else if (hg < 4) {
    lptr wb = lds + L1_WK + (160 * w + 8 * h + tq) * 40 + grp * 32 + 8 * tp;
    const int tok = kth * 32 + r32;
    u32x4 xf[10]; bf16x8 wf[10];
#pragma unroll
    for (int i = 0; i < 10; ++i) { const int c = 20 * w + 2 * i + h;
      xf[i] = lds128(lds + L1_X + (c >> 4) * 16384 + tok * 256 + ((c ^ tok) & 15) * 16); wf[i] = cat8(trread(wb + i * 640), trread(wb + i * 640 + 160)); }
    SBAR();
    if (w != 0) dma_first();
    if (isV) {
#pragma unroll
      for (int i = 0; i < 10; ++i) kacc = MFMA16(asbf8(xf[i]), wf[i], kacc);
    } else {
#pragma unroll
      for (int i = 0; i < 10; ++i) kacc = MFMA16(wf[i], asbf8(xf[i]), kacc);
    }
    float c4[4] = {};
#pragma unroll
    for (int i = 0; i < 10; ++i) { const u32x4 wu = asu4(wf[i]);
#pragma unroll
      for (int j = 0; j < 4; ++j) c4[j] = dot2(wu[j], ONES2, c4[j]); }
    kcs = swap32_sum((c4[0] + c4[1]) + (c4[2] + c4[3]));
    if (w > 0) {
      LAS f32x4* pa = (LAS f32x4*)(lds + L1_PART + (w - 1) * 4224) + lane;
#pragma unroll
      for (int g = 0; g < 4; ++g) pa[g * 64] = f32x4{kacc[4 * g], kacc[4 * g + 1], kacc[4 * g + 2], kacc[4 * g + 3]};
      if (h == 0) ((LAS float*)(lds + L1_PART + (w - 1) * 4224 + 4096))[r32] = kcs;
    }
  } else dma_first();
  if (w < 4) {
    LBAR();
    if (w != 0 || hg >= 4) dma_pair(wqb + 256 * 1280, lds0 + L1_WK);
  }
  if (hg < 4 && w == 0) {
#pragma unroll
    for (int v = 0; v < 3; ++v) {
      LAS f32x4* pa = (LAS f32x4*)(lds + L1_PART + v * 4224) + lane;
#pragma unroll
      for (int g = 0; g < 4; ++g) { const f32x4 t = pa[g * 64]; kacc[4 * g] += t[0]; kacc[4 * g + 1] += t[1]; kacc[4 * g + 2] += t[2]; kacc[4 * g + 3] += t[3]; }
      kcs += ((LAS float*)(lds + L1_PART + v * 4224 + 4096))[r32];
    }
    const int tile = tg * 2 + kth;
    const LAS float* st = (const LAS float*)(lds + L1_ST) + kth * 32;
    if (lane == 0 && hg == 0) { bst32<AUX_SC1>(ws, W_CNTO + tg * 256, 0u); bst32<AUX_SC1>(ws, W_CNTH + tg * 256, 0u); }
    if (lane == 0 && hg == 1 && !(tg & 1)) bst32<AUX_SC1>(ws, W_CNTG + (tg >> 1) * 256, 0u);
    if (!isV) {
      LAS float* tab = (LAS float*)(lds + L1_KC);
      if (h == 0) tab[r32] = kcs;
      const float mean = st[r32], rstd = st[64 + r32];
      asm volatile("" ::: "memory"); WAITL(); asm volatile("" ::: "memory");
      float kv[12];
#pragma unroll
      for (int g = 0; g < 3; ++g) { const f32x4 c4 = *(LAS f32x4*)(tab + 8 * g + 4 * h);
#pragma unroll
        for (int j = 0; j < 4; ++j) kv[4 * g + j] = (kacc[4 * g + j] - mean * c4[j]) * rstd; }
      const u32x4 k1 = {pk2(kv[0], kv[1]), pk2(kv[2], kv[3]), pk2(kv[4], kv[5]), pk2(kv[6], kv[7])};
      const u32x2 k2 = {pk2(kv[8], kv[9]), pk2(kv[10], kv[11])};
      bst128<AUX_SC1>(ws, W_KV + tile * KV_TILE + l16, k1);
      if (h == 0) bst64<AUX_SC1>(ws, W_KV + tile * KV_TILE + KV_K2 + r32 * 8, k2);
      float n2 = 0.f;
#pragma unroll
      for (int j = 0; j < 4; ++j) n2 = dot2(k1[j], k1[j], n2);
      if (h == 0) { n2 = dot2(k2[0], k2[0], n2); n2 = dot2(k2[1], k2[1], n2); }
      kn2 = __float_as_uint(max64(swap32_sum(n2)));
    } else {
      const unsigned cv = r32 == 20 ? ONES2 : 0u;
#pragma unroll
      for (int s = 0; s < 2; ++s) {
        float vv[8];
#pragma unroll
        for (int g = 0; g < 2; ++g) { const f32x4 m4 = *(const LAS f32x4*)(st + 16 * s + 8 * g + 4 * h), r4 = *(const LAS f32x4*)(st + 64 + 16 * s + 8 * g + 4 * h);
#pragma unroll
          for (int j = 0; j < 4; ++j) vv[4 * g + j] = (kacc[8 * s + 4 * g + j] - m4[j] * kcs) * r4[j]; }
        u32x4 o = {pk2(vv[0], vv[1]), pk2(vv[2], vv[3]), pk2(vv[4], vv[5]), pk2(vv[6], vv[7])};
        if (r32 >= 20) o = u32x4{cv, cv, cv, cv};
        if (r32 < 22) bst128<AUX_SC1>(ws, W_KV + tile * KV_TILE + KV_V + (2 * s + h) * KV_VS + r32 * 16, o);
      }
    }
  }
  if (hg < 4 && w == 0) {
    dma_first(); dma_pair(wqb + 256 * 1280, lds0 + L1_WK);
    WAITV(9);
    if (lane == 0) __hip_atomic_store((GAS unsigned long long*)(pa->ws + W_FKV) + tg * 4 + hg, ((unsigned long long)epoch << 32) | kn2, RLX_AGENT);
    WAITV(4);
  } else if (w >= 4) WAITV(4); else WAITV(3);
  LBAR();
  STAMPX(2);
  {
    const int hh = w & 3, th = w >> 2, tok = th * 32 + r32;
    f32x16 acc = {}; float c4[4] = {};
    const unsigned wrow = (8 * h + 2 * tq) * 160 + hh * 40 + grp * 32 + 8 * tp;
    unsigned xo[8];
#pragma unroll
    for (int i = 0; i < 8; ++i) xo[i] = L1_X + tok * 256 + (((2 * i + h) ^ tok) & 15) * 16;
    u32x4 xf[2][4]; bf16x8 wf[2][4];
    unsigned xa = 0, wa = L1_S + wrow;
#define Q_RD1(e, ks, XA, WA) do { xf[e][ks] = lds128(lds + xo[4 * (e) + (ks)] + (XA)); lptr a_ = lds + (WA) + (e) * 10240 + (ks) * 2560; wf[e][ks] = cat8(trread(a_), trread(a_ + 160)); } while (0)
#define Q_STEP(e, XA, WA) do { _Pragma("unroll") for (int ks = 0; ks < 4; ++ks) { acc = MFMA16(wf[e][ks], asbf8(xf[e][ks]), acc); Q_RD1(1 - (e), ks, XA, WA); SBAR(); } } while (0)
#define Q_CS(e) do { _Pragma("unroll") for (int ks = 0; ks < 4; ++ks) { const u32x4 wu = asu4(wf[e][ks]); _Pragma("unroll") for (int j = 0; j < 4; ++j) c4[j] = dot2(wu[j], ONES2, c4[j]); } } while (0)
#pragma unroll
    for (int ks = 0; ks < 4; ++ks) Q_RD1(0, ks, xa, wa);
#pragma unroll 1
    for (int pr = 0; pr < 5; ++pr) {
      Q_STEP(0, xa, wa);
      if (pr == 1 || pr == 2) WAITV(3); else if (pr == 3) WAITV(0);
      if (pr == 2 && w == 0) {
        const float km = max64(wait_granules(pa->ws, W_FKV, epoch, lane));
        if (lane == 0) *(LAS float*)(lds + L1_KM) = km;
      }
      LBAR();
      if (pr == 2) {
        kmax2 = *(LAS float*)(lds + L1_KM);
        if (th == 0) { dma_kv3(pa->ws + W_KV, lds0, 0, hh, lane); dma_kv3(pa->ws + W_KV, lds0, 2, hh, lane); } else dma_kv3(pa->ws + W_KV, lds0, 1, hh, lane);
      }
      const unsigned wn = pr == 1 ? L1_WK + wrow : (pr == 2 || pr == 4 ? L1_S + wrow : wa + 20480);
      if (pr < 2) dma_pair(wqb + (size_t)(pr + 3) * 128 * 1280, lds0 + L1_S + pr * 20480);
      if (th == 0) Q_CS(0); else Q_CS(1);
      SBAR();
      xa = pr < 4 ? xa + 16384 : 0;
      Q_STEP(1, xa, wn);
      wa = wn;
    }
#undef Q_RD1
#undef Q_STEP
#undef Q_CS
    const float cs = swap32_sum((c4[0] + c4[1]) + (c4[2] + c4[3]));
    if (h == 0) ((LAS float*)(lds + L1_CS))[w * 32 + r32] = cs;
    const float mean = ((LAS float*)(lds + L1_ST))[tok], rstd = ((LAS float*)(lds + L1_ST))[64 + tok] * QSCALE;
    LBAR();
    float qv[12];
#pragma unroll
    for (int g = 0; g < 3; ++g) {
      const f32x4 ca = *(LAS f32x4*)(lds + L1_CS + (hh * 32 + 8 * g + 4 * h) * 4), cb = *(LAS f32x4*)(lds + L1_CS + ((hh + 4) * 32 + 8 * g + 4 * h) * 4);
#pragma unroll
      for (int j = 0; j < 4; ++j) qv[4 * g + j] = (acc[4 * g + j] - mean * (ca[j] + cb[j])) * rstd;
    }
    q1 = u32x4{pk2(qv[0], qv[1]), pk2(qv[2], qv[3]), pk2(qv[4], qv[5]), pk2(qv[6], qv[7])};
    q2 = h ? u32x2{0u, 0u} : u32x2{pk2(qv[8], qv[9]), pk2(qv[10], qv[11])};
  }
  STAMPX(3);
}

constexpr unsigned L2_CO = 60 * KV_TILE, L2_CR = L2_CO + 16, L2_CP = L2_CO + 32, L2_CS = L2_CO + 48, L2_CD = L2_CO + 112, L2_CQ = L2_CO + 128, L2_CF = L2_CO + 144, L2_DUMP = L2_CO + 256;
constexpr unsigned L2_RT = 52 * KV_TILE;
constexpr unsigned L3_WO = 4 * KV_TILE, L3_RED = L3_WO + 102400;
#define PRIO_M 3
#define EXP2(a) __builtin_amdgcn_exp2f(a)

DI f32x16 attn_exact(rsrc_t ws, u32x4 q1, u32x2 q2, int lane) {
  const int r32 = lane & 31, h = lane >> 5, rv = r32 < 21 ? r32 : 21;
  float mx = -3.0e38f;
#pragma unroll 1
  for (int t = 0; t < 64; ++t) {
    const unsigned base = W_KV + t * KV_TILE;
    f32x16 s = MFMA8(asbf4(bld64<AUX_SC1>(ws, base + KV_K2 + r32 * 8)), asbf4(q2), f32x16{});
    s = MFMA16(asbf8(bld128<AUX_SC1>(ws, base + lane * 16)), asbf8(q1), s);
#pragma unroll
    for (int j = 0; j < 16; ++j) mx = fmaxf(mx, s[j]);
  }
  mx = swap32_max(mx);
  f32x16 o = {};
#pragma unroll 1
  for (int t = 0; t < 64; ++t) {
    const unsigned base = W_KV + t * KV_TILE;
    f32x16 s = MFMA8(asbf4(bld64<AUX_SC1>(ws, base + KV_K2 + r32 * 8)), asbf4(q2), f32x16{});
    s = MFMA16(asbf8(bld128<AUX_SC1>(ws, base + lane * 16)), asbf8(q1), s);
    u32x4 pp[2];
#pragma unroll
    for (int j = 0; j < 8; ++j) pp[j >> 2][j & 3] = pk2(EXP2(s[2 * j] - mx), EXP2(s[2 * j + 1] - mx));
#pragma unroll
    for (int e = 0; e < 2; ++e) o = MFMA16(asbf8(bld128<AUX_SC1>(ws, base + KV_V + (2 * e + h) * KV_VS + rv * 16)), asbf8(pp[e]), o);
  }
  return o;
}

DI void phase_attn(lptr lds, rsrc_t ws, int b, const u32x4 q1, const u32x2 q2, unsigned epoch, float kmax2) {
  const int tid = threadIdx.x, lane = tid & 63, w = rfl(tid >> 6);
  const int r32 = lane & 31, h = lane >> 5;
  const int xcd = b & 7, rk = b >> 3, tg = 4 * xcd + (rk >> 3), hg = rk & 7;
  const int hh = w & 3, th = w >> 2;
  const unsigned lds0 = (unsigned)(uintptr_t)lds, l16 = lane * 16;
  if (tid < 10) *(LAS unsigned*)(lds + L2_CO + tid * 16) = 0u;
  f32x16 cb;
  {
    float n2 = 0.f;
#pragma unroll
    for (int j = 0; j < 4; ++j) n2 = dot2(q1[j], q1[j], n2);
    n2 = dot2(q2[0], q2[0], n2); n2 = dot2(q2[1], q2[1], n2);
    const float ref = sqrtf(swap32_sum(n2) * kmax2) * 1.001f;
#pragma unroll
    for (int j = 0; j < 16; ++j) cb[j] = -ref;
    PIN(cb);
  }
  const int rv = r32 < 21 ? r32 : 21;
  const unsigned oK1 = l16, oK2 = KV_K2 + r32 * 8, oV = KV_V + h * KV_VS + rv * 16;
  f32x16 o = {};
  LBAR();
  STAMPX(9);
  {
    lptr aK1 = lds + oK1; lptr aK2 = lds + oK2; lptr aV = lds + oV;
    u32x4 k1 = lds128(aK1); u32x2 k2 = lds64(aK2);
    f32x16 sc = MFMA8(asbf4(k2), asbf4(q2), cb); sc = MFMA16(asbf8(k1), asbf8(q1), sc);
    k1 = lds128(aK1 + KV_TILE); k2 = lds64(aK2 + KV_TILE);
    u32x4 v0 = lds128(aV), v1 = {}, p0, p1 = {};
    u32x4 k1b, v0b, v1b; u32x2 k2b;
    f32x16 sn;
#define GRP(MF, SC, i, P, j) do { MF; SBAR(); { const float e0 = EXP2(SC[i]), e1 = EXP2(SC[i + 1]), e2 = EXP2(SC[i + 2]), e3 = EXP2(SC[i + 3]); P[j] = pk2(e0, e1); P[j + 1] = pk2(e2, e3); } PIN(P); SBAR(); } while (0)
#define TILE(SC, SN, K1, K2, V0, V1, K1N, K2N, V0N, V1N, PK1, PK2, PV0, PV1) do { \
      WAITL(); \
      K2N = lds64(PK2); K1N = lds128(PK1); V0N = lds128(PV0); V1N = lds128(PV1); SBAR(); \
      GRP(SN = MFMA8(asbf4(K2), asbf4(q2), cb), SC, 0, p0, 0); \
      GRP(o = MFMA16(asbf8(V1), asbf8(p1), o), SC, 4, p0, 2); \
      GRP(SN = MFMA16(asbf8(K1), asbf8(q1), SN), SC, 8, p1, 0); \
      GRP(o = MFMA16(asbf8(V0), asbf8(p0), o), SC, 12, p1, 2); } while (0)
#define TA(T) TILE(sc, sn, k1, k2, v0, v1, k1b, k2b, v0b, v1b, aK1 + ((T) + 2) * KV_TILE, aK2 + ((T) + 2) * KV_TILE, aV + ((T) + 1) * KV_TILE, aV + (T) * KV_TILE + 2 * KV_VS)
#define TB(T) TILE(sn, sc, k1b, k2b, v0b, v1b, k1, k2, v0, v1, aK1 + ((T) + 2) * KV_TILE, aK2 + ((T) + 2) * KV_TILE, aV + ((T) + 1) * KV_TILE, aV + (T) * KV_TILE + 2 * KV_VS)
#define ADV() do { aK1 += 8 * KV_TILE; aK2 += 8 * KV_TILE; aV += 8 * KV_TILE; } while (0)
#define BODY8() do { TA(0); TB(1); TA(2); TB(3); TA(4); TB(5); TA(6); TB(7); ADV(); } while (0)
    if (th == 1) {
      const char* kvg = kargs()->ws + W_KV;
#define DMA3(c) do { dma_kv3(kvg, lds0, c, hh, lane); SBAR(); } while (0)
      __builtin_amdgcn_s_setprio(2);
      PIN(sc);
      DMA3(3); TA(0); DMA3(4); TB(1); DMA3(5); TA(2); DMA3(6); TB(3);
      DMA3(7); TA(4); DMA3(8); TB(5); DMA3(9); TA(6); DMA3(10); TB(7); ADV();
      if (lane == 0) lds_add1(lds + L2_CP);
      WAITV(6); if (lane == 0) lds_add1(lds + L2_CS);
      lds_wait_ge(lds + L2_CS, 4u);
      DMA3(11); TA(0); DMA3(12); TB(1); TA(2); TB(3); TA(4); TB(5); TA(6); TB(7); ADV();
      WAITV(0); if (lane == 0) lds_add1(lds + L2_CS + 16);
#pragma nounroll
      for (int m = 2; m < 6; ++m) {
        if (m == 4) { lds_wait_ge(lds + L2_CS + 16, 4u); lds_wait_ge(lds + L2_CR, 4u); DMA3(13); DMA3(14); }
        if (m == 5) { WAITV(0); if (lane == 0) lds_add1(lds + L2_CS + 32); lds_wait_ge(lds + L2_CP, 8u); DMA3(15); }
        if (m == PRIO_M) __builtin_amdgcn_s_setprio(0);
        BODY8();
      }
      WAITV(0); if (lane == 0) lds_add1(lds + L2_CS + 48);
#undef DMA3
    } else {
      __builtin_amdgcn_s_setprio(1);
      const int ga = b * 4 + hh;
      u32x4 rg[4];
      auto gu_load = [&](int gj) {
        kargs_t pa = kargs(); const int cgf = gj / 40, r = gj % 40;
        const rsrc_t wr = __builtin_amdgcn_make_buffer_rsrc((void*)((r & 1) ? pa->wu : pa->wg), 0, DM * FF * 2, 0x00020000);
        const unsigned vo = (32 * (r >> 1) + (lane >> 3)) * (FF * 2) + cgf * 128 + (lane & 7) * 16;
#pragma unroll
        for (int i = 0; i < 4; ++i) rg[i] = __builtin_amdgcn_raw_buffer_load_b128(wr, vo, i * 8 * FF * 2, 0);
      };
      auto gu_store = [&](int gj) {
        const int cgf = gj / 40, r = gj % 40, i16 = lane & 15, tq = i16 >> 2, tp = i16 & 3, g4 = lane >> 4;
        lptr scr = lds + L2_RT + hh * 4096;
#pragma unroll
        for (int i = 0; i < 4; ++i) *(LAS u32x4*)(scr + i * 1024 + l16) = rg[i];
#pragma unroll
        for (int u = 0; u < 4; ++u) { lptr a = scr + (8 * g4 + tq) * 128 + u * 32 + tp * 8;
          const u32x4 f = asu4(cat8(trread(a), trread(a + 512)));
          bst128<AUX_SC1>(ws, W_WGU + (((cgf * 4 + u) * 20 + (r >> 1)) * 2 + (r & 1)) * 1024 + l16, f);
          float cs = ((bflo(f[0]) + bfhi(f[0])) + (bflo(f[1]) + bfhi(f[1]))) + ((bflo(f[2]) + bfhi(f[2])) + (bflo(f[3]) + bfhi(f[3])));
          cs = swap32_sum(swap16_sum(cs));
          if (lane < 16) bst32<AUX_SC1>(ws, W_CSP + (((cgf * 4 + u) * 2 + (r & 1)) * 20 + (r >> 1)) * 64 + lane * 4, __float_as_uint(cs)); }
      };
      {
        kargs_t pa = kargs();
#pragma unroll
        for (int e = 0; e < 2; ++e) { const int gj = ga + 1024 * e;
          if (e == 0 || gj < 1080) { const int cgf = gj / 40, r = gj % 40; if (lane < 32) touch((32 * (r >> 1) + lane) * (FF * 2) + cgf * 128, (r & 1) ? pa->wu : pa->wg, lds0 + L2_DUMP); } }
        const int nl = ga < 986 ? 24 : (ga == 986 ? 16 : 0);
        if (lane < nl) touch(lane * 128, ga < 720 ? pa->wd + (size_t)ga * 3072 : pa->wo + (size_t)(ga - 720) * 3072, lds0 + L2_DUMP);
      }
      PIN(sc);
#pragma nounroll
      for (int m = 0; m < 6; ++m) {
        if (m == 1) {
          if (lane == 0) lds_add1(lds + L2_CP);
          u32x4 rd[3];
          gu_load(ga);
          {
            kargs_t pa = kargs();
#pragma unroll
            for (int i = 0; i < 3; ++i) { const int j = ga * 3 + i;
              const char* src = j < 2160 ? pa->wd + (size_t)j * 1024 : pa->wo + (size_t)(j < 2960 ? j - 2160 : 0) * 1024;
              rd[i] = *(const GAS u32x4*)(src + l16); }
          }
          gu_store(ga);
#pragma unroll
          for (int i = 0; i < 3; ++i) { const int j = ga * 3 + i;
            if (j < 2960) { const bool dn = j < 2160; const int a5 = (dn ? j : j - 2160) * 4 + (lane >> 4), f = (a5 * 52429) >> 18, c = (a5 - 5 * f) * 16 + (lane & 15), cg = (c * 6554) >> 16, cc = c - cg * 10;
              bst128<AUX_SC1>(ws, dn ? W_WD + (cg * FF + ((f & ~12) | ((f & 4) << 1) | ((f & 8) >> 1))) * 160 + cc * 16 : W_WO + (cg * DM + f) * 160 + cc * 16, rd[i]); } }
          if (ga < 56) { gu_load(ga + 1024); gu_store(ga + 1024); }
          WAITL(); if (lane == 0) lds_add1(lds + L2_CR);
          lds_wait_ge(lds + L2_CS, 4u);
        }
        if (m == 2) {
          WAITV(0);
          unsigned old = 0; if (lane == 0) old = __hip_atomic_fetch_add((LAS unsigned*)(lds + L2_CD), 1u, __ATOMIC_RELAXED, __HIP_MEMORY_SCOPE_WORKGROUP);
          if (lane == 0 && old == 3u) __hip_atomic_store((GAS unsigned*)(kargs()->ws + W_FRT) + b, epoch, RLX_AGENT);
        }
        if (m == 4) {
          char* wsp = kargs()->ws;
          wait_flags<4>(wsp, W_FRT, epoch, lane);
          if (lane == 0) lds_add1(lds + L2_CF);
          if (b == 0 && w == 0 && lane == 0) __hip_atomic_store((GAS unsigned*)(wsp + W_EPOCH), epoch, RLX_AGENT);
          const int ngl = rk & 3, u0 = 27 * (xcd & 3) + 7 * ngl, part = (rk >> 2) * 4 + hh;
          const int nl = (ngl == 3 ? 6 : 7) * 10;
          touch_sc1((part * nl + lane) * 128, wsp + W_WGU + (size_t)u0 * 40960, lds0 + L2_DUMP);
          if (lane < nl - 64) touch_sc1((part * nl + 64 + lane) * 128, wsp + W_WGU + (size_t)u0 * 40960, lds0 + L2_DUMP);
          if (lane < 50) touch_sc1((((rk >> 3) * 4 + hh) * 50 + lane) * 128, wsp + W_WO + hg * DM * 160, lds0 + L2_DUMP);
          lds_wait_ge(lds + L2_CS + 16, 4u);
        }
        BODY8();
      }
    }
    lds_wait_ge(lds + L2_CS + 32, 4u);
    TA(0); TB(1);
    if (lane == 0) lds_add1(lds + L2_CQ);
    TA(2); TB(3); TA(4); TB(5); TA(6); TB(7); ADV();
    lds_wait_ge(lds + L2_CQ, 8u); lds_wait_ge(lds + L2_CF, 1u);
    {
      const char* src = kargs()->ws + W_WO + hg * DM * 160 + w * 1024; unsigned dst = lds0 + L3_WO + w * 1024, vo = l16;
#pragma unroll 1
      for (int i = 0; i < 12; ++i) { glds_sc1(vo, src, dst); vo += 8192; dst += 8192; }
      if (w < 4) glds_sc1(vo, src, dst);
    }
    lds_wait_ge(lds + L2_CS + 48, 4u);
    {
      lptr zK1 = lds + oK1; lptr zK2 = lds + oK2; lptr zV = lds + oV;
      TA(0); TB(1);
      TILE(sc, sn, k1, k2, v0, v1, k1b, k2b, v0b, v1b, zK1, zK2, aV + 3 * KV_TILE, aV + 2 * KV_TILE + 2 * KV_VS);
      TILE(sn, sc, k1b, k2b, v0b, v1b, k1, k2, v0, v1, zK1 + KV_TILE, zK2 + KV_TILE, zV, aV + 3 * KV_TILE + 2 * KV_VS);
      aK1 = zK1; aK2 = zK2; aV = zV;
      TA(0); TB(1); TA(2); TB(3);
    }
    WAITL();
    o = MFMA16(asbf8(v1), asbf8(p1), o);
    __builtin_amdgcn_s_setprio(0);
  }
  STAMPX(10);
  float lsum = __uint_as_float(__builtin_amdgcn_permlane32_swap(__float_as_uint(o[8]), __float_as_uint(o[8]), false, false)[1]);
  if (__builtin_expect(__any(!(lsum >= 5.421010862427522e-20f)), 0)) {
    o = attn_exact(ws, q1, q2, lane);
    lsum = __uint_as_float(__builtin_amdgcn_permlane32_swap(__float_as_uint(o[8]), __float_as_uint(o[8]), false, false)[1]);
  }
  {
    const float inv = __builtin_amdgcn_rcpf(lsum);
    const u32x4 oa = {pk2(o[0] * inv, o[1] * inv), pk2(o[2] * inv, o[3] * inv), pk2(o[4] * inv, o[5] * inv), pk2(o[6] * inv, o[7] * inv)};
    const u32x2 ob = {pk2(o[8] * inv, o[9] * inv), pk2(o[10] * inv, o[11] * inv)};
    const unsigned tile16 = tg * 4 + th * 2 + (r32 >> 4), g0 = 10 * hg + 5 * (hh >> 1);
    bst128<AUX_SC1>(ws, W_OF + ((tile16 * 80 + g0 + 2 * (hh & 1) + h) * 16 + (r32 & 15)) * 16, oa);
    if (h == 0) bst64<AUX_SC1>(ws, W_OF + ((tile16 * 80 + g0 + 4) * 16 + (r32 & 15)) * 16 + 8 * (hh & 1), ob);
  }
  WAITV(0);
  LBAR();
  if (tid == 0) (void)__hip_atomic_fetch_add((GAS unsigned*)(kargs()->ws + W_CNTO + tg * 256), 1u, RLX_AGENT);
}

DI void phase_oproj(lptr lds, rsrc_t ws, int b, u32x2 (&hres)[5], unsigned epoch) {
  kargs_t pa = kargs();
  const int tid = threadIdx.x, lane = tid & 63, w = rfl(tid >> 6);
  const int xcd = b & 7, rk = b >> 3, tg = 4 * xcd + (rk >> 3), cg = rk & 7;
  const int mt = w & 3, kh = w >> 2, t16 = lane & 15, g = lane >> 4, tq = t16 >> 2, tp = t16 & 3;
  const int tok = tg * 64 + mt * 16 + t16;
  u32x2 xr[5] = {};
  if (kh == 0) {
#pragma unroll
    for (int nt = 0; nt < 5; ++nt) xr[nt] = *(const GAS u32x2*)(pa->x + (size_t)tok * 1280 + cg * 160 + nt * 32 + g * 8);
  }
  unsigned r1[10], r2[10];
#pragma unroll
  for (int i = 0; i < 10; ++i) {
    const int t = 4 * i + g, a5 = (t * 13) >> 6, m = t - 5 * a5, pair = 8 * kh + a5;
    const int last = m >> 2, ra = pair * 40 + (m >> 1) * 20 + (m & 1) * 4 - last * 24;
    r1[i] = L3_WO + (ra + tq) * 160 + tp * 8; r2[i] = r1[i] + (8 + 12 * last) * 160;
  }
  wait_cnt2(pa->ws, W_CNTO + tg * 256, W_CNTO + tg * 256, 8u, epoch, 0);
  STAMPX(13);
  bf16x8 bf[2][5];
  u32x4 a[10];
#pragma unroll
  for (int i = 0; i < 10; ++i) a[i] = bld128<AUX_SC1>(ws, W_OF + ((tg * 4 + mt) * 80 + 4 * (10 * kh + i)) * 256 + lane * 16);
#pragma unroll
  for (int nt = 0; nt < 5; ++nt) bf[0][nt] = cat8(trread(lds + r1[0] + nt * 32), trread(lds + r2[0] + nt * 32));
  f32x4 acc[5] = {};
#pragma unroll
  for (int i = 0; i < 10; ++i) {
    if (i + 1 < 10) {
#pragma unroll
      for (int nt = 0; nt < 5; ++nt) bf[(i + 1) & 1][nt] = cat8(trread(lds + r1[i + 1] + nt * 32), trread(lds + r2[i + 1] + nt * 32));
    }
    SBAR();
#pragma unroll
    for (int nt = 0; nt < 5; ++nt) acc[nt] = MFMAS(bf[i & 1][nt], asbf8(a[i]), acc[nt]);
    SBAR();
  }
  STAMPX(14);
  LAS f32x4* red = (LAS f32x4*)(lds + L3_RED) + mt * 320 + lane;
  if (kh == 1) {
#pragma unroll
    for (int nt = 0; nt < 5; ++nt) red[nt * 64] = acc[nt];
  }
  LBAR();
  if (kh == 0) {
    float s1 = 0.f, s2 = 0.f;
#pragma unroll
    for (int nt = 0; nt < 5; ++nt) {
      const f32x4 o = red[nt * 64];
      const float h0 = bflo(xr[nt][0]) + acc[nt][0] + o[0], h1 = bfhi(xr[nt][0]) + acc[nt][1] + o[1], h2 = bflo(xr[nt][1]) + acc[nt][2] + o[2], h3 = bfhi(xr[nt][1]) + acc[nt][3] + o[3];
      const u32x2 hv = {pk2(h0, h1), pk2(h2, h3)};
      hres[nt] = hv;
      s1 = dot2(hv[0], ONES2, s1); s1 = dot2(hv[1], ONES2, s1); s2 = dot2(hv[0], hv[0], s2); s2 = dot2(hv[1], hv[1], s2);
      const int gam = 10 * cg + 2 * nt + (g >> 1);
      bst64<AUX_SC1>(ws, W_HF + (((tg * 4 + mt) * 20 + (gam >> 2)) * 64 + (gam & 3) * 16 + t16) * 16 + (g & 1) * 8, hv);
    }
    s1 = swap32_sum(swap16_sum(s1)); s2 = swap32_sum(swap16_sum(s2));
    if (g == 0) bst64<AUX_SC1>(ws, W_HST + ((tg * 8 + cg) * 64 + mt * 16 + t16) * 8, u32x2{__float_as_uint(s1), __float_as_uint(s2)});
  }
  signal_cnt(kargs()->ws, W_CNTH + tg * 256);
}

constexpr unsigned L4_S = 0  , L4_ST = 122880  , L4_CO = 163568;
DI constexpr unsigned l5_slot(int c) { return (c & 3) == 0 ? 123904u : ((c & 3) == 1 ? 40960u : ((c & 3) == 2 ? 71680u : 0u)); }
constexpr unsigned L5_RED = 102400;
static_assert(123904 + 30720 <= L4_CO, "lds");
DI void dma_wd_chunk(const char* wd, unsigned lds0, int w, int lane, int c) {
  const unsigned l16 = lane * 16;
#pragma unroll
  for (int i = 0; i < 4; ++i) { int n = w + 8 * i; const bool dup = i == 3 && n >= 30; if (dup) n -= 8; const int k2 = n >= 15, j = n - 15 * k2;
    if (i < 3 || !dup || lane == 0) glds_sc1(l16 + j * 1024 + k2 * (864 * 160), wd + (size_t)c * (96 * 160), lds0 + l5_slot(c) + n * 1024); }
}
DI void dma_wd_chunk7(const char* wd, unsigned lds0, int w, int lane, int c) {
  const unsigned l16 = lane * 16;
#pragma unroll
  for (int i = 0; i < 5; ++i) { int n = w + 7 * i; const bool dup = i == 4 && n >= 30; if (dup) n -= 7; const int k2 = n >= 15, j = n - 15 * k2;
    if (i < 4 || !dup || lane == 0) glds_sc1(l16 + j * 1024 + k2 * (864 * 160), wd + (size_t)c * (96 * 160), lds0 + l5_slot(c) + n * 1024); }
}
constexpr int GWIN = 10;
DI void phase_gateup(lptr lds, rsrc_t ws, int b, unsigned epoch) {
  char* wsp = kargs()->ws;
  const int tid = threadIdx.x, lane = tid & 63, w = rfl(tid >> 6);
  const int xcd = b & 7, rk = b >> 3, mb = 8 * (xcd >> 2) + (rk >> 2), ngl = rk & 3, u0 = 27 * (xcd & 3) + 7 * ngl, nu = ngl == 3 ? 6 : 7;
  const int t16 = lane & 15, g = lane >> 4;
  const unsigned lds0 = (unsigned)(uintptr_t)lds, l16 = lane * 16;
  const char* wd = wsp + W_WD + (size_t)(rk & 7) * FF * 160;
  const bool act = w < nu;
  const int u = u0 + (act ? w : 0);
  const unsigned wbase = W_WGU + u * 40960 + l16;
  constexpr int GPRE = 3;
  u32x4 ag[GWIN], au[GWIN];
  float csp[2][5] = {};
  if (act) {
#pragma unroll
    for (int i = 0; i < GPRE; ++i) { ag[i] = bld128<AUX_SC1>(ws, wbase + i * 2048); au[i] = bld128<AUX_SC1>(ws, wbase + i * 2048 + 1024); }
#pragma unroll
    for (int m = 0; m < 2; ++m)
#pragma unroll
      for (int i = 0; i < 5; ++i) csp[m][i] = __uint_as_float(bld32<AUX_SC1>(ws, W_CSP + ((u * 2 + m) * 20 + 5 * g + i) * 64 + t16 * 4));
  }
  SBAR();
  wait_cnt2(wsp, W_CNTH + (2 * mb) * 256, W_CNTH + (2 * mb + 1) * 256, 8u, epoch, 7);
  STAMPX(16);
  const char* hsrc = wsp + W_HF + (size_t)(mb * 8 + w) * 20 * 1024;
  auto dma_h = [&](int sc, int slot) {
#pragma unroll
    for (int i = 0; i < 5; ++i) glds_sc1(l16 + (5 * sc + i) * 1024, hsrc, lds0 + L4_S + slot * 40960 + i * 8192 + w * 1024);
  };
  if (tid == 0) *(LAS unsigned*)(lds + L4_CO) = 0u;
  u32x2 sv[8];
  if (tid < 128) {
#pragma unroll
    for (int c = 0; c < 8; ++c) sv[c] = bld64<AUX_SC1>(ws, W_HST + (((2 * mb + (tid >> 6)) * 8 + c) * 64 + (tid & 63)) * 8);
  }
  dma_h(0, 0);
  if (act) {
#pragma unroll
    for (int i = GPRE; i < GWIN; ++i) { ag[i] = bld128<AUX_SC1>(ws, wbase + i * 2048); au[i] = bld128<AUX_SC1>(ws, wbase + i * 2048 + 1024); }
  }
  SBAR();
  dma_h(1, 1); dma_h(2, 2);
  if (tid < 128) {
    float s1 = 0.f, s2 = 0.f;
#pragma unroll
    for (int c = 0; c < 8; ++c) { s1 += __uint_as_float(sv[c][0]); s2 += __uint_as_float(sv[c][1]); }
    const float mean = s1 * (1.f / DM), rstd = rsqrtf(fmaxf(s2 * (1.f / DM) - mean * mean, 0.f) + LN_EPS);
    *(LAS f32x2*)(lds + L4_ST + tid * 8) = f32x2{mean, rstd};
  }
  if (act) WAITV(24); else WAITV(10);
  LBAR();
  STAMPX(17);
  if (act) {
    f32x4 aG[8] = {}, aU[8] = {};
    lptr hb = lds + L4_S + l16;
    u32x4 bb[2][4];
#pragma unroll
    for (int t = 0; t < 4; ++t) bb[0][t] = lds128(hb + t * 1024);
#pragma unroll
    for (int s = 0; s < 20; ++s) {
      const int slot = (s / 5) % 3, nslot = ((s + 1) / 5) % 3;
      if (s == 4) { WAITV(13); LBAR(); }
      if (s == 5) dma_wd_chunk(wd, lds0, w, lane, 0);
      if (s == 9) { WAITV(22); LBAR(); dma_h(3, 0); }
      if (s == 14) { WAITV(2); LBAR(); dma_wd_chunk(wd, lds0, w, lane, 1); }
      if (s == 15) { BAR(); dma_wd_chunk(wd, lds0, w, lane, 2); }
      const u32x4 cg_ = ag[s % GWIN], cu_ = au[s % GWIN];
      if (s + GWIN < 20) { ag[s % GWIN] = bld128<AUX_SC1>(ws, wbase + (s + GWIN) * 2048); au[s % GWIN] = bld128<AUX_SC1>(ws, wbase + (s + GWIN) * 2048 + 1024); }
#pragma unroll
      for (int t = 0; t < 4; ++t) bb[1][t] = lds128(hb + slot * 40960 + (s % 5) * 8192 + (4 + t) * 1024);
      SBAR();
#pragma unroll
      for (int t = 0; t < 4; ++t) { aG[t] = MFMAS(asbf8(cg_), asbf8(bb[0][t]), aG[t]); aU[t] = MFMAS(asbf8(cu_), asbf8(bb[0][t]), aU[t]); }
      SBAR();
      if (s + 1 < 20) {
#pragma unroll
        for (int t = 0; t < 4; ++t) bb[0][t] = lds128(hb + nslot * 40960 + ((s + 1) % 5) * 8192 + t * 1024);
      }
      SBAR();
#pragma unroll
      for (int t = 0; t < 4; ++t) { aG[4 + t] = MFMAS(asbf8(cg_), asbf8(bb[1][t]), aG[4 + t]); aU[4 + t] = MFMAS(asbf8(cu_), asbf8(bb[1][t]), aU[4 + t]); }
      SBAR();
    }
    STAMPX(18);
    const float sg = swap32_sum(swap16_sum(((csp[0][0] + csp[0][1]) + (csp[0][2] + csp[0][3])) + csp[0][4])), su = swap32_sum(swap16_sum(((csp[1][0] + csp[1][1]) + (csp[1][2] + csp[1][3])) + csp[1][4]));
    float cg4[4], cu4[4];
#pragma unroll
    for (int j = 0; j < 4; ++j) { cg4[j] = __shfl(sg, 4 * g + j); cu4[j] = __shfl(su, 4 * g + j); }
#pragma unroll
    for (int i = 0; i < 4; ++i) {
      u32x2 pk[2];
#pragma unroll
      for (int e = 0; e < 2; ++e) {
        const int t = 2 * i + e;
        const f32x2 mr = *(LAS f32x2*)(lds + L4_ST + (t * 16 + t16) * 8);
        const float r2 = mr[1] * mr[1];
        float hv[4];
#pragma unroll
        for (int j = 0; j < 4; ++j) hv[j] = (aG[t][j] - mr[0] * cg4[j]) * (aU[t][j] - mr[0] * cu4[j]) * r2;
        pk[e] = u32x2{pk2(hv[0], hv[1]), pk2(hv[2], hv[3])};
      }
      const auto s0 = __builtin_amdgcn_permlane16_swap(pk[0][0], pk[1][0], false, false), s1 = __builtin_amdgcn_permlane16_swap(pk[0][1], pk[1][1], false, false);
      bst128<AUX_SC1>(ws, W_GF + (((mb * 8 + 2 * i + (g & 1)) * 54 + (u >> 1)) * 64 + (2 * (u & 1) + (g >> 1)) * 16 + t16) * 16, u32x4{s0[0], s1[0], s0[1], s1[1]});
    }
  } else {
    WAITV(5); LBAR();
    dma_wd_chunk(wd, lds0, w, lane, 0);
    WAITV(4); LBAR(); dma_h(3, 0);
    WAITV(0); LBAR(); dma_wd_chunk(wd, lds0, w, lane, 1);
    BAR(); dma_wd_chunk(wd, lds0, w, lane, 2);
  }
  STAMPX(21);
  BAR();
  if (w < 7) dma_wd_chunk7(wd, lds0, w, lane, 3);
  if (w < 7) WAITV(5); else WAITV(0);
  unsigned old = 0; if (lane == 0) old = __hip_atomic_fetch_add((LAS unsigned*)(lds + L4_CO), 1u, __ATOMIC_RELAXED, __HIP_MEMORY_SCOPE_WORKGROUP);
  if (lane == 0 && old == 7u) (void)__hip_atomic_fetch_add((GAS unsigned*)(wsp + W_CNTG + mb * 256), 1u, RLX_AGENT);
}

DI void gld128_sc1(u32x4& d, unsigned voff, const char* sbase) { asm volatile("global_load_dwordx4 %0, %1, %2 sc1" : "=v"(d) : "v"(voff), "s"(sbase) : "memory"); }
DI void phase_down(lptr lds, rsrc_t ws, int b, const u32x2 (&hres)[5], unsigned epoch) {
  char* wsp = kargs()->ws;
  const int tid = threadIdx.x, lane = tid & 63, w = rfl(tid >> 6);
  const int xcd = b & 7, rk = b >> 3, tg = 4 * xcd + (rk >> 3), cg = rk & 7;
  const int mt = w & 3, kh = w >> 2, t16 = lane & 15, g = lane >> 4, tq = t16 >> 2, tp = t16 & 3;
  const unsigned lds0 = (unsigned)(uintptr_t)lds, l16 = lane * 16;
  const char* wd = wsp + W_WD + (size_t)cg * FF * 160;
  wait_cnt2(wsp, W_CNTG + (tg >> 1) * 256, W_CNTG + (tg >> 1) * 256, 16u, epoch, 7);
  STAMPX(20);
  const char* abase = wsp + W_GF + (size_t)((tg * 4 + mt) * 54 + 27 * kh) * 1024;
  u32x4 a[4][3];
#pragma unroll
  for (int c = 0; c < 3; ++c)
#pragma unroll
    for (int k = 0; k < 3; ++k) gld128_sc1(a[c][k], l16 + (3 * c + k) * 1024, abase);
  f32x4 acc[5] = {};
  const unsigned brow = kh * 15360 + (16 * (g >> 1) + 4 * (g & 1) + tq) * 160 + tp * 8;
#define DOWN_WAIT(N, c) asm volatile("s_waitcnt vmcnt(" #N ")\n\ts_barrier" : "+v"(a[(c) & 3][0]), "+v"(a[(c) & 3][1]), "+v"(a[(c) & 3][2]) :: "memory")
  bf16x8 bf[2][5];
  { lptr bb = lds + l5_slot(0) + brow;
#pragma unroll
    for (int nt = 0; nt < 5; ++nt) bf[0][nt] = cat8(trread(bb + nt * 32), trread(bb + nt * 32 + 1280)); }
#pragma unroll
  for (int c = 0; c < 9; ++c) {
    asm volatile("" ::: "memory"); WAITL();
    if (c <= 1) DOWN_WAIT(6, c); else if (c <= 6) DOWN_WAIT(10, c); else if (c == 7) DOWN_WAIT(3, c); else DOWN_WAIT(0, c);
    if (c >= 1 && c + 3 < 9) dma_wd_chunk(wd, lds0, w, lane, c + 3);
    if (c + 3 < 9) {
#pragma unroll
      for (int k = 0; k < 3; ++k) gld128_sc1(a[(c + 3) & 3][k], l16 + (3 * (c + 3) + k) * 1024, abase); }
#pragma unroll
    for (int k = 0; k < 3; ++k) {
      const int sn = 3 * c + k + 1;
      if (sn < 27) { lptr bb = lds + l5_slot(sn / 3) + brow + (sn % 3) * 5120;
#pragma unroll
        for (int nt = 0; nt < 5; ++nt) bf[sn & 1][nt] = cat8(trread(bb + nt * 32), trread(bb + nt * 32 + 1280)); }
      SBAR();
#pragma unroll
      for (int nt = 0; nt < 5; ++nt) acc[nt] = MFMAS(bf[(3 * c + k) & 1][nt], asbf8(a[c & 3][k]), acc[nt]);
      SBAR();
    }
  }
  STAMPX(22);
  LAS f32x4* red = (LAS f32x4*)(lds + L5_RED) + mt * 320 + lane;
  if (kh == 1) {
#pragma unroll
    for (int nt = 0; nt < 5; ++nt) red[nt * 64] = acc[nt];
  }
  LBAR();
  if (kh == 0) {
    const int tok = tg * 64 + mt * 16 + t16; char* outp = kargs()->out;
#pragma unroll
    for (int nt = 0; nt < 5; ++nt) {
      const f32x4 o = red[nt * 64];
      const u32x2 r = {pk2(bflo(hres[nt][0]) + acc[nt][0] + o[0], bfhi(hres[nt][0]) + acc[nt][1] + o[1]), pk2(bflo(hres[nt][1]) + acc[nt][2] + o[2], bfhi(hres[nt][1]) + acc[nt][3] + o[3])};
      *(GAS u32x2*)(outp + (size_t)tok * 1280 + cg * 160 + nt * 32 + g * 8) = r;
    }
  }
}

#define CODE_BYTES 46080
__global__ void __launch_bounds__(NTHR, 2) k_block(Params p) {
  extern __shared__ __attribute__((aligned(16))) char smem[];
  lptr lds = (lptr)smem;
  const rsrc_t ws = __builtin_amdgcn_make_buffer_rsrc((void*)p.ws, 0, (int)W_END, 0x00020000);
  const int b = blockIdx.x;
  {
    unsigned long long pc; asm volatile("s_getpc_b64 %0" : "=s"(pc));
    const int ln = (b >> 3) * 16 + (threadIdx.x & 15);
    if (threadIdx.x >= 448 && (threadIdx.x & 63) < 16 && ln * 128 < CODE_BYTES) touch(ln * 128, (const char*)(pc & ~127ull), (unsigned)(uintptr_t)smem + 163584);
  }
  u32x4 q1; u32x2 q2; unsigned epoch; float kmax2 = 0.f;
  u32x2 hres[5];
  STAMPX(0);
  phase1(lds, ws, b, q1, q2, epoch, kmax2);
  STAMPX(5);
  phase_attn(lds, ws, b, q1, q2, epoch, kmax2);
  STAMPX(12);
  phase_oproj(lds, ws, b, hres, epoch);
  STAMPX(15);
  phase_gateup(lds, ws, b, epoch);
  STAMPX(19);
  phase_down(lds, ws, b, hres, epoch);
  STAMPX(23);
}

extern "C" void kernel_launch(void* const* d_in, const int* in_sizes, int n_in, void* d_out, int out_size, void* d_ws, size_t ws_size, hipStream_t stream) {
  Params p;
  p.x = (const char*)d_in[0]; p.wq = (const char*)d_in[1]; p.wk = (const char*)d_in[2]; p.wv = (const char*)d_in[3]; p.wo = (const char*)d_in[4];
  p.wg = (const char*)d_in[6]; p.wu = (const char*)d_in[7]; p.wd = (const char*)d_in[8];
  p.out = (char*)d_out; p.ws = (char*)d_ws;
  constexpr int LDS_BYTES = 163840;
  (void)hipFuncSetAttribute((const void*)k_block, hipFuncAttributeMaxDynamicSharedMemorySize, LDS_BYTES);
  k_block<<<NWG, NTHR, LDS_BYTES, stream>>>(p);
}
```

```cpp
#include <hip/hip_runtime.h>
#include <stdint.h>

#define DI __device__ __forceinline__
#define LAS __attribute__((address_space(3)))
#define GAS __attribute__((address_space(1)))
typedef short bf16x8 __attribute__((ext_vector_type(8)));
typedef short bf16x4 __attribute__((ext_vector_type(4)));
typedef __bf16 bfv2 __attribute__((ext_vector_type(2)));
typedef float f32x2 __attribute__((ext_vector_type(2)));
typedef float f32x4 __attribute__((ext_vector_type(4)));
typedef float f32x16 __attribute__((ext_vector_type(16)));
typedef unsigned u32x2 __attribute__((ext_vector_type(2)));
typedef unsigned u32x4 __attribute__((ext_vector_type(4)));
typedef LAS char* lptr;

constexpr int SEQ = 2048, DM = 640, FF = 1728;
constexpr int NWG = 256, NTHR = 512;
constexpr float LN_EPS = 1e-5f;
constexpr float QSCALE = 0.22360679774997896f * 1.4426950408889634f;
constexpr unsigned KV_TILE = 2688, KV_K2 = 1024, KV_V = 1280, KV_VS = 352;

constexpr unsigned W_EPOCH = 0, W_TMO = 256;
constexpr unsigned W_FKV = 1024  , W_FRT = 2048  ;
constexpr unsigned W_CNTO = 8192, W_CNTH = 16384, W_CNTG = 24576;
constexpr unsigned W_KV = 65536;
constexpr unsigned W_OF = W_KV + 64 * KV_TILE;
constexpr unsigned W_HF = W_OF + SEQ * DM * 2;
constexpr unsigned W_HST = W_HF + SEQ * DM * 2;
constexpr unsigned W_GF = W_HST + 32 * 8 * 64 * 8;
constexpr unsigned W_WGU = W_GF + 128 * 54 * 1024;
constexpr unsigned W_WD = W_WGU + 108 * 40 * 1024;
constexpr unsigned W_WO = W_WD + 8 * FF * 160;
constexpr unsigned W_CSP = W_WO + 8 * DM * 160;
constexpr unsigned W_END = W_CSP + 108 * 2 * 20 * 64;

#define STAMPW() do { } while (0)
#define STAMP(i) do { } while (0)
#define STW 0
#define STAMPX(i) do { STAMP(i); if (STW == (i)) STAMPW(); } while (0)

struct Params {
  const char* x; const char* wq; const char* wk; const char* wv; const char* wo;
  const char* wg; const char* wu; const char* wd;
  char* out; char* ws;
};

typedef const __attribute__((address_space(4))) Params* kargs_t;
DI kargs_t kargs() { kargs_t a = (kargs_t)__builtin_amdgcn_kernarg_segment_ptr(); asm volatile("" : "+s"(a)); return a; }
constexpr int AUX_SC1 = 16;
#define MFMA16(a, b, c) __builtin_amdgcn_mfma_f32_32x32x16_bf16((a), (b), (c), 0, 0, 0)
#define MFMA8(a, b, c) __builtin_amdgcn_mfma_f32_32x32x8bf16_1k((a), (b), (c), 0, 0, 0)
#define MFMAS(a, b, c) __builtin_amdgcn_mfma_f32_16x16x32_bf16((a), (b), (c), 0, 0, 0)
#define SBAR() __builtin_amdgcn_sched_barrier(0)
#define WAITV(n) asm volatile("s_waitcnt vmcnt(" #n ")" ::: "memory")
#define BAR() asm volatile("s_barrier" ::: "memory")
#define WAITL() __builtin_amdgcn_s_waitcnt(0xC07F)
#define LBAR() do { asm volatile("" ::: "memory"); WAITL(); BAR(); } while (0)
#define PIN(x) asm volatile("" : "+v"(x))

DI unsigned pk2(float lo, float hi) { f32x2 v = {lo, hi}; return __builtin_bit_cast(unsigned, __builtin_convertvector(v, bfv2)); }
DI float bflo(unsigned v) { return __uint_as_float(v << 16); }
DI float bfhi(unsigned v) { return __uint_as_float(v & 0xffff0000u); }
DI float dot2(unsigned a, unsigned b, float c) { return __builtin_amdgcn_fdot2_f32_bf16(__builtin_bit_cast(bfv2, a), __builtin_bit_cast(bfv2, b), c, false); }
constexpr unsigned ONES2 = 0x3f803f80u;
DI float swap32_sum(float v) { auto r = __builtin_amdgcn_permlane32_swap(__float_as_uint(v), __float_as_uint(v), false, false); return __uint_as_float(r[0]) + __uint_as_float(r[1]); }
DI float swap32_max(float v) { auto r = __builtin_amdgcn_permlane32_swap(__float_as_uint(v), __float_as_uint(v), false, false); return fmaxf(__uint_as_float(r[0]), __uint_as_float(r[1])); }
DI float swap16_sum(float v) { auto r = __builtin_amdgcn_permlane16_swap(__float_as_uint(v), __float_as_uint(v), false, false); return __uint_as_float(r[0]) + __uint_as_float(r[1]); }
template <int CTRL> DI float dppf(float v) { return __uint_as_float(__builtin_amdgcn_update_dpp(0u, __float_as_uint(v), CTRL, 0xf, 0xf, true)); }
DI float sum8(float v) { v += dppf<0xB1>(v); v += dppf<0x4E>(v); return v + dppf<0x141>(v); }
DI float max16(float v) { v = fmaxf(v, dppf<0x128>(v)); v = fmaxf(v, dppf<0x124>(v)); v = fmaxf(v, dppf<0x122>(v)); return fmaxf(v, dppf<0x121>(v)); }
DI float max64(float v) { v = max16(v); auto r = __builtin_amdgcn_permlane16_swap(__float_as_uint(v), __float_as_uint(v), false, false);
  return swap32_max(fmaxf(__uint_as_float(r[0]), __uint_as_float(r[1]))); }
typedef short v4i16_t __attribute__((ext_vector_type(4)));
DI bf16x4 trread(lptr p) { return __builtin_bit_cast(bf16x4, __builtin_amdgcn_ds_read_tr16_b64_v4i16((LAS v4i16_t*)p)); }
DI bf16x8 cat8(bf16x4 a, bf16x4 b) { return __builtin_shufflevector(a, b, 0, 1, 2, 3, 4, 5, 6, 7); }
DI u32x4 lds128(lptr p) { return *(LAS u32x4*)p; }
DI u32x2 lds64(lptr p) { return *(LAS u32x2*)p; }
DI bf16x8 asbf8(u32x4 v) { return __builtin_bit_cast(bf16x8, v); }
DI bf16x4 asbf4(u32x2 v) { return __builtin_bit_cast(bf16x4, v); }
DI u32x4 asu4(bf16x8 v) { return __builtin_bit_cast(u32x4, v); }
DI unsigned rfl(unsigned v) { return __builtin_amdgcn_readfirstlane(v); }
typedef __amdgpu_buffer_rsrc_t rsrc_t;
template <int AUX> DI u32x4 bld128(rsrc_t r, unsigned off) { return __builtin_amdgcn_raw_buffer_load_b128(r, off, 0, AUX); }
template <int AUX> DI u32x2 bld64(rsrc_t r, unsigned off) { return __builtin_amdgcn_raw_buffer_load_b64(r, off, 0, AUX); }
template <int AUX> DI unsigned bld32(rsrc_t r, unsigned off) { return __builtin_amdgcn_raw_buffer_load_b32(r, off, 0, AUX); }
template <int AUX> DI void bst128(rsrc_t r, unsigned off, u32x4 v) { __builtin_amdgcn_raw_buffer_store_b128(v, r, off, 0, AUX); }
template <int AUX> DI void bst64(rsrc_t r, unsigned off, u32x2 v) { __builtin_amdgcn_raw_buffer_store_b64(v, r, off, 0, AUX); }
template <int AUX> DI void bst32(rsrc_t r, unsigned off, unsigned v) { __builtin_amdgcn_raw_buffer_store_b32(v, r, off, 0, AUX); }
DI void glds(unsigned voff, const char* sbase, unsigned lds_base) {
  asm volatile("s_mov_b32 m0, %2\n\ts_nop 0\n\tglobal_load_lds_dwordx4 %0, %1" :: "v"(voff), "s"(sbase), "s"(rfl(lds_base)) : "memory");
}
DI void glds_sc1(unsigned voff, const char* sbase, unsigned lds_base) {
  asm volatile("s_mov_b32 m0, %2\n\ts_nop 0\n\tglobal_load_lds_dwordx4 %0, %1 sc1" :: "v"(voff), "s"(sbase), "s"(rfl(lds_base)) : "memory");
}
DI void touch(unsigned voff, const char* sbase, unsigned lds_dump) {
  asm volatile("s_mov_b32 m0, %2\n\ts_nop 0\n\tglobal_load_lds_dword %0, %1" :: "v"(voff), "s"(sbase), "s"(rfl(lds_dump)) : "memory");
}
DI void touch_sc1(unsigned voff, const char* sbase, unsigned lds_dump) {
  asm volatile("s_mov_b32 m0, %2\n\ts_nop 0\n\tglobal_load_lds_dword %0, %1 sc1" :: "v"(voff), "s"(sbase), "s"(rfl(lds_dump)) : "memory");
}

#define RLX_AGENT __ATOMIC_RELAXED, __HIP_MEMORY_SCOPE_AGENT
constexpr unsigned SPIN_CAP = 1u << 17;
DI bool spin_fail(unsigned& spins, char* wsp, unsigned epoch) {
  __builtin_amdgcn_s_sleep(1);
  GAS unsigned* tmo = (GAS unsigned*)(wsp + W_TMO);
  if (++spins > SPIN_CAP) { __hip_atomic_store(tmo, epoch, RLX_AGENT); return true; }
  return (spins & 255u) == 0u && rfl(__hip_atomic_load(tmo, RLX_AGENT)) == epoch;
}
template <int NW> DI void wait_flags(char* wsp, unsigned off, unsigned epoch, int lane) {
  for (unsigned spins = 0;;) {
    bool ok = true;
#pragma unroll
    for (int i = 0; i < NW; ++i) ok &= __hip_atomic_load((GAS unsigned*)(wsp + off) + i * 64 + lane, RLX_AGENT) == epoch;
    if (__all(ok)) break;
    if (spin_fail(spins, wsp, epoch)) break;
  }
  __builtin_amdgcn_fence(__ATOMIC_ACQUIRE, "wavefront");
}
DI float wait_granules(char* wsp, unsigned off, unsigned epoch, int lane) {
  unsigned mx;
  for (unsigned spins = 0;;) {
    const unsigned long long a = __hip_atomic_load((GAS unsigned long long*)(wsp + off) + lane, RLX_AGENT), b = __hip_atomic_load((GAS unsigned long long*)(wsp + off) + 64 + lane, RLX_AGENT);
    mx = max((unsigned)a, (unsigned)b);
    if (__all((unsigned)(a >> 32) == epoch && (unsigned)(b >> 32) == epoch)) break;
    if (spin_fail(spins, wsp, epoch)) break;
  }
  __builtin_amdgcn_fence(__ATOMIC_ACQUIRE, "wavefront");
  return __uint_as_float(mx);
}
DI void wait_cnt2(char* wsp, unsigned off0, unsigned off1, unsigned want, unsigned epoch, int poller) {
  if ((int)(threadIdx.x >> 6) == poller) {
    GAS unsigned* word = (GAS unsigned*)(wsp + ((threadIdx.x & 1) ? off1 : off0));
    for (unsigned spins = 0;;) {
      if (__all(__hip_atomic_load(word, RLX_AGENT) >= want)) break;
      if (spin_fail(spins, wsp, epoch)) break;
    }
    __builtin_amdgcn_fence(__ATOMIC_ACQUIRE, "wavefront");
  }
  LBAR();
}
DI void signal_cnt(char* wsp, unsigned off) {
  WAITV(0);
  LBAR();
  if (threadIdx.x == 0) (void)__hip_atomic_fetch_add((GAS unsigned*)(wsp + off), 1u, RLX_AGENT);
}
DI void lds_add1(lptr p) { (void)__hip_atomic_fetch_add((LAS unsigned*)p, 1u, __ATOMIC_RELAXED, __HIP_MEMORY_SCOPE_WORKGROUP); }
DI void lds_wait_ge(lptr p, unsigned want) {
  while ((unsigned)rfl(__hip_atomic_load((LAS unsigned*)p, __ATOMIC_RELAXED, __HIP_MEMORY_SCOPE_WORKGROUP)) < want) __builtin_amdgcn_s_sleep(2);
  asm volatile("" ::: "memory");
}

DI void dma_kv3(const char* kvg, unsigned lds0, int c, int j, int lane) {
  const unsigned dst = lds0 + (c == 15 ? 0 : c) * 4 * KV_TILE, l16 = lane * 16; const char* src = kvg + c * 4 * KV_TILE;
  glds_sc1(l16, src + j * 1024, dst + j * 1024);
  glds_sc1(l16, src + (j + 4) * 1024, dst + (j + 4) * 1024);
  const int n = j == 3 ? 7 : j + 8;
  if (j != 2 || lane < 32) glds_sc1(l16, src + n * 1024, dst + n * 1024);
}

constexpr unsigned L1_X = 0, L1_S = 81920, L1_WK = L1_S + 4 * 10240, L1_PART = L1_WK + 25600;
constexpr unsigned L1_CS = L1_PART  ;
constexpr unsigned L1_ST = 161792  , L1_KC = L1_ST + 512  , L1_KM = L1_KC + 128, L1_DUMP = L1_KM + 16;
static_assert(L1_PART + 12672 <= 161280 && L1_DUMP + 256 <= 163584, "lds");

DI void phase1(lptr lds, rsrc_t ws, int b, u32x4& q1, u32x2& q2, unsigned& epoch, float& kmax2) {
  kargs_t pa = kargs();
  const int tid = threadIdx.x, lane = tid & 63, w = rfl(tid >> 6);
  const int r32 = lane & 31, h = lane >> 5;
  const int xcd = b & 7, rk = b >> 3, tg = 4 * xcd + (rk >> 3), hg = rk & 7;
  const unsigned lds0 = (unsigned)(uintptr_t)lds, l16 = lane * 16;
  unsigned ep = __hip_atomic_load((GAS unsigned*)(pa->ws + W_EPOCH), RLX_AGENT);
  {
    const char* xb = pa->x + (size_t)(tg * 64 + 8 * w) * 1280;
    const int tl = lane >> 4;
    unsigned v0 = tl * 1280 + ((lane ^ (8 * w + tl)) & 15) * 16, v1 = (4 + tl) * 1280 + ((lane ^ (8 * w + 4 + tl)) & 15) * 16, xd = lds0 + L1_X + 8 * w * 256;
#pragma unroll 1
    for (int a = 0; a < 5; ++a) { glds(v0, xb, xd); glds(v1, xb, xd + 1024); v0 += 256; v1 += 256; xd += 16384; }
  }
  if (hg < 4) {
    const char* W = (hg < 2 ? pa->wk : pa->wv) + w * 1024;
#pragma unroll
    for (int i = 0; i < 3; ++i) glds(l16 + i * 8192, W, lds0 + L1_WK + w * 1024 + i * 8192);
    if (w == 0) glds(l16 + 3 * 8192, W, lds0 + L1_WK + 3 * 8192);
  }
  const char* wqb = pa->wq + hg * 160;
  unsigned vq[3], dq[3];
#pragma unroll
  for (int i = 0; i < 3; ++i) {
    const int n = i < 2 ? w + 8 * i : (w < 6 ? w + 16 : w + 8);
    const int slot = 6 * n + lane / 10, row = (slot & ~7) | ((slot & 1) << 2) | ((slot >> 1) & 3);
    vq[i] = row * 1280 + (lane % 10) * 16; dq[i] = n * 960;
  }
  const bool m3 = w < 5 || (w == 5 ? lane < 20 : lane == 0);
  auto dma_pair = [&](const char* src, unsigned dst) {
    glds(vq[0], src, dst + dq[0]);
    glds(vq[1], src, dst + dq[1]);
    if (m3) glds(vq[2], src, dst + dq[2]);
  };
  SBAR();
  STAMPX(8);
  const int i16 = lane & 15, tq = i16 >> 2, tp = i16 & 3, grp = (lane >> 4) & 1;
  WAITV(0);
  LBAR();
  PIN(ep); epoch = rfl(ep) + 1u;
  STAMPX(1);
  const int kth = hg & 1; const bool isV = hg >= 2;
  f32x16 kacc = {}; float kcs = 0.f; unsigned kn2 = 0u;
  auto dma_first = [&]() { dma_pair(wqb, lds0 + L1_S); dma_pair(wqb + 128 * 1280, lds0 + L1_S + 20480); };
  if (w >= 4) {
    dma_first();
#pragma unroll
    for (int ps = 0; ps < 2; ++ps) {
      const int t = (ps ? 32 - 32 * kth : 32 * kth) + 8 * (w - 4) + (lane >> 3), e = lane & 7;
      u32x4 v[10];
#pragma unroll
      for (int i = 0; i < 10; ++i) v[i] = lds128(lds + L1_X + (i >> 1) * 16384 + t * 256 + ((e + 8 * (i & 1) + 8 * ((t >> 1) & 1)) & 15) * 16);
      float a4[4] = {}, b4[4] = {};
#pragma unroll
      for (int i = 0; i < 10; ++i)
#pragma unroll
        for (int j = 0; j < 4; ++j) { a4[j] = dot2(v[i][j], v[i][j], a4[j]); b4[j] = dot2(v[i][j], ONES2, b4[j]); }
      const float sq = sum8((a4[0] + a4[1]) + (a4[2] + a4[3])), sm = sum8((b4[0] + b4[1]) + (b4[2] + b4[3]));
      const float mean = sm * (1.f / DM), rstd = rsqrtf(fmaxf(sq * (1.f / DM) - mean * mean, 0.f) + LN_EPS);
      if (e == 0) { ((LAS float*)(lds + L1_ST))[t] = mean; ((LAS float*)(lds + L1_ST))[64 + t] = rstd; }
      if (ps == 0) {
        LBAR(); dma_pair(wqb + 256 * 1280, lds0 + L1_WK);
        const int ln = ((rk >> 3) * 4 + (w - 4)) * 32 + r32;
        if (h == 0) touch((ln >> 1) * 1280 + (ln & 1) * 128, wqb + 384 * 1280 - ((hg * 160) & 127), lds0 + L1_DUMP);
      }
    }
  } else if (hg < 4) {
    lptr wb = lds + L1_WK + (160 * w + 8 * h + tq) * 40 + grp * 32 + 8 * tp;
    const int tok = kth * 32 + r32;
    u32x4 xf[10]; bf16x8 wf[10];
#pragma unroll
    for (int i = 0; i < 10; ++i) { const int c = 20 * w + 2 * i + h;
      xf[i] = lds128(lds + L1_X + (c >> 4) * 16384 + tok * 256 + ((c ^ tok) & 15) * 16); wf[i] = cat8(trread(wb + i * 640), trread(wb + i * 640 + 160)); }
    SBAR();
    if (w != 0) dma_first();
    if (isV) {
#pragma unroll
      for (int i = 0; i < 10; ++i) kacc = MFMA16(asbf8(xf[i]), wf[i], kacc);
    } else {
#pragma unroll
      for (int i = 0; i < 10; ++i) kacc = MFMA16(wf[i], asbf8(xf[i]), kacc);
    }
    float c4[4] = {};
#pragma unroll
    for (int i = 0; i < 10; ++i) { const u32x4 wu = asu4(wf[i]);
#pragma unroll
      for (int j = 0; j < 4; ++j) c4[j] = dot2(wu[j], ONES2, c4[j]); }
    kcs = swap32_sum((c4[0] + c4[1]) + (c4[2] + c4[3]));
    if (w > 0) {
      LAS f32x4* pa = (LAS f32x4*)(lds + L1_PART + (w - 1) * 4224) + lane;
#pragma unroll
      for (int g = 0; g < 4; ++g) pa[g * 64] = f32x4{kacc[4 * g], kacc[4 * g + 1], kacc[4 * g + 2], kacc[4 * g + 3]};
      if (h == 0) ((LAS float*)(lds + L1_PART + (w - 1) * 4224 + 4096))[r32] = kcs;
    }
  } else dma_first();
  if (w < 4) {
    LBAR();
    if (w != 0 || hg >= 4) dma_pair(wqb + 256 * 1280, lds0 + L1_WK);
  }
  if (hg < 4 && w == 0) {
#pragma unroll
    for (int v = 0; v < 3; ++v) {
      LAS f32x4* pa = (LAS f32x4*)(lds + L1_PART + v * 4224) + lane;
#pragma unroll
      for (int g = 0; g < 4; ++g) { const f32x4 t = pa[g * 64]; kacc[4 * g] += t[0]; kacc[4 * g + 1] += t[1]; kacc[4 * g + 2] += t[2]; kacc[4 * g + 3] += t[3]; }
      kcs += ((LAS float*)(lds + L1_PART + v * 4224 + 4096))[r32];
    }
    const int tile = tg * 2 + kth;
    const LAS float* st = (const LAS float*)(lds + L1_ST) + kth * 32;
    if (lane == 0 && hg == 0) { bst32<AUX_SC1>(ws, W_CNTO + tg * 256, 0u); bst32<AUX_SC1>(ws, W_CNTH + tg * 256, 0u); }
    if (lane == 0 && hg == 1 && !(tg & 1)) bst32<AUX_SC1>(ws, W_CNTG + (tg >> 1) * 256, 0u);
    if (!isV) {
      LAS float* tab = (LAS float*)(lds + L1_KC);
      if (h == 0) tab[r32] = kcs;
      const float mean = st[r32], rstd = st[64 + r32];
      asm volatile("" ::: "memory"); WAITL(); asm volatile("" ::: "memory");
      float kv[12];
#pragma unroll
      for (int g = 0; g < 3; ++g) { const f32x4 c4 = *(LAS f32x4*)(tab + 8 * g + 4 * h);
#pragma unroll
        for (int j = 0; j < 4; ++j) kv[4 * g + j] = (kacc[4 * g + j] - mean * c4[j]) * rstd; }
      const u32x4 k1 = {pk2(kv[0], kv[1]), pk2(kv[2], kv[3]), pk2(kv[4], kv[5]), pk2(kv[6], kv[7])};
      const u32x2 k2 = {pk2(kv[8], kv[9]), pk2(kv[10], kv[11])};
      bst128<AUX_SC1>(ws, W_KV + tile * KV_TILE + l16, k1);
      if (h == 0) bst64<AUX_SC1>(ws, W_KV + tile * KV_TILE + KV_K2 + r32 * 8, k2);
      float n2 = 0.f;
#pragma unroll
      for (int j = 0; j < 4; ++j) n2 = dot2(k1[j], k1[j], n2);
      if (h == 0) { n2 = dot2(k2[0], k2[0], n2); n2 = dot2(k2[1], k2[1], n2); }
      kn2 = __float_as_uint(max64(swap32_sum(n2)));
    } else {
      const unsigned cv = r32 == 20 ? ONES2 : 0u;
#pragma unroll
      for (int s = 0; s < 2; ++s) {
        float vv[8];
#pragma unroll
        for (int g = 0; g < 2; ++g) { const f32x4 m4 = *(const LAS f32x4*)(st + 16 * s + 8 * g + 4 * h), r4 = *(const LAS f32x4*)(st + 64 + 16 * s + 8 * g + 4 * h);
#pragma unroll
          for (int j = 0; j < 4; ++j) vv[4 * g + j] = (kacc[8 * s + 4 * g + j] - m4[j] * kcs) * r4[j]; }
        u32x4 o = {pk2(vv[0], vv[1]), pk2(vv[2], vv[3]), pk2(vv[4], vv[5]), pk2(vv[6], vv[7])};
        if (r32 >= 20) o = u32x4{cv, cv, cv, cv};
        if (r32 < 22) bst128<AUX_SC1>(ws, W_KV + tile * KV_TILE + KV_V + (2 * s + h) * KV_VS + r32 * 16, o);
      }
    }
  }
  if (hg < 4 && w == 0) {
    dma_first(); dma_pair(wqb + 256 * 1280, lds0 + L1_WK);
    WAITV(9);
    if (lane == 0) __hip_atomic_store((GAS unsigned long long*)(pa->ws + W_FKV) + tg * 4 + hg, ((unsigned long long)epoch << 32) | kn2, RLX_AGENT);
    WAITV(4);
  } else if (w >= 4) WAITV(4); else WAITV(3);
  LBAR();
  STAMPX(2);
  {
    const int hh = w & 3, th = w >> 2, tok = th * 32 + r32;
    f32x16 acc = {}; float c4[4] = {};
    const unsigned wrow = (8 * h + 2 * tq) * 160 + hh * 40 + grp * 32 + 8 * tp;
    unsigned xo[8];
#pragma unroll
    for (int i = 0; i < 8; ++i) xo[i] = L1_X + tok * 256 + (((2 * i + h) ^ tok) & 15) * 16;
    u32x4 xf[2][4]; bf16x8 wf[2][4];
    unsigned xa = 0, wa = L1_S + wrow;
#define Q_RD1(e, ks, XA, WA) do { xf[e][ks] = lds128(lds + xo[4 * (e) + (ks)] + (XA)); lptr a_ = lds + (WA) + (e) * 10240 + (ks) * 2560; wf[e][ks] = cat8(trread(a_), trread(a_ + 160)); } while (0)
#define Q_STEP(e, XA, WA) do { _Pragma("unroll") for (int ks = 0; ks < 4; ++ks) { acc = MFMA16(wf[e][ks], asbf8(xf[e][ks]), acc); Q_RD1(1 - (e), ks, XA, WA); SBAR(); } } while (0)
#define Q_CS(e) do { _Pragma("unroll") for (int ks = 0; ks < 4; ++ks) { const u32x4 wu = asu4(wf[e][ks]); _Pragma("unroll") for (int j = 0; j < 4; ++j) c4[j] = dot2(wu[j], ONES2, c4[j]); } } while (0)
#pragma unroll
    for (int ks = 0; ks < 4; ++ks) Q_RD1(0, ks, xa, wa);
#pragma unroll 1
    for (int pr = 0; pr < 5; ++pr) {
      Q_STEP(0, xa, wa);
      if (pr == 1 || pr == 2) WAITV(3); else if (pr == 3) WAITV(0);
      if (pr == 2 && w == 0) {
        const float km = max64(wait_granules(pa->ws, W_FKV, epoch, lane));
        if (lane == 0) *(LAS float*)(lds + L1_KM) = km;
      }
      LBAR();
      if (pr == 2) {
        kmax2 = *(LAS float*)(lds + L1_KM);
        if (th == 0) { dma_kv3(pa->ws + W_KV, lds0, 0, hh, lane); dma_kv3(pa->ws + W_KV, lds0, 2, hh, lane); } else dma_kv3(pa->ws + W_KV, lds0, 1, hh, lane);
      }
      const unsigned wn = pr == 1 ? L1_WK + wrow : (pr == 2 || pr == 4 ? L1_S + wrow : wa + 20480);
      if (pr < 2) dma_pair(wqb + (size_t)(pr + 3) * 128 * 1280, lds0 + L1_S + pr * 20480);
      if (th == 0) Q_CS(0); else Q_CS(1);
      SBAR();
      xa = pr < 4 ? xa + 16384 : 0;
      Q_STEP(1, xa, wn);
      wa = wn;
    }
#undef Q_RD1
#undef Q_STEP
#undef Q_CS
    const float cs = swap32_sum((c4[0] + c4[1]) + (c4[2] + c4[3]));
    if (h == 0) ((LAS float*)(lds + L1_CS))[w * 32 + r32] = cs;
    const float mean = ((LAS float*)(lds + L1_ST))[tok], rstd = ((LAS float*)(lds + L1_ST))[64 + tok] * QSCALE;
    LBAR();
    float qv[12];
#pragma unroll
    for (int g = 0; g < 3; ++g) {
      const f32x4 ca = *(LAS f32x4*)(lds + L1_CS + (hh * 32 + 8 * g + 4 * h) * 4), cb = *(LAS f32x4*)(lds + L1_CS + ((hh + 4) * 32 + 8 * g + 4 * h) * 4);
#pragma unroll
      for (int j = 0; j < 4; ++j) qv[4 * g + j] = (acc[4 * g + j] - mean * (ca[j] + cb[j])) * rstd;
    }
    q1 = u32x4{pk2(qv[0], qv[1]), pk2(qv[2], qv[3]), pk2(qv[4], qv[5]), pk2(qv[6], qv[7])};
    q2 = h ? u32x2{0u, 0u} : u32x2{pk2(qv[8], qv[9]), pk2(qv[10], qv[11])};
  }
  STAMPX(3);
}

constexpr unsigned L2_CO = 60 * KV_TILE, L2_CR = L2_CO + 16, L2_CP = L2_CO + 32, L2_CS = L2_CO + 48, L2_CD = L2_CO + 112, L2_CQ = L2_CO + 128, L2_CF = L2_CO + 144, L2_DUMP = L2_CO + 256;
constexpr unsigned L2_RT = 52 * KV_TILE;
constexpr unsigned L3_WO = 4 * KV_TILE, L3_RED = L3_WO + 102400;
#define PRIO_M 3
#define EXP2(a) __builtin_amdgcn_exp2f(a)
#define CODE2_BYTES 36608

DI f32x16 attn_exact(rsrc_t ws, u32x4 q1, u32x2 q2, int lane) {
  const int r32 = lane & 31, h = lane >> 5, rv = r32 < 21 ? r32 : 21;
  float mx = -3.0e38f;
#pragma unroll 1
  for (int t = 0; t < 64; ++t) {
    const unsigned base = W_KV + t * KV_TILE;
    f32x16 s = MFMA8(asbf4(bld64<AUX_SC1>(ws, base + KV_K2 + r32 * 8)), asbf4(q2), f32x16{});
    s = MFMA16(asbf8(bld128<AUX_SC1>(ws, base + lane * 16)), asbf8(q1), s);
#pragma unroll
    for (int j = 0; j < 16; ++j) mx = fmaxf(mx, s[j]);
  }
  mx = swap32_max(mx);
  f32x16 o = {};
#pragma unroll 1
  for (int t = 0; t < 64; ++t) {
    const unsigned base = W_KV + t * KV_TILE;
    f32x16 s = MFMA8(asbf4(bld64<AUX_SC1>(ws, base + KV_K2 + r32 * 8)), asbf4(q2), f32x16{});
    s = MFMA16(asbf8(bld128<AUX_SC1>(ws, base + lane * 16)), asbf8(q1), s);
    u32x4 pp[2];
#pragma unroll
    for (int j = 0; j < 8; ++j) pp[j >> 2][j & 3] = pk2(EXP2(s[2 * j] - mx), EXP2(s[2 * j + 1] - mx));
#pragma unroll
    for (int e = 0; e < 2; ++e) o = MFMA16(asbf8(bld128<AUX_SC1>(ws, base + KV_V + (2 * e + h) * KV_VS + rv * 16)), asbf8(pp[e]), o);
  }
  return o;
}

DI void phase_attn(lptr lds, rsrc_t ws, int b, const u32x4 q1, const u32x2 q2, unsigned epoch, float kmax2) {
  const int tid = threadIdx.x, lane = tid & 63, w = rfl(tid >> 6);
  const int r32 = lane & 31, h = lane >> 5;
  const int xcd = b & 7, rk = b >> 3, tg = 4 * xcd + (rk >> 3), hg = rk & 7;
  const int hh = w & 3, th = w >> 2;
  const unsigned lds0 = (unsigned)(uintptr_t)lds, l16 = lane * 16;
  if (tid < 10) *(LAS unsigned*)(lds + L2_CO + tid * 16) = 0u;
  f32x16 cb;
  {
    float n2 = 0.f;
#pragma unroll
    for (int j = 0; j < 4; ++j) n2 = dot2(q1[j], q1[j], n2);
    n2 = dot2(q2[0], q2[0], n2); n2 = dot2(q2[1], q2[1], n2);
    const float ref = sqrtf(swap32_sum(n2) * kmax2) * 1.001f;
#pragma unroll
    for (int j = 0; j < 16; ++j) cb[j] = -ref;
    PIN(cb);
  }
  const int rv = r32 < 21 ? r32 : 21;
  const unsigned oK1 = l16, oK2 = KV_K2 + r32 * 8, oV = KV_V + h * KV_VS + rv * 16;
  f32x16 o = {};
  LBAR();
  STAMPX(9);
  {
    lptr aK1 = lds + oK1; lptr aK2 = lds + oK2; lptr aV = lds + oV;
    u32x4 k1 = lds128(aK1); u32x2 k2 = lds64(aK2);
    f32x16 sc = MFMA8(asbf4(k2), asbf4(q2), cb); sc = MFMA16(asbf8(k1), asbf8(q1), sc);
    k1 = lds128(aK1 + KV_TILE); k2 = lds64(aK2 + KV_TILE);
    u32x4 v0 = lds128(aV), v1 = {}, p0, p1 = {};
    u32x4 k1b, v0b, v1b; u32x2 k2b;
    f32x16 sn;
#define GRP(MF, SC, i, P, j) do { MF; SBAR(); { const float e0 = EXP2(SC[i]), e1 = EXP2(SC[i + 1]), e2 = EXP2(SC[i + 2]), e3 = EXP2(SC[i + 3]); P[j] = pk2(e0, e1); P[j + 1] = pk2(e2, e3); } PIN(P); SBAR(); } while (0)
#define TILE(SC, SN, K1, K2, V0, V1, K1N, K2N, V0N, V1N, PK1, PK2, PV0, PV1) do { \
      WAITL(); \
      K2N = lds64(PK2); K1N = lds128(PK1); V0N = lds128(PV0); V1N = lds128(PV1); SBAR(); \
      GRP(SN = MFMA8(asbf4(K2), asbf4(q2), cb), SC, 0, p0, 0); \
      GRP(o = MFMA16(asbf8(V1), asbf8(p1), o), SC, 4, p0, 2); \
      GRP(SN = MFMA16(asbf8(K1), asbf8(q1), SN), SC, 8, p1, 0); \
      GRP(o = MFMA16(asbf8(V0), asbf8(p0), o), SC, 12, p1, 2); } while (0)
#define TA(T) TILE(sc, sn, k1, k2, v0, v1, k1b, k2b, v0b, v1b, aK1 + ((T) + 2) * KV_TILE, aK2 + ((T) + 2) * KV_TILE, aV + ((T) + 1) * KV_TILE, aV + (T) * KV_TILE + 2 * KV_VS)
#define TB(T) TILE(sn, sc, k1b, k2b, v0b, v1b, k1, k2, v0, v1, aK1 + ((T) + 2) * KV_TILE, aK2 + ((T) + 2) * KV_TILE, aV + ((T) + 1) * KV_TILE, aV + (T) * KV_TILE + 2 * KV_VS)
#define ADV() do { aK1 += 8 * KV_TILE; aK2 += 8 * KV_TILE; aV += 8 * KV_TILE; } while (0)
#define BODY8() do { TA(0); TB(1); TA(2); TB(3); TA(4); TB(5); TA(6); TB(7); ADV(); } while (0)
    if (th == 1) {
      const char* kvg = kargs()->ws + W_KV;
#define DMA3(c) do { dma_kv3(kvg, lds0, c, hh, lane); SBAR(); } while (0)
      __builtin_amdgcn_s_setprio(2);
      PIN(sc);
      DMA3(3); TA(0); DMA3(4); TB(1); DMA3(5); TA(2); DMA3(6); TB(3);
      DMA3(7); TA(4); DMA3(8); TB(5); DMA3(9); TA(6); DMA3(10); TB(7); ADV();
      if (lane == 0) lds_add1(lds + L2_CP);
      WAITV(6); if (lane == 0) lds_add1(lds + L2_CS);
      lds_wait_ge(lds + L2_CS, 4u);
      DMA3(11); TA(0); DMA3(12); TB(1); TA(2); TB(3); TA(4); TB(5); TA(6); TB(7); ADV();
      WAITV(0); if (lane == 0) lds_add1(lds + L2_CS + 16);
#pragma nounroll
      for (int m = 2; m < 6; ++m) {
        if (m == 4) { lds_wait_ge(lds + L2_CS + 16, 4u); lds_wait_ge(lds + L2_CR, 4u); DMA3(13); DMA3(14); }
        if (m == 5) { WAITV(0); if (lane == 0) lds_add1(lds + L2_CS + 32); lds_wait_ge(lds + L2_CP, 8u); DMA3(15); }
        if (m == PRIO_M) __builtin_amdgcn_s_setprio(0);
        BODY8();
      }
      WAITV(0); if (lane == 0) lds_add1(lds + L2_CS + 48);
#undef DMA3
    } else {
      __builtin_amdgcn_s_setprio(1);
      const int ga = b * 4 + hh;
      u32x4 rg[4];
      auto gu_load = [&](int gj) {
        kargs_t pa = kargs(); const int cgf = gj / 40, r = gj % 40;
        const rsrc_t wr = __builtin_amdgcn_make_buffer_rsrc((void*)((r & 1) ? pa->wu : pa->wg), 0, DM * FF * 2, 0x00020000);
        const unsigned vo = (32 * (r >> 1) + (lane >> 3)) * (FF * 2) + cgf * 128 + (lane & 7) * 16;
#pragma unroll
        for (int i = 0; i < 4; ++i) rg[i] = __builtin_amdgcn_raw_buffer_load_b128(wr, vo, i * 8 * FF * 2, 0);
      };
      auto gu_store = [&](int gj) {
        const int cgf = gj / 40, r = gj % 40, i16 = lane & 15, tq = i16 >> 2, tp = i16 & 3, g4 = lane >> 4;
        lptr scr = lds + L2_RT + hh * 4096;
#pragma unroll
        for (int i = 0; i < 4; ++i) *(LAS u32x4*)(scr + i * 1024 + l16) = rg[i];
#pragma unroll
        for (int u = 0; u < 4; ++u) { lptr a = scr + (8 * g4 + tq) * 128 + u * 32 + tp * 8;
          const u32x4 f = asu4(cat8(trread(a), trread(a + 512)));
          bst128<AUX_SC1>(ws, W_WGU + (((cgf * 4 + u) * 20 + (r >> 1)) * 2 + (r & 1)) * 1024 + l16, f);
          float cs = ((bflo(f[0]) + bfhi(f[0])) + (bflo(f[1]) + bfhi(f[1]))) + ((bflo(f[2]) + bfhi(f[2])) + (bflo(f[3]) + bfhi(f[3])));
          cs = swap32_sum(swap16_sum(cs));
          if (lane < 16) bst32<AUX_SC1>(ws, W_CSP + (((cgf * 4 + u) * 2 + (r & 1)) * 20 + (r >> 1)) * 64 + lane * 4, __float_as_uint(cs)); }
      };
      {
        kargs_t pa = kargs();
#pragma unroll
        for (int e = 0; e < 2; ++e) { const int gj = ga + 1024 * e;
          if (e == 0 || gj < 1080) { const int cgf = gj / 40, r = gj % 40; if (lane < 32) touch((32 * (r >> 1) + lane) * (FF * 2) + cgf * 128, (r & 1) ? pa->wu : pa->wg, lds0 + L2_DUMP); } }
        const int nl = ga < 986 ? 24 : (ga == 986 ? 16 : 0);
        if (lane < nl) touch(lane * 128, ga < 720 ? pa->wd + (size_t)ga * 3072 : pa->wo + (size_t)(ga - 720) * 3072, lds0 + L2_DUMP);
      }
      PIN(sc);
#pragma nounroll
      for (int m = 0; m < 6; ++m) {
        if (m == 1) {
          if (lane == 0) lds_add1(lds + L2_CP);
          u32x4 rd[3];
          gu_load(ga);
          {
            kargs_t pa = kargs();
#pragma unroll
            for (int i = 0; i < 3; ++i) { const int j = ga * 3 + i;
              const char* src = j < 2160 ? pa->wd + (size_t)j * 1024 : pa->wo + (size_t)(j < 2960 ? j - 2160 : 0) * 1024;
              rd[i] = *(const GAS u32x4*)(src + l16); }
          }
          gu_store(ga);
#pragma unroll
          for (int i = 0; i < 3; ++i) { const int j = ga * 3 + i;
            if (j < 2960) { const bool dn = j < 2160; const int a5 = (dn ? j : j - 2160) * 4 + (lane >> 4), f = (a5 * 52429) >> 18, c = (a5 - 5 * f) * 16 + (lane & 15), cg = (c * 6554) >> 16, cc = c - cg * 10;
              bst128<AUX_SC1>(ws, dn ? W_WD + (cg * FF + ((f & ~12) | ((f & 4) << 1) | ((f & 8) >> 1))) * 160 + cc * 16 : W_WO + (cg * DM + f) * 160 + cc * 16, rd[i]); } }
          if (ga < 56) { gu_load(ga + 1024); gu_store(ga + 1024); }
          WAITL(); if (lane == 0) lds_add1(lds + L2_CR);
          lds_wait_ge(lds + L2_CS, 4u);
        }
        if (m == 2) {
          WAITV(0);
          unsigned old = 0; if (lane == 0) old = __hip_atomic_fetch_add((LAS unsigned*)(lds + L2_CD), 1u, __ATOMIC_RELAXED, __HIP_MEMORY_SCOPE_WORKGROUP);
          if (lane == 0 && old == 3u) __hip_atomic_store((GAS unsigned*)(kargs()->ws + W_FRT) + b, epoch, RLX_AGENT);
        }
        if (m == 4) {
          char* wsp = kargs()->ws;
          wait_flags<4>(wsp, W_FRT, epoch, lane);
          if (lane == 0) lds_add1(lds + L2_CF);
          if (b == 0 && w == 0 && lane == 0) __hip_atomic_store((GAS unsigned*)(wsp + W_EPOCH), epoch, RLX_AGENT);
          const int ngl = rk & 3, u0 = 27 * (xcd & 3) + 7 * ngl, part = (rk >> 2) * 4 + hh;
          const int nl = (ngl == 3 ? 6 : 7) * 10;
          touch_sc1((part * nl + lane) * 128, wsp + W_WGU + (size_t)u0 * 40960, lds0 + L2_DUMP);
          if (lane < nl - 64) touch_sc1((part * nl + 64 + lane) * 128, wsp + W_WGU + (size_t)u0 * 40960, lds0 + L2_DUMP);
          if (lane < 50) touch_sc1((((rk >> 3) * 4 + hh) * 50 + lane) * 128, wsp + W_WO + hg * DM * 160, lds0 + L2_DUMP);
          { unsigned long long pc; asm volatile("s_getpc_b64 %0" : "=s"(pc));
            const int ln = rk * 9 + lane; if (hh == 0 && lane < 9 && ln * 128 < CODE2_BYTES) touch(ln * 128, (const char*)(pc & ~127ull), lds0 + L2_DUMP); }
          lds_wait_ge(lds + L2_CS + 16, 4u);
        }
        BODY8();
      }
    }
    lds_wait_ge(lds + L2_CS + 32, 4u);
    TA(0); TB(1);
    if (lane == 0) lds_add1(lds + L2_CQ);
    TA(2); TB(3); TA(4); TB(5); TA(6); TB(7); ADV();
    lds_wait_ge(lds + L2_CQ, 8u); lds_wait_ge(lds + L2_CF, 1u);
    {
      const char* src = kargs()->ws + W_WO + hg * DM * 160 + w * 1024; unsigned dst = lds0 + L3_WO + w * 1024, vo = l16;
#pragma unroll 1
      for (int i = 0; i < 12; ++i) { glds_sc1(vo, src, dst); vo += 8192; dst += 8192; }
      if (w < 4) glds_sc1(vo, src, dst);
    }
    lds_wait_ge(lds + L2_CS + 48, 4u);
    {
      lptr zK1 = lds + oK1; lptr zK2 = lds + oK2; lptr zV = lds + oV;
      TA(0); TB(1);
      TILE(sc, sn, k1, k2, v0, v1, k1b, k2b, v0b, v1b, zK1, zK2, aV + 3 * KV_TILE, aV + 2 * KV_TILE + 2 * KV_VS);
      TILE(sn, sc, k1b, k2b, v0b, v1b, k1, k2, v0, v1, zK1 + KV_TILE, zK2 + KV_TILE, zV, aV + 3 * KV_TILE + 2 * KV_VS);
      aK1 = zK1; aK2 = zK2; aV = zV;
      TA(0); TB(1); TA(2); TB(3);
    }
    WAITL();
    o = MFMA16(asbf8(v1), asbf8(p1), o);
    __builtin_amdgcn_s_setprio(0);
  }
  STAMPX(10);
  float lsum = __uint_as_float(__builtin_amdgcn_permlane32_swap(__float_as_uint(o[8]), __float_as_uint(o[8]), false, false)[1]);
  if (__builtin_expect(__any(!(lsum >= 5.421010862427522e-20f)), 0)) {
    o = attn_exact(ws, q1, q2, lane);
    lsum = __uint_as_float(__builtin_amdgcn_permlane32_swap(__float_as_uint(o[8]), __float_as_uint(o[8]), false, false)[1]);
  }
  {
    const float inv = __builtin_amdgcn_rcpf(lsum);
    const u32x4 oa = {pk2(o[0] * inv, o[1] * inv), pk2(o[2] * inv, o[3] * inv), pk2(o[4] * inv, o[5] * inv), pk2(o[6] * inv, o[7] * inv)};
    const u32x2 ob = {pk2(o[8] * inv, o[9] * inv), pk2(o[10] * inv, o[11] * inv)};
    const unsigned tile16 = tg * 4 + th * 2 + (r32 >> 4), g0 = 10 * hg + 5 * (hh >> 1);
    bst128<AUX_SC1>(ws, W_OF + ((tile16 * 80 + g0 + 2 * (hh & 1) + h) * 16 + (r32 & 15)) * 16, oa);
    if (h == 0) bst64<AUX_SC1>(ws, W_OF + ((tile16 * 80 + g0 + 4) * 16 + (r32 & 15)) * 16 + 8 * (hh & 1), ob);
  }
  WAITV(0);
  LBAR();
  if (tid == 0) (void)__hip_atomic_fetch_add((GAS unsigned*)(kargs()->ws + W_CNTO + tg * 256), 1u, RLX_AGENT);
}

DI void phase_oproj(lptr lds, rsrc_t ws, int b, u32x2 (&hres)[5], unsigned epoch) {
  kargs_t pa = kargs();
  const int tid = threadIdx.x, lane = tid & 63, w = rfl(tid >> 6);
  const int xcd = b & 7, rk = b >> 3, tg = 4 * xcd + (rk >> 3), cg = rk & 7;
  const int mt = w & 3, kh = w >> 2, t16 = lane & 15, g = lane >> 4, tq = t16 >> 2, tp = t16 & 3;
  const int tok = tg * 64 + mt * 16 + t16;
  u32x2 xr[5] = {};
  if (kh == 0) {
#pragma unroll
    for (int nt = 0; nt < 5; ++nt) xr[nt] = *(const GAS u32x2*)(pa->x + (size_t)tok * 1280 + cg * 160 + nt * 32 + g * 8);
  }
  unsigned r1[10], r2[10];
#pragma unroll
  for (int i = 0; i < 10; ++i) {
    const int t = 4 * i + g, a5 = (t * 13) >> 6, m = t - 5 * a5, pair = 8 * kh + a5;
    const int last = m >> 2, ra = pair * 40 + (m >> 1) * 20 + (m & 1) * 4 - last * 24;
    r1[i] = L3_WO + (ra + tq) * 160 + tp * 8; r2[i] = r1[i] + (8 + 12 * last) * 160;
  }
  wait_cnt2(pa->ws, W_CNTO + tg * 256, W_CNTO + tg * 256, 8u, epoch, 0);
  STAMPX(13);
  bf16x8 bf[2][5];
  u32x4 a[10];
#pragma unroll
  for (int i = 0; i < 10; ++i) a[i] = bld128<AUX_SC1>(ws, W_OF + ((tg * 4 + mt) * 80 + 4 * (10 * kh + i)) * 256 + lane * 16);
#pragma unroll
  for (int nt = 0; nt < 5; ++nt) bf[0][nt] = cat8(trread(lds + r1[0] + nt * 32), trread(lds + r2[0] + nt * 32));
  f32x4 acc[5] = {};
#pragma unroll
  for (int i = 0; i < 10; ++i) {
    if (i + 1 < 10) {
#pragma unroll
      for (int nt = 0; nt < 5; ++nt) bf[(i + 1) & 1][nt] = cat8(trread(lds + r1[i + 1] + nt * 32), trread(lds + r2[i + 1] + nt * 32));
    }
    SBAR();
#pragma unroll
    for (int nt = 0; nt < 5; ++nt) acc[nt] = MFMAS(bf[i & 1][nt], asbf8(a[i]), acc[nt]);
    SBAR();
  }
  STAMPX(14);
  LAS f32x4* red = (LAS f32x4*)(lds + L3_RED) + mt * 320 + lane;
  if (kh == 1) {
#pragma unroll
    for (int nt = 0; nt < 5; ++nt) red[nt * 64] = acc[nt];
  }
  LBAR();
  if (kh == 0) {
    float s1 = 0.f, s2 = 0.f;
#pragma unroll
    for (int nt = 0; nt < 5; ++nt) {
      const f32x4 o = red[nt * 64];
      const float h0 = bflo(xr[nt][0]) + acc[nt][0] + o[0], h1 = bfhi(xr[nt][0]) + acc[nt][1] + o[1], h2 = bflo(xr[nt][1]) + acc[nt][2] + o[2], h3 = bfhi(xr[nt][1]) + acc[nt][3] + o[3];
      const u32x2 hv = {pk2(h0, h1), pk2(h2, h3)};
      hres[nt] = hv;
      s1 = dot2(hv[0], ONES2, s1); s1 = dot2(hv[1], ONES2, s1); s2 = dot2(hv[0], hv[0], s2); s2 = dot2(hv[1], hv[1], s2);
      const int gam = 10 * cg + 2 * nt + (g >> 1);
      bst64<AUX_SC1>(ws, W_HF + (((tg * 4 + mt) * 20 + (gam >> 2)) * 64 + (gam & 3) * 16 + t16) * 16 + (g & 1) * 8, hv);
    }
    s1 = swap32_sum(swap16_sum(s1)); s2 = swap32_sum(swap16_sum(s2));
    if (g == 0) bst64<AUX_SC1>(ws, W_HST + ((tg * 8 + cg) * 64 + mt * 16 + t16) * 8, u32x2{__float_as_uint(s1), __float_as_uint(s2)});
  }
  signal_cnt(kargs()->ws, W_CNTH + tg * 256);
}

constexpr unsigned L4_S = 0  , L4_ST = 122880  , L4_CO = 163568;
DI constexpr unsigned l5_slot(int c) { return (c & 3) == 0 ? 123904u : ((c & 3) == 1 ? 40960u : ((c & 3) == 2 ? 71680u : 0u)); }
constexpr unsigned L5_RED = 102400;
static_assert(123904 + 30720 <= L4_CO, "lds");
DI void dma_wd_chunk(const char* wd, unsigned lds0, int w, int lane, int c) {
  const unsigned l16 = lane * 16;
#pragma unroll
  for (int i = 0; i < 4; ++i) { int n = w + 8 * i; const bool dup = i == 3 && n >= 30; if (dup) n -= 8; const int k2 = n >= 15, j = n - 15 * k2;
    if (i < 3 || !dup || lane == 0) glds_sc1(l16 + j * 1024 + k2 * (864 * 160), wd + (size_t)c * (96 * 160), lds0 + l5_slot(c) + n * 1024); }
}
DI void dma_wd_chunk7(const char* wd, unsigned lds0, int w, int lane, int c) {
  const unsigned l16 = lane * 16;
#pragma unroll
  for (int i = 0; i < 5; ++i) { int n = w + 7 * i; const bool dup = i == 4 && n >= 30; if (dup) n -= 7; const int k2 = n >= 15, j = n - 15 * k2;
    if (i < 4 || !dup || lane == 0) glds_sc1(l16 + j * 1024 + k2 * (864 * 160), wd + (size_t)c * (96 * 160), lds0 + l5_slot(c) + n * 1024); }
}
constexpr int GWIN = 10;
DI void phase_gateup(lptr lds, rsrc_t ws, int b, unsigned epoch) {
  char* wsp = kargs()->ws;
  const int tid = threadIdx.x, lane = tid & 63, w = rfl(tid >> 6);
  const int xcd = b & 7, rk = b >> 3, mb = 8 * (xcd >> 2) + (rk >> 2), ngl = rk & 3, u0 = 27 * (xcd & 3) + 7 * ngl, nu = ngl == 3 ? 6 : 7;
  const int t16 = lane & 15, g = lane >> 4;
  const unsigned lds0 = (unsigned)(uintptr_t)lds, l16 = lane * 16;
  const char* wd = wsp + W_WD + (size_t)(rk & 7) * FF * 160;
  const bool act = w < nu;
  const int u = u0 + (act ? w : 0);
  const unsigned wbase = W_WGU + u * 40960 + l16;
  constexpr int GPRE = 3;
  u32x4 ag[GWIN], au[GWIN];
  float csp[2][5] = {};
  if (act) {
#pragma unroll
    for (int i = 0; i < GPRE; ++i) { ag[i] = bld128<AUX_SC1>(ws, wbase + i * 2048); au[i] = bld128<AUX_SC1>(ws, wbase + i * 2048 + 1024); }
#pragma unroll
    for (int m = 0; m < 2; ++m)
#pragma unroll
      for (int i = 0; i < 5; ++i) csp[m][i] = __uint_as_float(bld32<AUX_SC1>(ws, W_CSP + ((u * 2 + m) * 20 + 5 * g + i) * 64 + t16 * 4));
  }
  SBAR();
  wait_cnt2(wsp, W_CNTH + (2 * mb) * 256, W_CNTH + (2 * mb + 1) * 256, 8u, epoch, 7);
  STAMPX(16);
  const char* hsrc = wsp + W_HF + (size_t)(mb * 8 + w) * 20 * 1024;
  auto dma_h = [&](int sc, int slot) {
#pragma unroll
    for (int i = 0; i < 5; ++i) glds_sc1(l16 + (5 * sc + i) * 1024, hsrc, lds0 + L4_S + slot * 40960 + i * 8192 + w * 1024);
  };
  if (tid == 0) *(LAS unsigned*)(lds + L4_CO) = 0u;
  u32x2 sv[8];
  if (tid < 128) {
#pragma unroll
    for (int c = 0; c < 8; ++c) sv[c] = bld64<AUX_SC1>(ws, W_HST + (((2 * mb + (tid >> 6)) * 8 + c) * 64 + (tid & 63)) * 8);
  }
  dma_h(0, 0);
  if (act) {
#pragma unroll
    for (int i = GPRE; i < GWIN; ++i) { ag[i] = bld128<AUX_SC1>(ws, wbase + i * 2048); au[i] = bld128<AUX_SC1>(ws, wbase + i * 2048 + 1024); }
  }
  SBAR();
  dma_h(1, 1); dma_h(2, 2);
  if (tid < 128) {
    float s1 = 0.f, s2 = 0.f;
#pragma unroll
    for (int c = 0; c < 8; ++c) { s1 += __uint_as_float(sv[c][0]); s2 += __uint_as_float(sv[c][1]); }
    const float mean = s1 * (1.f / DM), rstd = rsqrtf(fmaxf(s2 * (1.f / DM) - mean * mean, 0.f) + LN_EPS);
    *(LAS f32x2*)(lds + L4_ST + tid * 8) = f32x2{mean, rstd};
  }
  if (act) WAITV(24); else WAITV(10);
  LBAR();
  STAMPX(17);
  if (act) {
    f32x4 aG[8] = {}, aU[8] = {};
    lptr hb = lds + L4_S + l16;
    u32x4 bb[2][4];
#pragma unroll
    for (int t = 0; t < 4; ++t) bb[0][t] = lds128(hb + t * 1024);
#pragma unroll
    for (int s = 0; s < 20; ++s) {
      const int slot = (s / 5) % 3, nslot = ((s + 1) / 5) % 3;
      if (s == 4) { WAITV(13); LBAR(); }
      if (s == 5) dma_wd_chunk(wd, lds0, w, lane, 0);
      if (s == 9) { WAITV(22); LBAR(); dma_h(3, 0); }
      if (s == 14) { WAITV(2); LBAR(); dma_wd_chunk(wd, lds0, w, lane, 1); }
      if (s == 15) { BAR(); dma_wd_chunk(wd, lds0, w, lane, 2); }
      const u32x4 cg_ = ag[s % GWIN], cu_ = au[s % GWIN];
      if (s + GWIN < 20) { ag[s % GWIN] = bld128<AUX_SC1>(ws, wbase + (s + GWIN) * 2048); au[s % GWIN] = bld128<AUX_SC1>(ws, wbase + (s + GWIN) * 2048 + 1024); }
#pragma unroll
      for (int t = 0; t < 4; ++t) bb[1][t] = lds128(hb + slot * 40960 + (s % 5) * 8192 + (4 + t) * 1024);
      SBAR();
#pragma unroll
      for (int t = 0; t < 4; ++t) { aG[t] = MFMAS(asbf8(cg_), asbf8(bb[0][t]), aG[t]); aU[t] = MFMAS(asbf8(cu_), asbf8(bb[0][t]), aU[t]); }
      SBAR();
      if (s + 1 < 20) {
#pragma unroll
        for (int t = 0; t < 4; ++t) bb[0][t] = lds128(hb + nslot * 40960 + ((s + 1) % 5) * 8192 + t * 1024);
      }
      SBAR();
#pragma unroll
      for (int t = 0; t < 4; ++t) { aG[4 + t] = MFMAS(asbf8(cg_), asbf8(bb[1][t]), aG[4 + t]); aU[4 + t] = MFMAS(asbf8(cu_), asbf8(bb[1][t]), aU[4 + t]); }
      SBAR();
    }
    STAMPX(18);
    const float sg = swap32_sum(swap16_sum(((csp[0][0] + csp[0][1]) + (csp[0][2] + csp[0][3])) + csp[0][4])), su = swap32_sum(swap16_sum(((csp[1][0] + csp[1][1]) + (csp[1][2] + csp[1][3])) + csp[1][4]));
    float cg4[4], cu4[4];
#pragma unroll
    for (int j = 0; j < 4; ++j) { cg4[j] = __shfl(sg, 4 * g + j); cu4[j] = __shfl(su, 4 * g + j); }
#pragma unroll
    for (int i = 0; i < 4; ++i) {
      u32x2 pk[2];
#pragma unroll
      for (int e = 0; e < 2; ++e) {
        const int t = 2 * i + e;
        const f32x2 mr = *(LAS f32x2*)(lds + L4_ST + (t * 16 + t16) * 8);
        const float r2 = mr[1] * mr[1];
        float hv[4];
#pragma unroll
        for (int j = 0; j < 4; ++j) hv[j] = (aG[t][j] - mr[0] * cg4[j]) * (aU[t][j] - mr[0] * cu4[j]) * r2;
        pk[e] = u32x2{pk2(hv[0], hv[1]), pk2(hv[2], hv[3])};
      }
      const auto s0 = __builtin_amdgcn_permlane16_swap(pk[0][0], pk[1][0], false, false), s1 = __builtin_amdgcn_permlane16_swap(pk[0][1], pk[1][1], false, false);
      bst128<AUX_SC1>(ws, W_GF + (((mb * 8 + 2 * i + (g & 1)) * 54 + (u >> 1)) * 64 + (2 * (u & 1) + (g >> 1)) * 16 + t16) * 16, u32x4{s0[0], s1[0], s0[1], s1[1]});
    }
  } else {
    WAITV(5); LBAR();
    dma_wd_chunk(wd, lds0, w, lane, 0);
    WAITV(4); LBAR(); dma_h(3, 0);
    WAITV(0); LBAR(); dma_wd_chunk(wd, lds0, w, lane, 1);
    BAR(); dma_wd_chunk(wd, lds0, w, lane, 2);
  }
  STAMPX(21);
  BAR();
  if (w < 7) dma_wd_chunk7(wd, lds0, w, lane, 3);
  if (w < 7) WAITV(5); else WAITV(0);
  unsigned old = 0; if (lane == 0) old = __hip_atomic_fetch_add((LAS unsigned*)(lds + L4_CO), 1u, __ATOMIC_RELAXED, __HIP_MEMORY_SCOPE_WORKGROUP);
  if (lane == 0 && old == 7u) (void)__hip_atomic_fetch_add((GAS unsigned*)(wsp + W_CNTG + mb * 256), 1u, RLX_AGENT);
}

DI void gld128_sc1(u32x4& d, unsigned voff, const char* sbase) { asm volatile("global_load_dwordx4 %0, %1, %2 sc1" : "=v"(d) : "v"(voff), "s"(sbase) : "memory"); }
DI void phase_down(lptr lds, rsrc_t ws, int b, const u32x2 (&hres)[5], unsigned epoch) {
  char* wsp = kargs()->ws;
  const int tid = threadIdx.x, lane = tid & 63, w = rfl(tid >> 6);
  const int xcd = b & 7, rk = b >> 3, tg = 4 * xcd + (rk >> 3), cg = rk & 7;
  const int mt = w & 3, kh = w >> 2, t16 = lane & 15, g = lane >> 4, tq = t16 >> 2, tp = t16 & 3;
  const unsigned lds0 = (unsigned)(uintptr_t)lds, l16 = lane * 16;
  const char* wd = wsp + W_WD + (size_t)cg * FF * 160;
  wait_cnt2(wsp, W_CNTG + (tg >> 1) * 256, W_CNTG + (tg >> 1) * 256, 16u, epoch, 7);
  STAMPX(20);
  const char* abase = wsp + W_GF + (size_t)((tg * 4 + mt) * 54 + 27 * kh) * 1024;
  u32x4 a[4][3];
#pragma unroll
  for (int c = 0; c < 3; ++c)
#pragma unroll
    for (int k = 0; k < 3; ++k) gld128_sc1(a[c][k], l16 + (3 * c + k) * 1024, abase);
  f32x4 acc[5] = {};
  const unsigned brow = kh * 15360 + (16 * (g >> 1) + 4 * (g & 1) + tq) * 160 + tp * 8;
#define DOWN_WAIT(N, c) asm volatile("s_waitcnt vmcnt(" #N ")\n\ts_barrier" : "+v"(a[(c) & 3][0]), "+v"(a[(c) & 3][1]), "+v"(a[(c) & 3][2]) :: "memory")
  bf16x8 bf[2][5];
  { lptr bb = lds + l5_slot(0) + brow;
#pragma unroll
    for (int nt = 0; nt < 5; ++nt) bf[0][nt] = cat8(trread(bb + nt * 32), trread(bb + nt * 32 + 1280)); }
#pragma unroll
  for (int c = 0; c < 9; ++c) {
    asm volatile("" ::: "memory"); WAITL();
    if (c <= 1) DOWN_WAIT(6, c); else if (c <= 6) DOWN_WAIT(10, c); else if (c == 7) DOWN_WAIT(3, c); else DOWN_WAIT(0, c);
    if (c >= 1 && c + 3 < 9) dma_wd_chunk(wd, lds0, w, lane, c + 3);
    if (c + 3 < 9) {
#pragma unroll
      for (int k = 0; k < 3; ++k) gld128_sc1(a[(c + 3) & 3][k], l16 + (3 * (c + 3) + k) * 1024, abase); }
#pragma unroll
    for (int k = 0; k < 3; ++k) {
      const int sn = 3 * c + k + 1;
      if (sn < 27) { lptr bb = lds + l5_slot(sn / 3) + brow + (sn % 3) * 5120;
#pragma unroll
        for (int nt = 0; nt < 5; ++nt) bf[sn & 1][nt] = cat8(trread(bb + nt * 32), trread(bb + nt * 32 + 1280)); }
      SBAR();
#pragma unroll
      for (int nt = 0; nt < 5; ++nt) acc[nt] = MFMAS(bf[(3 * c + k) & 1][nt], asbf8(a[c & 3][k]), acc[nt]);
      SBAR();
    }
  }
  STAMPX(22);
  LAS f32x4* red = (LAS f32x4*)(lds + L5_RED) + mt * 320 + lane;
  if (kh == 1) {
#pragma unroll
    for (int nt = 0; nt < 5; ++nt) red[nt * 64] = acc[nt];
  }
  LBAR();
  if (kh == 0) {
    const int tok = tg * 64 + mt * 16 + t16; char* outp = kargs()->out;
#pragma unroll
    for (int nt = 0; nt < 5; ++nt) {
      const f32x4 o = red[nt * 64];
      const u32x2 r = {pk2(bflo(hres[nt][0]) + acc[nt][0] + o[0], bfhi(hres[nt][0]) + acc[nt][1] + o[1]), pk2(bflo(hres[nt][1]) + acc[nt][2] + o[2], bfhi(hres[nt][1]) + acc[nt][3] + o[3])};
      *(GAS u32x2*)(outp + (size_t)tok * 1280 + cg * 160 + nt * 32 + g * 8) = r;
    }
  }
}

#define CODE_BYTES 50176
__global__ void __launch_bounds__(NTHR, 2) k_block(Params p) {
  extern __shared__ __attribute__((aligned(16))) char smem[];
  lptr lds = (lptr)smem;
  const rsrc_t ws = __builtin_amdgcn_make_buffer_rsrc((void*)p.ws, 0, (int)W_END, 0x00020000);
  const int b = blockIdx.x;
  {
    unsigned long long pc; asm volatile("s_getpc_b64 %0" : "=s"(pc));
    const int ln = (b >> 3) * 16 + (threadIdx.x & 15);
    if (threadIdx.x >= 448 && (threadIdx.x & 63) < 16 && ln * 128 < CODE_BYTES) touch(ln * 128, (const char*)(pc & ~127ull), (unsigned)(uintptr_t)smem + 163584);
  }
  u32x4 q1; u32x2 q2; unsigned epoch; float kmax2 = 0.f;
  u32x2 hres[5];
  STAMPX(0);
  phase1(lds, ws, b, q1, q2, epoch, kmax2);
  STAMPX(5);
  phase_attn(lds, ws, b, q1, q2, epoch, kmax2);
  STAMPX(12);
  phase_oproj(lds, ws, b, hres, epoch);
  STAMPX(15);
  phase_gateup(lds, ws, b, epoch);
  STAMPX(19);
  phase_down(lds, ws, b, hres, epoch);
  STAMPX(23);
}

extern "C" void kernel_launch(void* const* d_in, const int* in_sizes, int n_in, void* d_out, int out_size, void* d_ws, size_t ws_size, hipStream_t stream) {
  Params p;
  p.x = (const char*)d_in[0]; p.wq = (const char*)d_in[1]; p.wk = (const char*)d_in[2]; p.wv = (const char*)d_in[3]; p.wo = (const char*)d_in[4];
  p.wg = (const char*)d_in[6]; p.wu = (const char*)d_in[7]; p.wd = (const char*)d_in[8];
  p.out = (char*)d_out; p.ws = (char*)d_ws;
  constexpr int LDS_BYTES = 163840;
  (void)hipFuncSetAttribute((const void*)k_block, hipFuncAttributeMaxDynamicSharedMemorySize, LDS_BYTES);
  k_block<<<NWG, NTHR, LDS_BYTES, stream>>>(p);
}
```

```cpp
#define SWEEP 7
#include <hip/hip_runtime.h>
#include <stdint.h>

#define DI __device__ __forceinline__
#define LAS __attribute__((address_space(3)))
#define GAS __attribute__((address_space(1)))
typedef short bf16x8 __attribute__((ext_vector_type(8)));
typedef short bf16x4 __attribute__((ext_vector_type(4)));
typedef __bf16 bfv2 __attribute__((ext_vector_type(2)));
typedef float f32x2 __attribute__((ext_vector_type(2)));
typedef float f32x4 __attribute__((ext_vector_type(4)));
typedef float f32x16 __attribute__((ext_vector_type(16)));
typedef unsigned u32x2 __attribute__((ext_vector_type(2)));
typedef unsigned u32x4 __attribute__((ext_vector_type(4)));
typedef LAS char* lptr;

constexpr int SEQ = 2048, DM = 640, FF = 1728;
constexpr int NWG = 256, NTHR = 512;
constexpr float LN_EPS = 1e-5f;
constexpr float QSCALE = 0.22360679774997896f * 1.4426950408889634f;
constexpr unsigned KV_TILE = 2688, KV_K2 = 1024, KV_V = 1280, KV_VS = 352;

constexpr unsigned W_EPOCH = 0, W_TMO = 256;
constexpr unsigned W_FKV = 1024  , W_FRT = 2048  ;
constexpr unsigned W_CNTO = 8192, W_CNTH = 16384, W_CNTG = 24576;
constexpr unsigned W_KV = 65536;
constexpr unsigned W_OF = W_KV + 64 * KV_TILE;
constexpr unsigned W_HF = W_OF + SEQ * DM * 2;
constexpr unsigned W_HST = W_HF + SEQ * DM * 2;
constexpr unsigned W_GF = W_HST + 32 * 8 * 64 * 8;
constexpr unsigned W_WGU = W_GF + 128 * 54 * 1024;
constexpr unsigned W_WD = W_WGU + 108 * 40 * 1024;
constexpr unsigned W_WO = W_WD + 8 * FF * 160;
constexpr unsigned W_CSP = W_WO + 8 * DM * 160;
constexpr unsigned W_END = W_CSP + 108 * 2 * 20 * 64;

#define STAMPW() do { } while (0)
#define STAMP(i) do { } while (0)
#define STW 0
#define STAMPX(i) do { STAMP(i); if (STW == (i)) STAMPW(); } while (0)

struct Params {
  const char* x; const char* wq; const char* wk; const char* wv; const char* wo;
  const char* wg; const char* wu; const char* wd;
  char* out; char* ws;
};

typedef const __attribute__((address_space(4))) Params* kargs_t;
DI kargs_t kargs() { kargs_t a = (kargs_t)__builtin_amdgcn_kernarg_segment_ptr(); asm volatile("" : "+s"(a)); return a; }
constexpr int AUX_SC1 = 16;
#define MFMA16(a, b, c) __builtin_amdgcn_mfma_f32_32x32x16_bf16((a), (b), (c), 0, 0, 0)
#define MFMA8(a, b, c) __builtin_amdgcn_mfma_f32_32x32x8bf16_1k((a), (b), (c), 0, 0, 0)
#define MFMAS(a, b, c) __builtin_amdgcn_mfma_f32_16x16x32_bf16((a), (b), (c), 0, 0, 0)
#define SBAR() __builtin_amdgcn_sched_barrier(0)
#define WAITV(n) asm volatile("s_waitcnt vmcnt(" #n ")" ::: "memory")
#define BAR() asm volatile("s_barrier" ::: "memory")
#define WAITL() __builtin_amdgcn_s_waitcnt(0xC07F)
#define LBAR() do { asm volatile("" ::: "memory"); WAITL(); BAR(); } while (0)
#define PIN(x) asm volatile("" : "+v"(x))

DI unsigned pk2(float lo, float hi) { f32x2 v = {lo, hi}; return __builtin_bit_cast(unsigned, __builtin_convertvector(v, bfv2)); }
DI float bflo(unsigned v) { return __uint_as_float(v << 16); }
DI float bfhi(unsigned v) { return __uint_as_float(v & 0xffff0000u); }
DI float dot2(unsigned a, unsigned b, float c) { return __builtin_amdgcn_fdot2_f32_bf16(__builtin_bit_cast(bfv2, a), __builtin_bit_cast(bfv2, b), c, false); }
constexpr unsigned ONES2 = 0x3f803f80u;
DI float swap32_sum(float v) { auto r = __builtin_amdgcn_permlane32_swap(__float_as_uint(v), __float_as_uint(v), false, false); return __uint_as_float(r[0]) + __uint_as_float(r[1]); }
DI float swap32_max(float v) { auto r = __builtin_amdgcn_permlane32_swap(__float_as_uint(v), __float_as_uint(v), false, false); return fmaxf(__uint_as_float(r[0]), __uint_as_float(r[1])); }
DI float swap16_sum(float v) { auto r = __builtin_amdgcn_permlane16_swap(__float_as_uint(v), __float_as_uint(v), false, false); return __uint_as_float(r[0]) + __uint_as_float(r[1]); }
template <int CTRL> DI float dppf(float v) { return __uint_as_float(__builtin_amdgcn_update_dpp(0u, __float_as_uint(v), CTRL, 0xf, 0xf, true)); }
DI float sum8(float v) { v += dppf<0xB1>(v); v += dppf<0x4E>(v); return v + dppf<0x141>(v); }
DI float max16(float v) { v = fmaxf(v, dppf<0x128>(v)); v = fmaxf(v, dppf<0x124>(v)); v = fmaxf(v, dppf<0x122>(v)); return fmaxf(v, dppf<0x121>(v)); }
DI float max64(float v) { v = max16(v); auto r = __builtin_amdgcn_permlane16_swap(__float_as_uint(v), __float_as_uint(v), false, false);
  return swap32_max(fmaxf(__uint_as_float(r[0]), __uint_as_float(r[1]))); }
typedef short v4i16_t __attribute__((ext_vector_type(4)));
DI bf16x4 trread(lptr p) { return __builtin_bit_cast(bf16x4, __builtin_amdgcn_ds_read_tr16_b64_v4i16((LAS v4i16_t*)p)); }
DI bf16x8 cat8(bf16x4 a, bf16x4 b) { return __builtin_shufflevector(a, b, 0, 1, 2, 3, 4, 5, 6, 7); }
DI u32x4 lds128(lptr p) { return *(LAS u32x4*)p; }
DI u32x2 lds64(lptr p) { return *(LAS u32x2*)p; }
DI bf16x8 asbf8(u32x4 v) { return __builtin_bit_cast(bf16x8, v); }
DI bf16x4 asbf4(u32x2 v) { return __builtin_bit_cast(bf16x4, v); }
DI u32x4 asu4(bf16x8 v) { return __builtin_bit_cast(u32x4, v); }
DI unsigned rfl(unsigned v) { return __builtin_amdgcn_readfirstlane(v); }
typedef __amdgpu_buffer_rsrc_t rsrc_t;
template <int AUX> DI u32x4 bld128(rsrc_t r, unsigned off) { return __builtin_amdgcn_raw_buffer_load_b128(r, off, 0, AUX); }
template <int AUX> DI u32x2 bld64(rsrc_t r, unsigned off) { return __builtin_amdgcn_raw_buffer_load_b64(r, off, 0, AUX); }
template <int AUX> DI unsigned bld32(rsrc_t r, unsigned off) { return __builtin_amdgcn_raw_buffer_load_b32(r, off, 0, AUX); }
template <int AUX> DI void bst128(rsrc_t r, unsigned off, u32x4 v) { __builtin_amdgcn_raw_buffer_store_b128(v, r, off, 0, AUX); }
template <int AUX> DI void bst64(rsrc_t r, unsigned off, u32x2 v) { __builtin_amdgcn_raw_buffer_store_b64(v, r, off, 0, AUX); }
template <int AUX> DI void bst32(rsrc_t r, unsigned off, unsigned v) { __builtin_amdgcn_raw_buffer_store_b32(v, r, off, 0, AUX); }
DI void glds(unsigned voff, const char* sbase, unsigned lds_base) {
  asm volatile("s_mov_b32 m0, %2\n\ts_nop 0\n\tglobal_load_lds_dwordx4 %0, %1" :: "v"(voff), "s"(sbase), "s"(rfl(lds_base)) : "memory");
}
DI void glds_sc1(unsigned voff, const char* sbase, unsigned lds_base) {
  asm volatile("s_mov_b32 m0, %2\n\ts_nop 0\n\tglobal_load_lds_dwordx4 %0, %1 sc1" :: "v"(voff), "s"(sbase), "s"(rfl(lds_base)) : "memory");
}
DI void touch(unsigned voff, const char* sbase, unsigned lds_dump) {
  asm volatile("s_mov_b32 m0, %2\n\ts_nop 0\n\tglobal_load_lds_dword %0, %1" :: "v"(voff), "s"(sbase), "s"(rfl(lds_dump)) : "memory");
}
DI void touch_sc1(unsigned voff, const char* sbase, unsigned lds_dump) {
  asm volatile("s_mov_b32 m0, %2\n\ts_nop 0\n\tglobal_load_lds_dword %0, %1 sc1" :: "v"(voff), "s"(sbase), "s"(rfl(lds_dump)) : "memory");
}

#define RLX_AGENT __ATOMIC_RELAXED, __HIP_MEMORY_SCOPE_AGENT
constexpr unsigned SPIN_CAP = 1u << 17;
DI bool spin_fail(unsigned& spins, char* wsp, unsigned epoch) {
  __builtin_amdgcn_s_sleep(1);
  GAS unsigned* tmo = (GAS unsigned*)(wsp + W_TMO);
  if (++spins > SPIN_CAP) { __hip_atomic_store(tmo, epoch, RLX_AGENT); return true; }
  return (spins & 255u) == 0u && rfl(__hip_atomic_load(tmo, RLX_AGENT)) == epoch;
}
template <int NW> DI void wait_flags(char* wsp, unsigned off, unsigned epoch, int lane) {
  for (unsigned spins = 0;;) {
    bool ok = true;
#pragma unroll
    for (int i = 0; i < NW; ++i) ok &= __hip_atomic_load((GAS unsigned*)(wsp + off) + i * 64 + lane, RLX_AGENT) == epoch;
    if (__all(ok)) break;
    if (spin_fail(spins, wsp, epoch)) break;
  }
  __builtin_amdgcn_fence(__ATOMIC_ACQUIRE, "wavefront");
}
DI float wait_granules(char* wsp, unsigned off, unsigned epoch, int lane) {
  unsigned mx;
  for (unsigned spins = 0;;) {
    const unsigned long long a = __hip_atomic_load((GAS unsigned long long*)(wsp + off) + lane, RLX_AGENT), b = __hip_atomic_load((GAS unsigned long long*)(wsp + off) + 64 + lane, RLX_AGENT);
    mx = max((unsigned)a, (unsigned)b);
    if (__all((unsigned)(a >> 32) == epoch && (unsigned)(b >> 32) == epoch)) break;
    if (spin_fail(spins, wsp, epoch)) break;
  }
  __builtin_amdgcn_fence(__ATOMIC_ACQUIRE, "wavefront");
  return __uint_as_float(mx);
}
DI void wait_cnt2(char* wsp, unsigned off0, unsigned off1, unsigned want, unsigned epoch, int poller) {
  if ((int)(threadIdx.x >> 6) == poller) {
    GAS unsigned* word = (GAS unsigned*)(wsp + ((threadIdx.x & 1) ? off1 : off0));
    for (unsigned spins = 0;;) {
      if (__all(__hip_atomic_load(word, RLX_AGENT) >= want)) break;
      if (spin_fail(spins, wsp, epoch)) break;
    }
    __builtin_amdgcn_fence(__ATOMIC_ACQUIRE, "wavefront");
  }
  LBAR();
}
DI void signal_cnt(char* wsp, unsigned off) {
  WAITV(0);
  LBAR();
  if (threadIdx.x == 0) (void)__hip_atomic_fetch_add((GAS unsigned*)(wsp + off), 1u, RLX_AGENT);
}
DI void lds_add1(lptr p) { (void)__hip_atomic_fetch_add((LAS unsigned*)p, 1u, __ATOMIC_RELAXED, __HIP_MEMORY_SCOPE_WORKGROUP); }
DI void lds_wait_ge(lptr p, unsigned want) {
  while ((unsigned)rfl(__hip_atomic_load((LAS unsigned*)p, __ATOMIC_RELAXED, __HIP_MEMORY_SCOPE_WORKGROUP)) < want) __builtin_amdgcn_s_sleep(2);
  asm volatile("" ::: "memory");
}

DI void dma_kv3(const char* kvg, unsigned lds0, int c, int j, int lane) {
  const unsigned dst = lds0 + (c == 15 ? 0 : c) * 4 * KV_TILE, l16 = lane * 16; const char* src = kvg + c * 4 * KV_TILE;
  glds_sc1(l16, src + j * 1024, dst + j * 1024);
  glds_sc1(l16, src + (j + 4) * 1024, dst + (j + 4) * 1024);
  const int n = j == 3 ? 7 : j + 8;
  if (j != 2 || lane < 32) glds_sc1(l16, src + n * 1024, dst + n * 1024);
}

constexpr unsigned L1_X = 0, L1_S = 81920, L1_WK = L1_S + 4 * 10240, L1_PART = L1_WK + 25600;
constexpr unsigned L1_CS = L1_PART  ;
constexpr unsigned L1_ST = 161792  , L1_KC = L1_ST + 512  , L1_KM = L1_KC + 128, L1_DUMP = L1_KM + 16;
static_assert(L1_PART + 12672 <= 161280 && L1_DUMP + 256 <= 163584, "lds");

DI void phase1(lptr lds, rsrc_t ws, int b, u32x4& q1, u32x2& q2, unsigned& epoch, float& kmax2) {
  kargs_t pa = kargs();
  const int tid = threadIdx.x, lane = tid & 63, w = rfl(tid >> 6);
  const int r32 = lane & 31, h = lane >> 5;
  const int xcd = b & 7, rk = b >> 3, tg = 4 * xcd + (rk >> 3), hg = rk & 7;
  const unsigned lds0 = (unsigned)(uintptr_t)lds, l16 = lane * 16;
  unsigned ep = __hip_atomic_load((GAS unsigned*)(pa->ws + W_EPOCH), RLX_AGENT);
  {
    const char* xb = pa->x + (size_t)(tg * 64 + 8 * w) * 1280;
    const int tl = lane >> 4;
    unsigned v0 = tl * 1280 + ((lane ^ (8 * w + tl)) & 15) * 16, v1 = (4 + tl) * 1280 + ((lane ^ (8 * w + 4 + tl)) & 15) * 16, xd = lds0 + L1_X + 8 * w * 256;
#pragma unroll 1
    for (int a = 0; a < 5; ++a) { glds(v0, xb, xd); glds(v1, xb, xd + 1024); v0 += 256; v1 += 256; xd += 16384; }
  }
  if (hg < 4) {
    const char* W = (hg < 2 ? pa->wk : pa->wv) + w * 1024;
#pragma unroll
    for (int i = 0; i < 3; ++i) glds(l16 + i * 8192, W, lds0 + L1_WK + w * 1024 + i * 8192);
    if (w == 0) glds(l16 + 3 * 8192, W, lds0 + L1_WK + 3 * 8192);
  }
  const char* wqb = pa->wq + hg * 160;
  unsigned vq[3], dq[3];
#pragma unroll
  for (int i = 0; i < 3; ++i) {
    const int n = i < 2 ? w + 8 * i : (w < 6 ? w + 16 : w + 8);
    const int slot = 6 * n + lane / 10, row = (slot & ~7) | ((slot & 1) << 2) | ((slot >> 1) & 3);
    vq[i] = row * 1280 + (lane % 10) * 16; dq[i] = n * 960;
  }
  const bool m3 = w < 5 || (w == 5 ? lane < 20 : lane == 0);
  auto dma_pair = [&](const char* src, unsigned dst) {
    glds(vq[0], src, dst + dq[0]);
    glds(vq[1], src, dst + dq[1]);
    if (m3) glds(vq[2], src, dst + dq[2]);
  };
  SBAR();
  STAMPX(8);
  const int i16 = lane & 15, tq = i16 >> 2, tp = i16 & 3, grp = (lane >> 4) & 1;
  WAITV(0);
  LBAR();
  PIN(ep); epoch = rfl(ep) + 1u;
  STAMPX(1);
  const int kth = hg & 1; const bool isV = hg >= 2;
  f32x16 kacc = {}; float kcs = 0.f; unsigned kn2 = 0u;
  auto dma_first = [&]() { dma_pair(wqb, lds0 + L1_S); dma_pair(wqb + 128 * 1280, lds0 + L1_S + 20480); };
  if (w >= 4) {
    dma_first();
#pragma unroll
    for (int ps = 0; ps < 2; ++ps) {
      const int t = (ps ? 32 - 32 * kth : 32 * kth) + 8 * (w - 4) + (lane >> 3), e = lane & 7;
      u32x4 v[10];
#pragma unroll
      for (int i = 0; i < 10; ++i) v[i] = lds128(lds + L1_X + (i >> 1) * 16384 + t * 256 + ((e + 8 * (i & 1) + 8 * ((t >> 1) & 1)) & 15) * 16);
      float a4[4] = {}, b4[4] = {};
#pragma unroll
      for (int i = 0; i < 10; ++i)
#pragma unroll
        for (int j = 0; j < 4; ++j) { a4[j] = dot2(v[i][j], v[i][j], a4[j]); b4[j] = dot2(v[i][j], ONES2, b4[j]); }
      const float sq = sum8((a4[0] + a4[1]) + (a4[2] + a4[3])), sm = sum8((b4[0] + b4[1]) + (b4[2] + b4[3]));
      const float mean = sm * (1.f / DM), rstd = rsqrtf(fmaxf(sq * (1.f / DM) - mean * mean, 0.f) + LN_EPS);
      if (e == 0) { ((LAS float*)(lds + L1_ST))[t] = mean; ((LAS float*)(lds + L1_ST))[64 + t] = rstd; }
      if (ps == 0) {
        LBAR(); dma_pair(wqb + 256 * 1280, lds0 + L1_WK);
        const int ln = ((rk >> 3) * 4 + (w - 4)) * 32 + r32;
        if (h == 0) touch((ln >> 1) * 1280 + (ln & 1) * 128, wqb + 384 * 1280 - ((hg * 160) & 127), lds0 + L1_DUMP);
      }
    }
  } else if (hg < 4) {
    lptr wb = lds + L1_WK + (160 * w + 8 * h + tq) * 40 + grp * 32 + 8 * tp;
    const int tok = kth * 32 + r32;
    u32x4 xf[10]; bf16x8 wf[10];
#pragma unroll
    for (int i = 0; i < 10; ++i) { const int c = 20 * w + 2 * i + h;
      xf[i] = lds128(lds + L1_X + (c >> 4) * 16384 + tok * 256 + ((c ^ tok) & 15) * 16); wf[i] = cat8(trread(wb + i * 640), trread(wb + i * 640 + 160)); }
    SBAR();
    if (w != 0) dma_first();
    if (isV) {
#pragma unroll
      for (int i = 0; i < 10; ++i) kacc = MFMA16(asbf8(xf[i]), wf[i], kacc);
    } else {
#pragma unroll
      for (int i = 0; i < 10; ++i) kacc = MFMA16(wf[i], asbf8(xf[i]), kacc);
    }
    float c4[4] = {};
#pragma unroll
    for (int i = 0; i < 10; ++i) { const u32x4 wu = asu4(wf[i]);
#pragma unroll
      for (int j = 0; j < 4; ++j) c4[j] = dot2(wu[j], ONES2, c4[j]); }
    kcs = swap32_sum((c4[0] + c4[1]) + (c4[2] + c4[3]));
    if (w > 0) {
      LAS f32x4* pa = (LAS f32x4*)(lds + L1_PART + (w - 1) * 4224) + lane;
#pragma unroll
      for (int g = 0; g < 4; ++g) pa[g * 64] = f32x4{kacc[4 * g], kacc[4 * g + 1], kacc[4 * g + 2], kacc[4 * g + 3]};
      if (h == 0) ((LAS float*)(lds + L1_PART + (w - 1) * 4224 + 4096))[r32] = kcs;
    }
  } else dma_first();
  if (w < 4) {
    LBAR();
    if (w != 0 || hg >= 4) dma_pair(wqb + 256 * 1280, lds0 + L1_WK);
  }
  if (hg < 4 && w == 0) {
#pragma unroll
    for (int v = 0; v < 3; ++v) {
      LAS f32x4* pa = (LAS f32x4*)(lds + L1_PART + v * 4224) + lane;
#pragma unroll
      for (int g = 0; g < 4; ++g) { const f32x4 t = pa[g * 64]; kacc[4 * g] += t[0]; kacc[4 * g + 1] += t[1]; kacc[4 * g + 2] += t[2]; kacc[4 * g + 3] += t[3]; }
      kcs += ((LAS float*)(lds + L1_PART + v * 4224 + 4096))[r32];
    }
    const int tile = tg * 2 + kth;
    const LAS float* st = (const LAS float*)(lds + L1_ST) + kth * 32;
    if (lane == 0 && hg == 0) { bst32<AUX_SC1>(ws, W_CNTO + tg * 256, 0u); bst32<AUX_SC1>(ws, W_CNTH + tg * 256, 0u); }
    if (lane == 0 && hg == 1 && !(tg & 1)) bst32<AUX_SC1>(ws, W_CNTG + (tg >> 1) * 256, 0u);
    if (!isV) {
      LAS float* tab = (LAS float*)(lds + L1_KC);
      if (h == 0) tab[r32] = kcs;
      const float mean = st[r32], rstd = st[64 + r32];
      asm volatile("" ::: "memory"); WAITL(); asm volatile("" ::: "memory");
      float kv[12];
#pragma unroll
      for (int g = 0; g < 3; ++g) { const f32x4 c4 = *(LAS f32x4*)(tab + 8 * g + 4 * h);
#pragma unroll
        for (int j = 0; j < 4; ++j) kv[4 * g + j] = (kacc[4 * g + j] - mean * c4[j]) * rstd; }
      const u32x4 k1 = {pk2(kv[0], kv[1]), pk2(kv[2], kv[3]), pk2(kv[4], kv[5]), pk2(kv[6], kv[7])};
      const u32x2 k2 = {pk2(kv[8], kv[9]), pk2(kv[10], kv[11])};
      bst128<AUX_SC1>(ws, W_KV + tile * KV_TILE + l16, k1);
      if (h == 0) bst64<AUX_SC1>(ws, W_KV + tile * KV_TILE + KV_K2 + r32 * 8, k2);
      float n2 = 0.f;
#pragma unroll
      for (int j = 0; j < 4; ++j) n2 = dot2(k1[j], k1[j], n2);
      if (h == 0) { n2 = dot2(k2[0], k2[0], n2); n2 = dot2(k2[1], k2[1], n2); }
      kn2 = __float_as_uint(max64(swap32_sum(n2)));
    } else {
      const unsigned cv = r32 == 20 ? ONES2 : 0u;
#pragma unroll
      for (int s = 0; s < 2; ++s) {
        float vv[8];
#pragma unroll
        for (int g = 0; g < 2; ++g) { const f32x4 m4 = *(const LAS f32x4*)(st + 16 * s + 8 * g + 4 * h), r4 = *(const LAS f32x4*)(st + 64 + 16 * s + 8 * g + 4 * h);
#pragma unroll
          for (int j = 0; j < 4; ++j) vv[4 * g + j] = (kacc[8 * s + 4 * g + j] - m4[j] * kcs) * r4[j]; }
        u32x4 o = {pk2(vv[0], vv[1]), pk2(vv[2], vv[3]), pk2(vv[4], vv[5]), pk2(vv[6], vv[7])};
        if (r32 >= 20) o = u32x4{cv, cv, cv, cv};
        if (r32 < 22) bst128<AUX_SC1>(ws, W_KV + tile * KV_TILE + KV_V + (2 * s + h) * KV_VS + r32 * 16, o);
      }
    }
  }
  if (hg < 4 && w == 0) {
    dma_first(); dma_pair(wqb + 256 * 1280, lds0 + L1_WK);
    WAITV(9);
    if (lane == 0) __hip_atomic_store((GAS unsigned long long*)(pa->ws + W_FKV) + tg * 4 + hg, ((unsigned long long)epoch << 32) | kn2, RLX_AGENT);
    WAITV(4);
  } else if (w >= 4) WAITV(4); else WAITV(3);
  LBAR();
  STAMPX(2);
  {
    const int hh = w & 3, th = w >> 2, tok = th * 32 + r32;
    f32x16 acc = {}; float c4[4] = {};
    const unsigned wrow = (8 * h + 2 * tq) * 160 + hh * 40 + grp * 32 + 8 * tp;
    unsigned xo[8];
#pragma unroll
    for (int i = 0; i < 8; ++i) xo[i] = L1_X + tok * 256 + (((2 * i + h) ^ tok) & 15) * 16;
    u32x4 xf[2][4]; bf16x8 wf[2][4];
    unsigned xa = 0, wa = L1_S + wrow;
#define Q_RD1(e, ks, XA, WA) do { xf[e][ks] = lds128(lds + xo[4 * (e) + (ks)] + (XA)); lptr a_ = lds + (WA) + (e) * 10240 + (ks) * 2560; wf[e][ks] = cat8(trread(a_), trread(a_ + 160)); } while (0)
#define Q_STEP(e, XA, WA) do { _Pragma("unroll") for (int ks = 0; ks < 4; ++ks) { acc = MFMA16(wf[e][ks], asbf8(xf[e][ks]), acc); Q_RD1(1 - (e), ks, XA, WA); SBAR(); } } while (0)
#define Q_CS(e) do { _Pragma("unroll") for (int ks = 0; ks < 4; ++ks) { const u32x4 wu = asu4(wf[e][ks]); _Pragma("unroll") for (int j = 0; j < 4; ++j) c4[j] = dot2(wu[j], ONES2, c4[j]); } } while (0)
#pragma unroll
    for (int ks = 0; ks < 4; ++ks) Q_RD1(0, ks, xa, wa);
#pragma unroll 1
    for (int pr = 0; pr < 5; ++pr) {
      Q_STEP(0, xa, wa);
      if (pr == 1 || pr == 2) WAITV(3); else if (pr == 3) WAITV(0);
      if (pr == 2 && w == 0) {
        const float km = max64(wait_granules(pa->ws, W_FKV, epoch, lane));
        if (lane == 0) *(LAS float*)(lds + L1_KM) = km;
      }
      LBAR();
      if (pr == 2) {
        kmax2 = *(LAS float*)(lds + L1_KM);
        if (th == 0) { dma_kv3(pa->ws + W_KV, lds0, 0, hh, lane); dma_kv3(pa->ws + W_KV, lds0, 2, hh, lane); } else dma_kv3(pa->ws + W_KV, lds0, 1, hh, lane);
      }
      const unsigned wn = pr == 1 ? L1_WK + wrow : (pr == 2 || pr == 4 ? L1_S + wrow : wa + 20480);
      if (pr < 2) dma_pair(wqb + (size_t)(pr + 3) * 128 * 1280, lds0 + L1_S + pr * 20480);
      if (th == 0) Q_CS(0); else Q_CS(1);
      SBAR();
      xa = pr < 4 ? xa + 16384 : 0;
      Q_STEP(1, xa, wn);
      wa = wn;
    }
#undef Q_RD1
#undef Q_STEP
#undef Q_CS
    const float cs = swap32_sum((c4[0] + c4[1]) + (c4[2] + c4[3]));
    if (h == 0) ((LAS float*)(lds + L1_CS))[w * 32 + r32] = cs;
    const float mean = ((LAS float*)(lds + L1_ST))[tok], rstd = ((LAS float*)(lds + L1_ST))[64 + tok] * QSCALE;
    LBAR();
    float qv[12];
#pragma unroll
    for (int g = 0; g < 3; ++g) {
      const f32x4 ca = *(LAS f32x4*)(lds + L1_CS + (hh * 32 + 8 * g + 4 * h) * 4), cb = *(LAS f32x4*)(lds + L1_CS + ((hh + 4) * 32 + 8 * g + 4 * h) * 4);
#pragma unroll
      for (int j = 0; j < 4; ++j) qv[4 * g + j] = (acc[4 * g + j] - mean * (ca[j] + cb[j])) * rstd;
    }
    q1 = u32x4{pk2(qv[0], qv[1]), pk2(qv[2], qv[3]), pk2(qv[4], qv[5]), pk2(qv[6], qv[7])};
    q2 = h ? u32x2{0u, 0u} : u32x2{pk2(qv[8], qv[9]), pk2(qv[10], qv[11])};
  }
  STAMPX(3);
}

constexpr unsigned L2_CO = 60 * KV_TILE, L2_CR = L2_CO + 16, L2_CP = L2_CO + 32, L2_CS = L2_CO + 48, L2_CD = L2_CO + 112, L2_CQ = L2_CO + 128, L2_CF = L2_CO + 144, L2_DUMP = L2_CO + 256;
constexpr unsigned L2_RT = 52 * KV_TILE;
constexpr unsigned L3_WO = 4 * KV_TILE, L3_RED = L3_WO + 102400;
#define PRIO_M 3
#define EXP2(a) __builtin_amdgcn_exp2f(a)

DI f32x16 attn_exact(rsrc_t ws, u32x4 q1, u32x2 q2, int lane) {
  const int r32 = lane & 31, h = lane >> 5, rv = r32 < 21 ? r32 : 21;
  float mx = -3.0e38f;
#pragma unroll 1
  for (int t = 0; t < 64; ++t) {
    const unsigned base = W_KV + t * KV_TILE;
    f32x16 s = MFMA8(asbf4(bld64<AUX_SC1>(ws, base + KV_K2 + r32 * 8)), asbf4(q2), f32x16{});
    s = MFMA16(asbf8(bld128<AUX_SC1>(ws, base + lane * 16)), asbf8(q1), s);
#pragma unroll
    for (int j = 0; j < 16; ++j) mx = fmaxf(mx, s[j]);
  }
  mx = swap32_max(mx);
  f32x16 o = {};
#pragma unroll 1
  for (int t = 0; t < 64; ++t) {
    const unsigned base = W_KV + t * KV_TILE;
    f32x16 s = MFMA8(asbf4(bld64<AUX_SC1>(ws, base + KV_K2 + r32 * 8)), asbf4(q2), f32x16{});
    s = MFMA16(asbf8(bld128<AUX_SC1>(ws, base + lane * 16)), asbf8(q1), s);
    u32x4 pp[2];
#pragma unroll
    for (int j = 0; j < 8; ++j) pp[j >> 2][j & 3] = pk2(EXP2(s[2 * j] - mx), EXP2(s[2 * j + 1] - mx));
#pragma unroll
    for (int e = 0; e < 2; ++e) o = MFMA16(asbf8(bld128<AUX_SC1>(ws, base + KV_V + (2 * e + h) * KV_VS + rv * 16)), asbf8(pp[e]), o);
  }
  return o;
}

DI void phase_attn(lptr lds, rsrc_t ws, int b, const u32x4 q1, const u32x2 q2, unsigned epoch, float kmax2) {
  const int tid = threadIdx.x, lane = tid & 63, w = rfl(tid >> 6);
  const int r32 = lane & 31, h = lane >> 5;
  const int xcd = b & 7, rk = b >> 3, tg = 4 * xcd + (rk >> 3), hg = rk & 7;
  const int hh = w & 3, th = w >> 2;
  const unsigned lds0 = (unsigned)(uintptr_t)lds, l16 = lane * 16;
  if (tid < 10) *(LAS unsigned*)(lds + L2_CO + tid * 16) = 0u;
  f32x16 cb;
  {
    float n2 = 0.f;
#pragma unroll
    for (int j = 0; j < 4; ++j) n2 = dot2(q1[j], q1[j], n2);
    n2 = dot2(q2[0], q2[0], n2); n2 = dot2(q2[1], q2[1], n2);
    const float ref = sqrtf(swap32_sum(n2) * kmax2) * 1.001f;
#pragma unroll
    for (int j = 0; j < 16; ++j) cb[j] = -ref;
    PIN(cb);
  }
  const int rv = r32 < 21 ? r32 : 21;
  const unsigned oK1 = l16, oK2 = KV_K2 + r32 * 8, oV = KV_V + h * KV_VS + rv * 16;
  f32x16 o = {};
  LBAR();
  STAMPX(9);
  {
    lptr aK1 = lds + oK1; lptr aK2 = lds + oK2; lptr aV = lds + oV;
    u32x4 k1 = lds128(aK1); u32x2 k2 = lds64(aK2);
    f32x16 sc = MFMA8(asbf4(k2), asbf4(q2), cb); sc = MFMA16(asbf8(k1), asbf8(q1), sc);
    k1 = lds128(aK1 + KV_TILE); k2 = lds64(aK2 + KV_TILE);
    u32x4 v0 = lds128(aV), v1 = {}, p0, p1 = {};
    u32x4 k1b, v0b, v1b; u32x2 k2b;
    f32x16 sn;
#define GRP(MF, SC, i, P, j) do { MF; SBAR(); { const float e0 = EXP2(SC[i]), e1 = EXP2(SC[i + 1]), e2 = EXP2(SC[i + 2]), e3 = EXP2(SC[i + 3]); P[j] = pk2(e0, e1); P[j + 1] = pk2(e2, e3); } PIN(P); SBAR(); } while (0)
#define TILE(SC, SN, K1, K2, V0, V1, K1N, K2N, V0N, V1N, PK1, PK2, PV0, PV1) do { \
      WAITL(); \
      K2N = lds64(PK2); K1N = lds128(PK1); V0N = lds128(PV0); V1N = lds128(PV1); SBAR(); \
      GRP(SN = MFMA8(asbf4(K2), asbf4(q2), cb), SC, 0, p0, 0); \
      GRP(o = MFMA16(asbf8(V1), asbf8(p1), o), SC, 4, p0, 2); \
      GRP(SN = MFMA16(asbf8(K1), asbf8(q1), SN), SC, 8, p1, 0); \
      GRP(o = MFMA16(asbf8(V0), asbf8(p0), o), SC, 12, p1, 2); } while (0)
#define TA(T) TILE(sc, sn, k1, k2, v0, v1, k1b, k2b, v0b, v1b, aK1 + ((T) + 2) * KV_TILE, aK2 + ((T) + 2) * KV_TILE, aV + ((T) + 1) * KV_TILE, aV + (T) * KV_TILE + 2 * KV_VS)
#define TB(T) TILE(sn, sc, k1b, k2b, v0b, v1b, k1, k2, v0, v1, aK1 + ((T) + 2) * KV_TILE, aK2 + ((T) + 2) * KV_TILE, aV + ((T) + 1) * KV_TILE, aV + (T) * KV_TILE + 2 * KV_VS)
#define ADV() do { aK1 += 8 * KV_TILE; aK2 += 8 * KV_TILE; aV += 8 * KV_TILE; } while (0)
#define BODY8() do { TA(0); TB(1); TA(2); TB(3); TA(4); TB(5); TA(6); TB(7); ADV(); } while (0)
    if (th == 1) {
      const char* kvg = kargs()->ws + W_KV;
#define DMA3(c) do { dma_kv3(kvg, lds0, c, hh, lane); SBAR(); } while (0)
      __builtin_amdgcn_s_setprio(2);
      PIN(sc);
      DMA3(3); TA(0); DMA3(4); TB(1); DMA3(5); TA(2); DMA3(6); TB(3);
      DMA3(7); TA(4); DMA3(8); TB(5); DMA3(9); TA(6); DMA3(10); TB(7); ADV();
      if (lane == 0) lds_add1(lds + L2_CP);
      WAITV(6); if (lane == 0) lds_add1(lds + L2_CS);
      lds_wait_ge(lds + L2_CS, 4u);
      DMA3(11); TA(0); DMA3(12); TB(1); TA(2); TB(3); TA(4); TB(5); TA(6); TB(7); ADV();
      WAITV(0); if (lane == 0) lds_add1(lds + L2_CS + 16);
#pragma nounroll
      for (int m = 2; m < 6; ++m) {
        if (m == 4) { lds_wait_ge(lds + L2_CS + 16, 4u); lds_wait_ge(lds + L2_CR, 4u); DMA3(13); DMA3(14); }
        if (m == 5) { WAITV(0); if (lane == 0) lds_add1(lds + L2_CS + 32); lds_wait_ge(lds + L2_CP, 8u); DMA3(15); }
        if (m == PRIO_M) __builtin_amdgcn_s_setprio(0);
        BODY8();
      }
      WAITV(0); if (lane == 0) lds_add1(lds + L2_CS + 48);
#undef DMA3
    } else {
      __builtin_amdgcn_s_setprio(1);
      const int ga = b * 4 + hh;
      u32x4 rg[4];
      auto gu_load = [&](int gj) {
        kargs_t pa = kargs(); const int cgf = gj / 40, r = gj % 40;
        const rsrc_t wr = __builtin_amdgcn_make_buffer_rsrc((void*)((r & 1) ? pa->wu : pa->wg), 0, DM * FF * 2, 0x00020000);
        const unsigned vo = (32 * (r >> 1) + (lane >> 3)) * (FF * 2) + cgf * 128 + (lane & 7) * 16;
#pragma unroll
        for (int i = 0; i < 4; ++i) rg[i] = __builtin_amdgcn_raw_buffer_load_b128(wr, vo, i * 8 * FF * 2, 0);
      };
      auto gu_store = [&](int gj) {
        const int cgf = gj / 40, r = gj % 40, i16 = lane & 15, tq = i16 >> 2, tp = i16 & 3, g4 = lane >> 4;
        lptr scr = lds + L2_RT + hh * 4096;
#pragma unroll
        for (int i = 0; i < 4; ++i) *(LAS u32x4*)(scr + i * 1024 + l16) = rg[i];
#pragma unroll
        for (int u = 0; u < 4; ++u) { lptr a = scr + (8 * g4 + tq) * 128 + u * 32 + tp * 8;
          const u32x4 f = asu4(cat8(trread(a), trread(a + 512)));
          bst128<AUX_SC1>(ws, W_WGU + (((cgf * 4 + u) * 20 + (r >> 1)) * 2 + (r & 1)) * 1024 + l16, f);
          float cs = ((bflo(f[0]) + bfhi(f[0])) + (bflo(f[1]) + bfhi(f[1]))) + ((bflo(f[2]) + bfhi(f[2])) + (bflo(f[3]) + bfhi(f[3])));
          cs = swap32_sum(swap16_sum(cs));
          if (lane < 16) bst32<AUX_SC1>(ws, W_CSP + (((cgf * 4 + u) * 2 + (r & 1)) * 20 + (r >> 1)) * 64 + lane * 4, __float_as_uint(cs)); }
      };
      {
        kargs_t pa = kargs();
#pragma unroll
        for (int e = 0; e < 2; ++e) { const int gj = ga + 1024 * e;
          if (e == 0 || gj < 1080) { const int cgf = gj / 40, r = gj % 40; if (lane < 32) touch((32 * (r >> 1) + lane) * (FF * 2) + cgf * 128, (r & 1) ? pa->wu : pa->wg, lds0 + L2_DUMP); } }
        const int nl = ga < 986 ? 24 : (ga == 986 ? 16 : 0);
        if (lane < nl) touch(lane * 128, ga < 720 ? pa->wd + (size_t)ga * 3072 : pa->wo + (size_t)(ga - 720) * 3072, lds0 + L2_DUMP);
      }
      PIN(sc);
#pragma nounroll
      for (int m = 0; m < 6; ++m) {
        if (m == 1) {
          if (lane == 0) lds_add1(lds + L2_CP);
          u32x4 rd[3];
          gu_load(ga);
          {
            kargs_t pa = kargs();
#pragma unroll
            for (int i = 0; i < 3; ++i) { const int j = ga * 3 + i;
              const char* src = j < 2160 ? pa->wd + (size_t)j * 1024 : pa->wo + (size_t)(j < 2960 ? j - 2160 : 0) * 1024;
              rd[i] = *(const GAS u32x4*)(src + l16); }
          }
          gu_store(ga);
#pragma unroll
          for (int i = 0; i < 3; ++i) { const int j = ga * 3 + i;
            if (j < 2960) { const bool dn = j < 2160; const int a5 = (dn ? j : j - 2160) * 4 + (lane >> 4), f = (a5 * 52429) >> 18, c = (a5 - 5 * f) * 16 + (lane & 15), cg = (c * 6554) >> 16, cc = c - cg * 10;
              bst128<AUX_SC1>(ws, dn ? W_WD + (cg * FF + ((f & ~12) | ((f & 4) << 1) | ((f & 8) >> 1))) * 160 + cc * 16 : W_WO + (cg * DM + f) * 160 + cc * 16, rd[i]); } }
          if (ga < 56) { gu_load(ga + 1024); gu_store(ga + 1024); }
          WAITL(); if (lane == 0) lds_add1(lds + L2_CR);
          lds_wait_ge(lds + L2_CS, 4u);
        }
        if (m == 2) {
          WAITV(0);
          unsigned old = 0; if (lane == 0) old = __hip_atomic_fetch_add((LAS unsigned*)(lds + L2_CD), 1u, __ATOMIC_RELAXED, __HIP_MEMORY_SCOPE_WORKGROUP);
          if (lane == 0 && old == 3u) __hip_atomic_store((GAS unsigned*)(kargs()->ws + W_FRT) + b, epoch, RLX_AGENT);
        }
        if (m == 4) {
          char* wsp = kargs()->ws;
          wait_flags<4>(wsp, W_FRT, epoch, lane);
          if (lane == 0) lds_add1(lds + L2_CF);
          if (b == 0 && w == 0 && lane == 0) __hip_atomic_store((GAS unsigned*)(wsp + W_EPOCH), epoch, RLX_AGENT);
          const int ngl = rk & 3, u0 = 27 * (xcd & 3) + 7 * ngl, part = (rk >> 2) * 4 + hh;
          const int nl = (ngl == 3 ? 6 : 7) * 10;
          touch_sc1((part * nl + lane) * 128, wsp + W_WGU + (size_t)u0 * 40960, lds0 + L2_DUMP);
          if (lane < nl - 64) touch_sc1((part * nl + 64 + lane) * 128, wsp + W_WGU + (size_t)u0 * 40960, lds0 + L2_DUMP);
          if (lane < 50) touch_sc1((((rk >> 3) * 4 + hh) * 50 + lane) * 128, wsp + W_WO + hg * DM * 160, lds0 + L2_DUMP);
          lds_wait_ge(lds + L2_CS + 16, 4u);
        }
        BODY8();
      }
    }
    lds_wait_ge(lds + L2_CS + 32, 4u);
    TA(0); TB(1);
    if (lane == 0) lds_add1(lds + L2_CQ);
    TA(2); TB(3); TA(4); TB(5); TA(6); TB(7); ADV();
    lds_wait_ge(lds + L2_CQ, 8u); lds_wait_ge(lds + L2_CF, 1u);
    {
      const char* src = kargs()->ws + W_WO + hg * DM * 160 + w * 1024; unsigned dst = lds0 + L3_WO + w * 1024, vo = l16;
#pragma unroll 1
      for (int i = 0; i < 12; ++i) { glds_sc1(vo, src, dst); vo += 8192; dst += 8192; }
      if (w < 4) glds_sc1(vo, src, dst);
    }
    lds_wait_ge(lds + L2_CS + 48, 4u);
    {
      lptr zK1 = lds + oK1; lptr zK2 = lds + oK2; lptr zV = lds + oV;
      TA(0); TB(1);
      TILE(sc, sn, k1, k2, v0, v1, k1b, k2b, v0b, v1b, zK1, zK2, aV + 3 * KV_TILE, aV + 2 * KV_TILE + 2 * KV_VS);
      TILE(sn, sc, k1b, k2b, v0b, v1b, k1, k2, v0, v1, zK1 + KV_TILE, zK2 + KV_TILE, zV, aV + 3 * KV_TILE + 2 * KV_VS);
      aK1 = zK1; aK2 = zK2; aV = zV;
      TA(0); TB(1); TA(2); TB(3);
    }
    WAITL();
    o = MFMA16(asbf8(v1), asbf8(p1), o);
    __builtin_amdgcn_s_setprio(0);
  }
  STAMPX(10);
  float lsum = __uint_as_float(__builtin_amdgcn_permlane32_swap(__float_as_uint(o[8]), __float_as_uint(o[8]), false, false)[1]);
  if (__builtin_expect(__any(!(lsum >= 5.421010862427522e-20f)), 0)) {
    o = attn_exact(ws, q1, q2, lane);
    lsum = __uint_as_float(__builtin_amdgcn_permlane32_swap(__float_as_uint(o[8]), __float_as_uint(o[8]), false, false)[1]);
  }
  {
    const float inv = __builtin_amdgcn_rcpf(lsum);
    const u32x4 oa = {pk2(o[0] * inv, o[1] * inv), pk2(o[2] * inv, o[3] * inv), pk2(o[4] * inv, o[5] * inv), pk2(o[6] * inv, o[7] * inv)};
    const u32x2 ob = {pk2(o[8] * inv, o[9] * inv), pk2(o[10] * inv, o[11] * inv)};
    const unsigned tile16 = tg * 4 + th * 2 + (r32 >> 4), g0 = 10 * hg + 5 * (hh >> 1);
    bst128<AUX_SC1>(ws, W_OF + ((tile16 * 80 + g0 + 2 * (hh & 1) + h) * 16 + (r32 & 15)) * 16, oa);
    if (h == 0) bst64<AUX_SC1>(ws, W_OF + ((tile16 * 80 + g0 + 4) * 16 + (r32 & 15)) * 16 + 8 * (hh & 1), ob);
  }
  WAITV(0);
  LBAR();
  if (tid == 0) (void)__hip_atomic_fetch_add((GAS unsigned*)(kargs()->ws + W_CNTO + tg * 256), 1u, RLX_AGENT);
}

DI void phase_oproj(lptr lds, rsrc_t ws, int b, u32x2 (&hres)[5], unsigned epoch) {
  kargs_t pa = kargs();
  const int tid = threadIdx.x, lane = tid & 63, w = rfl(tid >> 6);
  const int xcd = b & 7, rk = b >> 3, tg = 4 * xcd + (rk >> 3), cg = rk & 7;
  const int mt = w & 3, kh = w >> 2, t16 = lane & 15, g = lane >> 4, tq = t16 >> 2, tp = t16 & 3;
  const int tok = tg * 64 + mt * 16 + t16;
  u32x2 xr[5] = {};
  if (kh == 0) {
#pragma unroll
    for (int nt = 0; nt < 5; ++nt) xr[nt] = *(const GAS u32x2*)(pa->x + (size_t)tok * 1280 + cg * 160 + nt * 32 + g * 8);
  }
  unsigned r1[10], r2[10];
#pragma unroll
  for (int i = 0; i < 10; ++i) {
    const int t = 4 * i + g, a5 = (t * 13) >> 6, m = t - 5 * a5, pair = 8 * kh + a5;
    const int last = m >> 2, ra = pair * 40 + (m >> 1) * 20 + (m & 1) * 4 - last * 24;
    r1[i] = L3_WO + (ra + tq) * 160 + tp * 8; r2[i] = r1[i] + (8 + 12 * last) * 160;
  }
  wait_cnt2(pa->ws, W_CNTO + tg * 256, W_CNTO + tg * 256, 8u, epoch, 0);
  STAMPX(13);
  bf16x8 bf[2][5];
  u32x4 a[10];
#pragma unroll
  for (int i = 0; i < 10; ++i) a[i] = bld128<AUX_SC1>(ws, W_OF + ((tg * 4 + mt) * 80 + 4 * (10 * kh + i)) * 256 + lane * 16);
#pragma unroll
  for (int nt = 0; nt < 5; ++nt) bf[0][nt] = cat8(trread(lds + r1[0] + nt * 32), trread(lds + r2[0] + nt * 32));
  f32x4 acc[5] = {};
#pragma unroll
  for (int i = 0; i < 10; ++i) {
    if (i + 1 < 10) {
#pragma unroll
      for (int nt = 0; nt < 5; ++nt) bf[(i + 1) & 1][nt] = cat8(trread(lds + r1[i + 1] + nt * 32), trread(lds + r2[i + 1] + nt * 32));
    }
    SBAR();
#pragma unroll
    for (int nt = 0; nt < 5; ++nt) acc[nt] = MFMAS(bf[i & 1][nt], asbf8(a[i]), acc[nt]);
    SBAR();
  }
  STAMPX(14);
  LAS f32x4* red = (LAS f32x4*)(lds + L3_RED) + mt * 320 + lane;
  if (kh == 1) {
#pragma unroll
    for (int nt = 0; nt < 5; ++nt) red[nt * 64] = acc[nt];
  }
  LBAR();
  if (kh == 0) {
    float s1 = 0.f, s2 = 0.f;
#pragma unroll
    for (int nt = 0; nt < 5; ++nt) {
      const f32x4 o = red[nt * 64];
      const float h0 = bflo(xr[nt][0]) + acc[nt][0] + o[0], h1 = bfhi(xr[nt][0]) + acc[nt][1] + o[1], h2 = bflo(xr[nt][1]) + acc[nt][2] + o[2], h3 = bfhi(xr[nt][1]) + acc[nt][3] + o[3];
      const u32x2 hv = {pk2(h0, h1), pk2(h2, h3)};
      hres[nt] = hv;
      s1 = dot2(hv[0], ONES2, s1); s1 = dot2(hv[1], ONES2, s1); s2 = dot2(hv[0], hv[0], s2); s2 = dot2(hv[1], hv[1], s2);
      const int gam = 10 * cg + 2 * nt + (g >> 1);
      bst64<AUX_SC1>(ws, W_HF + (((tg * 4 + mt) * 20 + (gam >> 2)) * 64 + (gam & 3) * 16 + t16) * 16 + (g & 1) * 8, hv);
    }
    s1 = swap32_sum(swap16_sum(s1)); s2 = swap32_sum(swap16_sum(s2));
    if (g == 0) bst64<AUX_SC1>(ws, W_HST + ((tg * 8 + cg) * 64 + mt * 16 + t16) * 8, u32x2{__float_as_uint(s1), __float_as_uint(s2)});
  }
  signal_cnt(kargs()->ws, W_CNTH + tg * 256);
}

constexpr unsigned L4_S = 0  , L4_ST = 122880  , L4_CO = 163568;
DI constexpr unsigned l5_slot(int c) { return (c & 3) == 0 ? 123904u : ((c & 3) == 1 ? 40960u : ((c & 3) == 2 ? 71680u : 0u)); }
constexpr unsigned L5_RED = 102400;
static_assert(123904 + 30720 <= L4_CO, "lds");
DI void dma_wd_chunk(const char* wd, unsigned lds0, int w, int lane, int c) {
  const unsigned l16 = lane * 16;
#pragma unroll
  for (int i = 0; i < 4; ++i) { int n = w + 8 * i; const bool dup = i == 3 && n >= 30; if (dup) n -= 8; const int k2 = n >= 15, j = n - 15 * k2;
    if (i < 3 || !dup || lane == 0) glds_sc1(l16 + j * 1024 + k2 * (864 * 160), wd + (size_t)c * (96 * 160), lds0 + l5_slot(c) + n * 1024); }
}
DI void dma_wd_chunk7(const char* wd, unsigned lds0, int w, int lane, int c) {
  const unsigned l16 = lane * 16;
#pragma unroll
  for (int i = 0; i < 5; ++i) { int n = w + 7 * i; const bool dup = i == 4 && n >= 30; if (dup) n -= 7; const int k2 = n >= 15, j = n - 15 * k2;
    if (i < 4 || !dup || lane == 0) glds_sc1(l16 + j * 1024 + k2 * (864 * 160), wd + (size_t)c * (96 * 160), lds0 + l5_slot(c) + n * 1024); }
}
constexpr int GWIN = 10;
DI void phase_gateup(lptr lds, rsrc_t ws, int b, unsigned epoch) {
  char* wsp = kargs()->ws;
  const int tid = threadIdx.x, lane = tid & 63, w = rfl(tid >> 6);
  const int xcd = b & 7, rk = b >> 3, mb = 8 * (xcd >> 2) + (rk >> 2), ngl = rk & 3, u0 = 27 * (xcd & 3) + 7 * ngl, nu = ngl == 3 ? 6 : 7;
  const int t16 = lane & 15, g = lane >> 4;
  const unsigned lds0 = (unsigned)(uintptr_t)lds, l16 = lane * 16;
  const char* wd = wsp + W_WD + (size_t)(rk & 7) * FF * 160;
  const bool act = w < nu;
  const int u = u0 + (act ? w : 0);
  const unsigned wbase = W_WGU + u * 40960 + l16;
  constexpr int GPRE = 3;
  u32x4 ag[GWIN], au[GWIN];
  float csp[2][5] = {};
  if (act) {
#pragma unroll
    for (int i = 0; i < GPRE; ++i) { ag[i] = bld128<AUX_SC1>(ws, wbase + i * 2048); au[i] = bld128<AUX_SC1>(ws, wbase + i * 2048 + 1024); }
#pragma unroll
    for (int m = 0; m < 2; ++m)
#pragma unroll
      for (int i = 0; i < 5; ++i) csp[m][i] = __uint_as_float(bld32<AUX_SC1>(ws, W_CSP + ((u * 2 + m) * 20 + 5 * g + i) * 64 + t16 * 4));
  }
  SBAR();
  wait_cnt2(wsp, W_CNTH + (2 * mb) * 256, W_CNTH + (2 * mb + 1) * 256, 8u, epoch, 7);
  STAMPX(16);
  const char* hsrc = wsp + W_HF + (size_t)(mb * 8 + w) * 20 * 1024;
  auto dma_h = [&](int sc, int slot) {
#pragma unroll
    for (int i = 0; i < 5; ++i) glds_sc1(l16 + (5 * sc + i) * 1024, hsrc, lds0 + L4_S + slot * 40960 + i * 8192 + w * 1024);
  };
  if (tid == 0) *(LAS unsigned*)(lds + L4_CO) = 0u;
  u32x2 sv[8];
  if (tid < 128) {
#pragma unroll
    for (int c = 0; c < 8; ++c) sv[c] = bld64<AUX_SC1>(ws, W_HST + (((2 * mb + (tid >> 6)) * 8 + c) * 64 + (tid & 63)) * 8);
  }
  dma_h(0, 0);
  if (act) {
#pragma unroll
    for (int i = GPRE; i < GWIN; ++i) { ag[i] = bld128<AUX_SC1>(ws, wbase + i * 2048); au[i] = bld128<AUX_SC1>(ws, wbase + i * 2048 + 1024); }
  }
  SBAR();
  dma_h(1, 1); dma_h(2, 2);
  if (tid < 128) {
    float s1 = 0.f, s2 = 0.f;
#pragma unroll
    for (int c = 0; c < 8; ++c) { s1 += __uint_as_float(sv[c][0]); s2 += __uint_as_float(sv[c][1]); }
    const float mean = s1 * (1.f / DM), rstd = rsqrtf(fmaxf(s2 * (1.f / DM) - mean * mean, 0.f) + LN_EPS);
    *(LAS f32x2*)(lds + L4_ST + tid * 8) = f32x2{mean, rstd};
  }
  if (act) WAITV(24); else WAITV(10);
  LBAR();
  STAMPX(17);
  if (act) {
    f32x4 aG[8] = {}, aU[8] = {};
    lptr hb = lds + L4_S + l16;
    u32x4 bb[2][4];
#pragma unroll
    for (int t = 0; t < 4; ++t) bb[0][t] = lds128(hb + t * 1024);
#pragma unroll
    for (int s = 0; s < 20; ++s) {
      const int slot = (s / 5) % 3, nslot = ((s + 1) / 5) % 3;
      if (s == 4) { WAITV(13); LBAR(); }
      if (s == 5) dma_wd_chunk(wd, lds0, w, lane, 0);
      if (s == 9) { WAITV(22); LBAR(); dma_h(3, 0); }
      if (s == 14) { WAITV(2); LBAR(); dma_wd_chunk(wd, lds0, w, lane, 1); }
      if (s == 15) { BAR(); dma_wd_chunk(wd, lds0, w, lane, 2); }
      const u32x4 cg_ = ag[s % GWIN], cu_ = au[s % GWIN];
      if (s + GWIN < 20) { ag[s % GWIN] = bld128<AUX_SC1>(ws, wbase + (s + GWIN) * 2048); au[s % GWIN] = bld128<AUX_SC1>(ws, wbase + (s + GWIN) * 2048 + 1024); }
#pragma unroll
      for (int t = 0; t < 4; ++t) bb[1][t] = lds128(hb + slot * 40960 + (s % 5) * 8192 + (4 + t) * 1024);
      SBAR();
#pragma unroll
      for (int t = 0; t < 4; ++t) { aG[t] = MFMAS(asbf8(cg_), asbf8(bb[0][t]), aG[t]); aU[t] = MFMAS(asbf8(cu_), asbf8(bb[0][t]), aU[t]); }
      SBAR();
      if (s + 1 < 20) {
#pragma unroll
        for (int t = 0; t < 4; ++t) bb[0][t] = lds128(hb + nslot * 40960 + ((s + 1) % 5) * 8192 + t * 1024);
      }
      SBAR();
#pragma unroll
      for (int t = 0; t < 4; ++t) { aG[4 + t] = MFMAS(asbf8(cg_), asbf8(bb[1][t]), aG[4 + t]); aU[4 + t] = MFMAS(asbf8(cu_), asbf8(bb[1][t]), aU[4 + t]); }
      SBAR();
    }
    STAMPX(18);
    const float sg = swap32_sum(swap16_sum(((csp[0][0] + csp[0][1]) + (csp[0][2] + csp[0][3])) + csp[0][4])), su = swap32_sum(swap16_sum(((csp[1][0] + csp[1][1]) + (csp[1][2] + csp[1][3])) + csp[1][4]));
    float cg4[4], cu4[4];
#pragma unroll
    for (int j = 0; j < 4; ++j) { cg4[j] = __shfl(sg, 4 * g + j); cu4[j] = __shfl(su, 4 * g + j); }
#pragma unroll
    for (int i = 0; i < 4; ++i) {
      u32x2 pk[2];
#pragma unroll
      for (int e = 0; e < 2; ++e) {
        const int t = 2 * i + e;
        const f32x2 mr = *(LAS f32x2*)(lds + L4_ST + (t * 16 + t16) * 8);
        const float r2 = mr[1] * mr[1];
        float hv[4];
#pragma unroll
        for (int j = 0; j < 4; ++j) hv[j] = (aG[t][j] - mr[0] * cg4[j]) * (aU[t][j] - mr[0] * cu4[j]) * r2;
        pk[e] = u32x2{pk2(hv[0], hv[1]), pk2(hv[2], hv[3])};
      }
      const auto s0 = __builtin_amdgcn_permlane16_swap(pk[0][0], pk[1][0], false, false), s1 = __builtin_amdgcn_permlane16_swap(pk[0][1], pk[1][1], false, false);
      bst128<AUX_SC1>(ws, W_GF + (((mb * 8 + 2 * i + (g & 1)) * 54 + (u >> 1)) * 64 + (2 * (u & 1) + (g >> 1)) * 16 + t16) * 16, u32x4{s0[0], s1[0], s0[1], s1[1]});
    }
  } else {
    WAITV(5); LBAR();
    dma_wd_chunk(wd, lds0, w, lane, 0);
    WAITV(4); LBAR(); dma_h(3, 0);
    WAITV(0); LBAR(); dma_wd_chunk(wd, lds0, w, lane, 1);
    BAR(); dma_wd_chunk(wd, lds0, w, lane, 2);
  }
  STAMPX(21);
  BAR();
  if (w < 7) dma_wd_chunk7(wd, lds0, w, lane, 3);
  if (w < 7) WAITV(5); else WAITV(0);
  unsigned old = 0; if (lane == 0) old = __hip_atomic_fetch_add((LAS unsigned*)(lds + L4_CO), 1u, __ATOMIC_RELAXED, __HIP_MEMORY_SCOPE_WORKGROUP);
  if (lane == 0 && old == 7u) (void)__hip_atomic_fetch_add((GAS unsigned*)(wsp + W_CNTG + mb * 256), 1u, RLX_AGENT);
}

DI void gld128_sc1(u32x4& d, unsigned voff, const char* sbase) { asm volatile("global_load_dwordx4 %0, %1, %2 sc1" : "=v"(d) : "v"(voff), "s"(sbase) : "memory"); }
DI void phase_down(lptr lds, rsrc_t ws, int b, const u32x2 (&hres)[5], unsigned epoch) {
  char* wsp = kargs()->ws;
  const int tid = threadIdx.x, lane = tid & 63, w = rfl(tid >> 6);
  const int xcd = b & 7, rk = b >> 3, tg = 4 * xcd + (rk >> 3), cg = rk & 7;
  const int mt = w & 3, kh = w >> 2, t16 = lane & 15, g = lane >> 4, tq = t16 >> 2, tp = t16 & 3;
  const unsigned lds0 = (unsigned)(uintptr_t)lds, l16 = lane * 16;
  const char* wd = wsp + W_WD + (size_t)cg * FF * 160;
  wait_cnt2(wsp, W_CNTG + (tg >> 1) * 256, W_CNTG + (tg >> 1) * 256, 16u, epoch, 7);
  STAMPX(20);
  const char* abase = wsp + W_GF + (size_t)((tg * 4 + mt) * 54 + 27 * kh) * 1024;
  u32x4 a[4][3];
#pragma unroll
  for (int c = 0; c < 3; ++c)
#pragma unroll
    for (int k = 0; k < 3; ++k) gld128_sc1(a[c][k], l16 + (3 * c + k) * 1024, abase);
  f32x4 acc[5] = {};
  const unsigned brow = kh * 15360 + (16 * (g >> 1) + 4 * (g & 1) + tq) * 160 + tp * 8;
#define DOWN_WAIT(N, c) asm volatile("s_waitcnt vmcnt(" #N ")\n\ts_barrier" : "+v"(a[(c) & 3][0]), "+v"(a[(c) & 3][1]), "+v"(a[(c) & 3][2]) :: "memory")
  bf16x8 bf[2][5];
  { lptr bb = lds + l5_slot(0) + brow;
#pragma unroll
    for (int nt = 0; nt < 5; ++nt) bf[0][nt] = cat8(trread(bb + nt * 32), trread(bb + nt * 32 + 1280)); }
#pragma unroll
  for (int c = 0; c < 9; ++c) {
    asm volatile("" ::: "memory"); WAITL();
    if (c <= 1) DOWN_WAIT(6, c); else if (c <= 6) DOWN_WAIT(10, c); else if (c == 7) DOWN_WAIT(3, c); else DOWN_WAIT(0, c);
    if (c >= 1 && c + 3 < 9) dma_wd_chunk(wd, lds0, w, lane, c + 3);
    if (c + 3 < 9) {
#pragma unroll
      for (int k = 0; k < 3; ++k) gld128_sc1(a[(c + 3) & 3][k], l16 + (3 * (c + 3) + k) * 1024, abase); }
#pragma unroll
    for (int k = 0; k < 3; ++k) {
      const int sn = 3 * c + k + 1;
      if (sn < 27) { lptr bb = lds + l5_slot(sn / 3) + brow + (sn % 3) * 5120;
#pragma unroll
        for (int nt = 0; nt < 5; ++nt) bf[sn & 1][nt] = cat8(trread(bb + nt * 32), trread(bb + nt * 32 + 1280)); }
      SBAR();
#pragma unroll
      for (int nt = 0; nt < 5; ++nt) acc[nt] = MFMAS(bf[(3 * c + k) & 1][nt], asbf8(a[c & 3][k]), acc[nt]);
      SBAR();
    }
  }
  STAMPX(22);
  LAS f32x4* red = (LAS f32x4*)(lds + L5_RED) + mt * 320 + lane;
  if (kh == 1) {
#pragma unroll
    for (int nt = 0; nt < 5; ++nt) red[nt * 64] = acc[nt];
  }
  LBAR();
  if (kh == 0) {
    const int tok = tg * 64 + mt * 16 + t16; char* outp = kargs()->out;
#pragma unroll
    for (int nt = 0; nt < 5; ++nt) {
      const f32x4 o = red[nt * 64];
      const u32x2 r = {pk2(bflo(hres[nt][0]) + acc[nt][0] + o[0], bfhi(hres[nt][0]) + acc[nt][1] + o[1]), pk2(bflo(hres[nt][1]) + acc[nt][2] + o[2], bfhi(hres[nt][1]) + acc[nt][3] + o[3])};
      *(GAS u32x2*)(outp + (size_t)tok * 1280 + cg * 160 + nt * 32 + g * 8) = r;
    }
  }
}

#define CODE_BYTES 46080
__global__ void __launch_bounds__(NTHR, 2) k_block(Params p) {
  extern __shared__ __attribute__((aligned(16))) char smem[];
  lptr lds = (lptr)smem;
  const rsrc_t ws = __builtin_amdgcn_make_buffer_rsrc((void*)p.ws, 0, (int)W_END, 0x00020000);
  const int b = blockIdx.x;
  {
    unsigned long long pc; asm volatile("s_getpc_b64 %0" : "=s"(pc));
    const int ln = (b >> 3) * 16 + (threadIdx.x & 15);
    if (threadIdx.x >= 448 && (threadIdx.x & 63) < 16 && ln * 128 < CODE_BYTES) touch(ln * 128, (const char*)(pc & ~127ull), (unsigned)(uintptr_t)smem + 163584);
  }
  { const unsigned long long t0_ = __builtin_amdgcn_s_memrealtime();
    kargs_t pa = kargs(); const unsigned dump_ = (unsigned)(uintptr_t)smem + 163584;
    auto sweep = [&](const char* base, unsigned bytes) { for (unsigned o = (b * 512 + threadIdx.x) * 128; o < bytes; o += 256 * 512 * 128) touch(o, base, dump_); };
    if (SWEEP & 1) sweep(pa->ws, W_END);
    if (SWEEP & 2) { sweep(pa->x, SEQ * DM * 2); sweep(pa->wq, DM * DM * 2); sweep(pa->wo, DM * DM * 2); sweep(pa->wg, DM * FF * 2); sweep(pa->wu, DM * FF * 2); sweep(pa->wd, DM * FF * 2); sweep(pa->wk, DM * 40); sweep(pa->wv, DM * 40); }
    if (SWEEP & 4) sweep(pa->out, SEQ * DM * 2);
    asm volatile("s_waitcnt vmcnt(0)" ::: "memory");
    while (__builtin_amdgcn_s_memrealtime() - t0_ < 2000ull) __builtin_amdgcn_s_sleep(8); }
  u32x4 q1; u32x2 q2; unsigned epoch; float kmax2 = 0.f;
  u32x2 hres[5];
  STAMPX(0);
  phase1(lds, ws, b, q1, q2, epoch, kmax2);
  STAMPX(5);
  phase_attn(lds, ws, b, q1, q2, epoch, kmax2);
  STAMPX(12);
  phase_oproj(lds, ws, b, hres, epoch);
  STAMPX(15);
  phase_gateup(lds, ws, b, epoch);
  STAMPX(19);
  phase_down(lds, ws, b, hres, epoch);
  STAMPX(23);
}

extern "C" void kernel_launch(void* const* d_in, const int* in_sizes, int n_in, void* d_out, int out_size, void* d_ws, size_t ws_size, hipStream_t stream) {
  Params p;
  p.x = (const char*)d_in[0]; p.wq = (const char*)d_in[1]; p.wk = (const char*)d_in[2]; p.wv = (const char*)d_in[3]; p.wo = (const char*)d_in[4];
  p.wg = (const char*)d_in[6]; p.wu = (const char*)d_in[7]; p.wd = (const char*)d_in[8];
  p.out = (char*)d_out; p.ws = (char*)d_ws;
  constexpr int LDS_BYTES = 163840;
  (void)hipFuncSetAttribute((const void*)k_block, hipFuncAttributeMaxDynamicSharedMemorySize, LDS_BYTES);
  k_block<<<NWG, NTHR, LDS_BYTES, stream>>>(p);
}
```

```cpp
#include <hip/hip_runtime.h>
#include <stdint.h>

#define DI __device__ __forceinline__
#define LAS __attribute__((address_space(3)))
#define GAS __attribute__((address_space(1)))
typedef short bf16x8 __attribute__((ext_vector_type(8)));
typedef short bf16x4 __attribute__((ext_vector_type(4)));
typedef __bf16 bfv2 __attribute__((ext_vector_type(2)));
typedef float f32x2 __attribute__((ext_vector_type(2)));
typedef float f32x4 __attribute__((ext_vector_type(4)));
typedef float f32x16 __attribute__((ext_vector_type(16)));
typedef unsigned u32x2 __attribute__((ext_vector_type(2)));
typedef unsigned u32x4 __attribute__((ext_vector_type(4)));
typedef LAS char* lptr;

constexpr int SEQ = 2048, DM = 640, FF = 1728;
constexpr int NWG = 256, NTHR = 512;
constexpr float LN_EPS = 1e-5f;
constexpr float QSCALE = 0.22360679774997896f * 1.4426950408889634f;
constexpr unsigned KV_TILE = 2688, KV_K2 = 1024, KV_V = 1280, KV_VS = 352;

constexpr unsigned W_EPOCH = 0, W_TMO = 256;
constexpr unsigned W_FKV = 1024  , W_FRT = 2048  ;
constexpr unsigned W_CNTO = 8192, W_CNTH = 16384, W_CNTG = 24576;
constexpr unsigned W_KV = 65536;
constexpr unsigned W_OF = W_KV + 64 * KV_TILE;
constexpr unsigned W_HF = W_OF + SEQ * DM * 2;
constexpr unsigned W_HST = W_HF + SEQ * DM * 2;
constexpr unsigned W_GF = W_HST + 32 * 8 * 64 * 8;
constexpr unsigned W_WGU = W_GF + 128 * 54 * 1024;
constexpr unsigned W_WD = W_WGU + 108 * 40 * 1024;
constexpr unsigned W_WO = W_WD + 8 * FF * 160;
constexpr unsigned W_CSP = W_WO + 8 * DM * 160;
constexpr unsigned W_END = W_CSP + 108 * 2 * 20 * 64;

#define STAMPW() do { } while (0)
#define STAMP(i) do { } while (0)
#define STW 0
#define STAMPX(i) do { STAMP(i); if (STW == (i)) STAMPW(); } while (0)

struct Params {
  const char* x; const char* wq; const char* wk; const char* wv; const char* wo;
  const char* wg; const char* wu; const char* wd;
  char* out; char* ws;
};

typedef const __attribute__((address_space(4))) Params* kargs_t;
DI kargs_t kargs() { kargs_t a = (kargs_t)__builtin_amdgcn_kernarg_segment_ptr(); asm volatile("" : "+s"(a)); return a; }
constexpr int AUX_SC1 = 16;
#define MFMA16(a, b, c) __builtin_amdgcn_mfma_f32_32x32x16_bf16((a), (b), (c), 0, 0, 0)
#define MFMA8(a, b, c) __builtin_amdgcn_mfma_f32_32x32x8bf16_1k((a), (b), (c), 0, 0, 0)
#define MFMAS(a, b, c) __builtin_amdgcn_mfma_f32_16x16x32_bf16((a), (b), (c), 0, 0, 0)
#define SBAR() __builtin_amdgcn_sched_barrier(0)
#define WAITV(n) asm volatile("s_waitcnt vmcnt(" #n ")" ::: "memory")
#define BAR() asm volatile("s_barrier" ::: "memory")
#define WAITL() __builtin_amdgcn_s_waitcnt(0xC07F)
#define LBAR() do { asm volatile("" ::: "memory"); WAITL(); BAR(); } while (0)
#define PIN(x) asm volatile("" : "+v"(x))

DI unsigned pk2(float lo, float hi) { f32x2 v = {lo, hi}; return __builtin_bit_cast(unsigned, __builtin_convertvector(v, bfv2)); }
DI float bflo(unsigned v) { return __uint_as_float(v << 16); }
DI float bfhi(unsigned v) { return __uint_as_float(v & 0xffff0000u); }
DI float dot2(unsigned a, unsigned b, float c) { return __builtin_amdgcn_fdot2_f32_bf16(__builtin_bit_cast(bfv2, a), __builtin_bit_cast(bfv2, b), c, false); }
constexpr unsigned ONES2 = 0x3f803f80u;
DI float swap32_sum(float v) { auto r = __builtin_amdgcn_permlane32_swap(__float_as_uint(v), __float_as_uint(v), false, false); return __uint_as_float(r[0]) + __uint_as_float(r[1]); }
DI float swap32_max(float v) { auto r = __builtin_amdgcn_permlane32_swap(__float_as_uint(v), __float_as_uint(v), false, false); return fmaxf(__uint_as_float(r[0]), __uint_as_float(r[1])); }
DI float swap16_sum(float v) { auto r = __builtin_amdgcn_permlane16_swap(__float_as_uint(v), __float_as_uint(v), false, false); return __uint_as_float(r[0]) + __uint_as_float(r[1]); }
template <int CTRL> DI float dppf(float v) { return __uint_as_float(__builtin_amdgcn_update_dpp(0u, __float_as_uint(v), CTRL, 0xf, 0xf, true)); }
DI float sum8(float v) { v += dppf<0xB1>(v); v += dppf<0x4E>(v); return v + dppf<0x141>(v); }
DI float max16(float v) { v = fmaxf(v, dppf<0x128>(v)); v = fmaxf(v, dppf<0x124>(v)); v = fmaxf(v, dppf<0x122>(v)); return fmaxf(v, dppf<0x121>(v)); }
DI float max64(float v) { v = max16(v); auto r = __builtin_amdgcn_permlane16_swap(__float_as_uint(v), __float_as_uint(v), false, false);
  return swap32_max(fmaxf(__uint_as_float(r[0]), __uint_as_float(r[1]))); }
typedef short v4i16_t __attribute__((ext_vector_type(4)));
DI bf16x4 trread(lptr p) { return __builtin_bit_cast(bf16x4, __builtin_amdgcn_ds_read_tr16_b64_v4i16((LAS v4i16_t*)p)); }
DI bf16x8 cat8(bf16x4 a, bf16x4 b) { return __builtin_shufflevector(a, b, 0, 1, 2, 3, 4, 5, 6, 7); }
DI u32x4 lds128(lptr p) { return *(LAS u32x4*)p; }
DI u32x2 lds64(lptr p) { return *(LAS u32x2*)p; }
DI bf16x8 asbf8(u32x4 v) { return __builtin_bit_cast(bf16x8, v); }
DI bf16x4 asbf4(u32x2 v) { return __builtin_bit_cast(bf16x4, v); }
DI u32x4 asu4(bf16x8 v) { return __builtin_bit_cast(u32x4, v); }
DI unsigned rfl(unsigned v) { return __builtin_amdgcn_readfirstlane(v); }
typedef __amdgpu_buffer_rsrc_t rsrc_t;
template <int AUX> DI u32x4 bld128(rsrc_t r, unsigned off) { return __builtin_amdgcn_raw_buffer_load_b128(r, off, 0, AUX); }
template <int AUX> DI u32x2 bld64(rsrc_t r, unsigned off) { return __builtin_amdgcn_raw_buffer_load_b64(r, off, 0, AUX); }
template <int AUX> DI unsigned bld32(rsrc_t r, unsigned off) { return __builtin_amdgcn_raw_buffer_load_b32(r, off, 0, AUX); }
template <int AUX> DI void bst128(rsrc_t r, unsigned off, u32x4 v) { __builtin_amdgcn_raw_buffer_store_b128(v, r, off, 0, AUX); }
template <int AUX> DI void bst64(rsrc_t r, unsigned off, u32x2 v) { __builtin_amdgcn_raw_buffer_store_b64(v, r, off, 0, AUX); }
template <int AUX> DI void bst32(rsrc_t r, unsigned off, unsigned v) { __builtin_amdgcn_raw_buffer_store_b32(v, r, off, 0, AUX); }
DI void glds(unsigned voff, const char* sbase, unsigned lds_base) {
  asm volatile("s_mov_b32 m0, %2\n\ts_nop 0\n\tglobal_load_lds_dwordx4 %0, %1" :: "v"(voff), "s"(sbase), "s"(rfl(lds_base)) : "memory");
}
DI void glds_sc1(unsigned voff, const char* sbase, unsigned lds_base) {
  asm volatile("s_mov_b32 m0, %2\n\ts_nop 0\n\tglobal_load_lds_dwordx4 %0, %1 sc1" :: "v"(voff), "s"(sbase), "s"(rfl(lds_base)) : "memory");
}
DI void touch(unsigned voff, const char* sbase, unsigned lds_dump) {
  asm volatile("s_mov_b32 m0, %2\n\ts_nop 0\n\tglobal_load_lds_dword %0, %1" :: "v"(voff), "s"(sbase), "s"(rfl(lds_dump)) : "memory");
}
DI void touch_sc1(unsigned voff, const char* sbase, unsigned lds_dump) {
  asm volatile("s_mov_b32 m0, %2\n\ts_nop 0\n\tglobal_load_lds_dword %0, %1 sc1" :: "v"(voff), "s"(sbase), "s"(rfl(lds_dump)) : "memory");
}

DI int hg_of(int rk) { return ((rk >> 4) << 2) | (rk & 3); }
DI int tgl_of(int rk) { return (rk >> 2) & 3; }

#define RLX_AGENT __ATOMIC_RELAXED, __HIP_MEMORY_SCOPE_AGENT
constexpr unsigned SPIN_CAP = 1u << 17;
DI bool spin_fail(unsigned& spins, char* wsp, unsigned epoch) {
  __builtin_amdgcn_s_sleep(1);
  GAS unsigned* tmo = (GAS unsigned*)(wsp + W_TMO);
  if (++spins > SPIN_CAP) { __hip_atomic_store(tmo, epoch, RLX_AGENT); return true; }
  return (spins & 255u) == 0u && rfl(__hip_atomic_load(tmo, RLX_AGENT)) == epoch;
}
template <int NW> DI void wait_flags(char* wsp, unsigned off, unsigned epoch, int lane) {
  for (unsigned spins = 0;;) {
    bool ok = true;
#pragma unroll
    for (int i = 0; i < NW; ++i) ok &= __hip_atomic_load((GAS unsigned*)(wsp + off) + i * 64 + lane, RLX_AGENT) == epoch;
    if (__all(ok)) break;
    if (spin_fail(spins, wsp, epoch)) break;
  }
  __builtin_amdgcn_fence(__ATOMIC_ACQUIRE, "wavefront");
}
DI float wait_granules(char* wsp, unsigned off, unsigned epoch, int lane) {
  unsigned mx;
  for (unsigned spins = 0;;) {
    const unsigned long long a = __hip_atomic_load((GAS unsigned long long*)(wsp + off) + lane, RLX_AGENT), b = __hip_atomic_load((GAS unsigned long long*)(wsp + off) + 64 + lane, RLX_AGENT);
    mx = max((unsigned)a, (unsigned)b);
    if (__all((unsigned)(a >> 32) == epoch && (unsigned)(b >> 32) == epoch)) break;
    if (spin_fail(spins, wsp, epoch)) break;
  }
  __builtin_amdgcn_fence(__ATOMIC_ACQUIRE, "wavefront");
  return __uint_as_float(mx);
}
DI void wait_cnt2(char* wsp, unsigned off0, unsigned off1, unsigned want, unsigned epoch, int poller) {
  if ((int)(threadIdx.x >> 6) == poller) {
    GAS unsigned* word = (GAS unsigned*)(wsp + ((threadIdx.x & 1) ? off1 : off0));
    for (unsigned spins = 0;;) {
      if (__all(__hip_atomic_load(word, RLX_AGENT) >= want)) break;
      if (spin_fail(spins, wsp, epoch)) break;
    }
    __builtin_amdgcn_fence(__ATOMIC_ACQUIRE, "wavefront");
  }
  LBAR();
}
DI void signal_cnt(char* wsp, unsigned off) {
  WAITV(0);
  LBAR();
  if (threadIdx.x == 0) (void)__hip_atomic_fetch_add((GAS unsigned*)(wsp + off), 1u, RLX_AGENT);
}
DI void lds_add1(lptr p) { (void)__hip_atomic_fetch_add((LAS unsigned*)p, 1u, __ATOMIC_RELAXED, __HIP_MEMORY_SCOPE_WORKGROUP); }
DI void lds_wait_ge(lptr p, unsigned want) {
  while ((unsigned)rfl(__hip_atomic_load((LAS unsigned*)p, __ATOMIC_RELAXED, __HIP_MEMORY_SCOPE_WORKGROUP)) < want) __builtin_amdgcn_s_sleep(2);
  asm volatile("" ::: "memory");
}

DI void dma_kv3(const char* kvg, unsigned lds0, int c, int j, int lane) {
  const unsigned dst = lds0 + (c == 15 ? 0 : c) * 4 * KV_TILE, l16 = lane * 16; const char* src = kvg + c * 4 * KV_TILE;
  glds_sc1(l16, src + j * 1024, dst + j * 1024);
  glds_sc1(l16, src + (j + 4) * 1024, dst + (j + 4) * 1024);
  const int n = j == 3 ? 7 : j + 8;
  if (j != 2 || lane < 32) glds_sc1(l16, src + n * 1024, dst + n * 1024);
}

constexpr unsigned L1_X = 0, L1_S = 81920, L1_WK = L1_S + 4 * 10240, L1_PART = L1_WK + 25600;
constexpr unsigned L1_CS = L1_PART  ;
constexpr unsigned L1_ST = 161792  , L1_KC = L1_ST + 512  , L1_KM = L1_KC + 128, L1_DUMP = L1_KM + 16;
static_assert(L1_PART + 12672 <= 161280 && L1_DUMP + 256 <= 163584, "lds");

DI void phase1(lptr lds, rsrc_t ws, int b, u32x4& q1, u32x2& q2, unsigned& epoch, float& kmax2) {
  kargs_t pa = kargs();
  const int tid = threadIdx.x, lane = tid & 63, w = rfl(tid >> 6);
  const int r32 = lane & 31, h = lane >> 5;
  const int xcd = b & 7, rk = b >> 3, tg = 4 * xcd + tgl_of(rk), hg = hg_of(rk);
  const unsigned lds0 = (unsigned)(uintptr_t)lds, l16 = lane * 16;
  unsigned ep = __hip_atomic_load((GAS unsigned*)(pa->ws + W_EPOCH), RLX_AGENT);
  {
    const char* xb = pa->x + (size_t)(tg * 64 + 8 * w) * 1280;
    const int tl = lane >> 4;
    unsigned v0 = tl * 1280 + ((lane ^ (8 * w + tl)) & 15) * 16, v1 = (4 + tl) * 1280 + ((lane ^ (8 * w + 4 + tl)) & 15) * 16, xd = lds0 + L1_X + 8 * w * 256;
#pragma unroll 1
    for (int a = 0; a < 5; ++a) { glds(v0, xb, xd); glds(v1, xb, xd + 1024); v0 += 256; v1 += 256; xd += 16384; }
  }
  if (hg < 4) {
    const char* W = (hg < 2 ? pa->wk : pa->wv) + w * 1024;
#pragma unroll
    for (int i = 0; i < 3; ++i) glds(l16 + i * 8192, W, lds0 + L1_WK + w * 1024 + i * 8192);
    if (w == 0) glds(l16 + 3 * 8192, W, lds0 + L1_WK + 3 * 8192);
  }
  const char* wqb = pa->wq + hg * 160;
  unsigned vq[3], dq[3];
#pragma unroll
  for (int i = 0; i < 3; ++i) {
    const int n = i < 2 ? w + 8 * i : (w < 6 ? w + 16 : w + 8);
    const int slot = 6 * n + lane / 10, row = (slot & ~7) | ((slot & 1) << 2) | ((slot >> 1) & 3);
    vq[i] = row * 1280 + (lane % 10) * 16; dq[i] = n * 960;
  }
  const bool m3 = w < 5 || (w == 5 ? lane < 20 : lane == 0);
  auto dma_pair = [&](const char* src, unsigned dst) {
    glds(vq[0], src, dst + dq[0]);
    glds(vq[1], src, dst + dq[1]);
    if (m3) glds(vq[2], src, dst + dq[2]);
  };
  SBAR();
  STAMPX(8);
  const int i16 = lane & 15, tq = i16 >> 2, tp = i16 & 3, grp = (lane >> 4) & 1;
  WAITV(0);
  LBAR();
  PIN(ep); epoch = rfl(ep) + 1u;
  STAMPX(1);
  const int kth = hg & 1; const bool isV = hg >= 2;
  f32x16 kacc = {}; float kcs = 0.f; unsigned kn2 = 0u;
  auto dma_first = [&]() { dma_pair(wqb, lds0 + L1_S); dma_pair(wqb + 128 * 1280, lds0 + L1_S + 20480); };
  if (w >= 4) {
    dma_first();
#pragma unroll
    for (int ps = 0; ps < 2; ++ps) {
      const int t = (ps ? 32 - 32 * kth : 32 * kth) + 8 * (w - 4) + (lane >> 3), e = lane & 7;
      u32x4 v[10];
#pragma unroll
      for (int i = 0; i < 10; ++i) v[i] = lds128(lds + L1_X + (i >> 1) * 16384 + t * 256 + ((e + 8 * (i & 1) + 8 * ((t >> 1) & 1)) & 15) * 16);
      float a4[4] = {}, b4[4] = {};
#pragma unroll
      for (int i = 0; i < 10; ++i)
#pragma unroll
        for (int j = 0; j < 4; ++j) { a4[j] = dot2(v[i][j], v[i][j], a4[j]); b4[j] = dot2(v[i][j], ONES2, b4[j]); }
      const float sq = sum8((a4[0] + a4[1]) + (a4[2] + a4[3])), sm = sum8((b4[0] + b4[1]) + (b4[2] + b4[3]));
      const float mean = sm * (1.f / DM), rstd = rsqrtf(fmaxf(sq * (1.f / DM) - mean * mean, 0.f) + LN_EPS);
      if (e == 0) { ((LAS float*)(lds + L1_ST))[t] = mean; ((LAS float*)(lds + L1_ST))[64 + t] = rstd; }
      if (ps == 0) {
        LBAR(); dma_pair(wqb + 256 * 1280, lds0 + L1_WK);
        const int ln = (tgl_of(rk) * 4 + (w - 4)) * 32 + r32;
        if (h == 0) touch((ln >> 1) * 1280 + (ln & 1) * 128, wqb + 384 * 1280 - ((hg * 160) & 127), lds0 + L1_DUMP);
      }
    }
  } else if (hg < 4) {
    lptr wb = lds + L1_WK + (160 * w + 8 * h + tq) * 40 + grp * 32 + 8 * tp;
    const int tok = kth * 32 + r32;
    u32x4 xf[10]; bf16x8 wf[10];
#pragma unroll
    for (int i = 0; i < 10; ++i) { const int c = 20 * w + 2 * i + h;
      xf[i] = lds128(lds + L1_X + (c >> 4) * 16384 + tok * 256 + ((c ^ tok) & 15) * 16); wf[i] = cat8(trread(wb + i * 640), trread(wb + i * 640 + 160)); }
    SBAR();
    if (w != 0) dma_first();
    if (isV) {
#pragma unroll
      for (int i = 0; i < 10; ++i) kacc = MFMA16(asbf8(xf[i]), wf[i], kacc);
    } else {
#pragma unroll
      for (int i = 0; i < 10; ++i) kacc = MFMA16(wf[i], asbf8(xf[i]), kacc);
    }
    float c4[4] = {};
#pragma unroll
    for (int i = 0; i < 10; ++i) { const u32x4 wu = asu4(wf[i]);
#pragma unroll
      for (int j = 0; j < 4; ++j) c4[j] = dot2(wu[j], ONES2, c4[j]); }
    kcs = swap32_sum((c4[0] + c4[1]) + (c4[2] + c4[3]));
    if (w > 0) {
      LAS f32x4* pa = (LAS f32x4*)(lds + L1_PART + (w - 1) * 4224) + lane;
#pragma unroll
      for (int g = 0; g < 4; ++g) pa[g * 64] = f32x4{kacc[4 * g], kacc[4 * g + 1], kacc[4 * g + 2], kacc[4 * g + 3]};
      if (h == 0) ((LAS float*)(lds + L1_PART + (w - 1) * 4224 + 4096))[r32] = kcs;
    }
  } else dma_first();
  if (w < 4) {
    LBAR();
    if (w != 0 || hg >= 4) dma_pair(wqb + 256 * 1280, lds0 + L1_WK);
  }
  if (hg < 4 && w == 0) {
#pragma unroll
    for (int v = 0; v < 3; ++v) {
      LAS f32x4* pa = (LAS f32x4*)(lds + L1_PART + v * 4224) + lane;
#pragma unroll
      for (int g = 0; g < 4; ++g) { const f32x4 t = pa[g * 64]; kacc[4 * g] += t[0]; kacc[4 * g + 1] += t[1]; kacc[4 * g + 2] += t[2]; kacc[4 * g + 3] += t[3]; }
      kcs += ((LAS float*)(lds + L1_PART + v * 4224 + 4096))[r32];
    }
    const int tile = tg * 2 + kth;
    const LAS float* st = (const LAS float*)(lds + L1_ST) + kth * 32;
    if (lane == 0 && hg == 0) { bst32<AUX_SC1>(ws, W_CNTO + tg * 256, 0u); bst32<AUX_SC1>(ws, W_CNTH + tg * 256, 0u); }
    if (lane == 0 && hg == 1 && !(tg & 1)) bst32<AUX_SC1>(ws, W_CNTG + (tg >> 1) * 256, 0u);
    if (!isV) {
      LAS float* tab = (LAS float*)(lds + L1_KC);
      if (h == 0) tab[r32] = kcs;
      const float mean = st[r32], rstd = st[64 + r32];
      asm volatile("" ::: "memory"); WAITL(); asm volatile("" ::: "memory");
      float kv[12];
#pragma unroll
      for (int g = 0; g < 3; ++g) { const f32x4 c4 = *(LAS f32x4*)(tab + 8 * g + 4 * h);
#pragma unroll
        for (int j = 0; j < 4; ++j) kv[4 * g + j] = (kacc[4 * g + j] - mean * c4[j]) * rstd; }
      const u32x4 k1 = {pk2(kv[0], kv[1]), pk2(kv[2], kv[3]), pk2(kv[4], kv[5]), pk2(kv[6], kv[7])};
      const u32x2 k2 = {pk2(kv[8], kv[9]), pk2(kv[10], kv[11])};
      bst128<AUX_SC1>(ws, W_KV + tile * KV_TILE + l16, k1);
      if (h == 0) bst64<AUX_SC1>(ws, W_KV + tile * KV_TILE + KV_K2 + r32 * 8, k2);
      float n2 = 0.f;
#pragma unroll
      for (int j = 0; j < 4; ++j) n2 = dot2(k1[j], k1[j], n2);
      if (h == 0) { n2 = dot2(k2[0], k2[0], n2); n2 = dot2(k2[1], k2[1], n2); }
      kn2 = __float_as_uint(max64(swap32_sum(n2)));
    } else {
      const unsigned cv = r32 == 20 ? ONES2 : 0u;
#pragma unroll
      for (int s = 0; s < 2; ++s) {
        float vv[8];
#pragma unroll
        for (int g = 0; g < 2; ++g) { const f32x4 m4 = *(const LAS f32x4*)(st + 16 * s + 8 * g + 4 * h), r4 = *(const LAS f32x4*)(st + 64 + 16 * s + 8 * g + 4 * h);
#pragma unroll
          for (int j = 0; j < 4; ++j) vv[4 * g + j] = (kacc[8 * s + 4 * g + j] - m4[j] * kcs) * r4[j]; }
        u32x4 o = {pk2(vv[0], vv[1]), pk2(vv[2], vv[3]), pk2(vv[4], vv[5]), pk2(vv[6], vv[7])};
        if (r32 >= 20) o = u32x4{cv, cv, cv, cv};
        if (r32 < 22) bst128<AUX_SC1>(ws, W_KV + tile * KV_TILE + KV_V + (2 * s + h) * KV_VS + r32 * 16, o);
      }
    }
  }
  if (hg < 4 && w == 0) {
    dma_first(); dma_pair(wqb + 256 * 1280, lds0 + L1_WK);
    WAITV(9);
    if (lane == 0) __hip_atomic_store((GAS unsigned long long*)(pa->ws + W_FKV) + tg * 4 + hg, ((unsigned long long)epoch << 32) | kn2, RLX_AGENT);
    WAITV(4);
  } else if (w >= 4) WAITV(4); else WAITV(3);
  LBAR();
  STAMPX(2);
  {
    const int hh = w & 3, th = w >> 2, tok = th * 32 + r32;
    f32x16 acc = {}; float c4[4] = {};
    const unsigned wrow = (8 * h + 2 * tq) * 160 + hh * 40 + grp * 32 + 8 * tp;
    unsigned xo[8];
#pragma unroll
    for (int i = 0; i < 8; ++i) xo[i] = L1_X + tok * 256 + (((2 * i + h) ^ tok) & 15) * 16;
    u32x4 xf[2][4]; bf16x8 wf[2][4];
    unsigned xa = 0, wa = L1_S + wrow;
#define Q_RD1(e, ks, XA, WA) do { xf[e][ks] = lds128(lds + xo[4 * (e) + (ks)] + (XA)); lptr a_ = lds + (WA) + (e) * 10240 + (ks) * 2560; wf[e][ks] = cat8(trread(a_), trread(a_ + 160)); } while (0)
#define Q_STEP(e, XA, WA) do { _Pragma("unroll") for (int ks = 0; ks < 4; ++ks) { acc = MFMA16(wf[e][ks], asbf8(xf[e][ks]), acc); Q_RD1(1 - (e), ks, XA, WA); SBAR(); } } while (0)
#define Q_CS(e) do { _Pragma("unroll") for (int ks = 0; ks < 4; ++ks) { const u32x4 wu = asu4(wf[e][ks]); _Pragma("unroll") for (int j = 0; j < 4; ++j) c4[j] = dot2(wu[j], ONES2, c4[j]); } } while (0)
#pragma unroll
    for (int ks = 0; ks < 4; ++ks) Q_RD1(0, ks, xa, wa);
#pragma unroll 1
    for (int pr = 0; pr < 5; ++pr) {
      Q_STEP(0, xa, wa);
      if (pr == 1 || pr == 2) WAITV(3); else if (pr == 3) WAITV(0);
      if (pr == 2 && w == 0) {
        const float km = max64(wait_granules(pa->ws, W_FKV, epoch, lane));
        if (lane == 0) *(LAS float*)(lds + L1_KM) = km;
      }
      LBAR();
      if (pr == 2) {
        kmax2 = *(LAS float*)(lds + L1_KM);
        if (th == 0) { dma_kv3(pa->ws + W_KV, lds0, 0, hh, lane); dma_kv3(pa->ws + W_KV, lds0, 2, hh, lane); } else dma_kv3(pa->ws + W_KV, lds0, 1, hh, lane);
      }
      const unsigned wn = pr == 1 ? L1_WK + wrow : (pr == 2 || pr == 4 ? L1_S + wrow : wa + 20480);
      if (pr < 2) dma_pair(wqb + (size_t)(pr + 3) * 128 * 1280, lds0 + L1_S + pr * 20480);
      if (th == 0) Q_CS(0); else Q_CS(1);
      SBAR();
      xa = pr < 4 ? xa + 16384 : 0;
      Q_STEP(1, xa, wn);
      wa = wn;
    }
#undef Q_RD1
#undef Q_STEP
#undef Q_CS
    const float cs = swap32_sum((c4[0] + c4[1]) + (c4[2] + c4[3]));
    if (h == 0) ((LAS float*)(lds + L1_CS))[w * 32 + r32] = cs;
    const float mean = ((LAS float*)(lds + L1_ST))[tok], rstd = ((LAS float*)(lds + L1_ST))[64 + tok] * QSCALE;
    LBAR();
    float qv[12];
#pragma unroll
    for (int g = 0; g < 3; ++g) {
      const f32x4 ca = *(LAS f32x4*)(lds + L1_CS + (hh * 32 + 8 * g + 4 * h) * 4), cb = *(LAS f32x4*)(lds + L1_CS + ((hh + 4) * 32 + 8 * g + 4 * h) * 4);
#pragma unroll
      for (int j = 0; j < 4; ++j) qv[4 * g + j] = (acc[4 * g + j] - mean * (ca[j] + cb[j])) * rstd;
    }
    q1 = u32x4{pk2(qv[0], qv[1]), pk2(qv[2], qv[3]), pk2(qv[4], qv[5]), pk2(qv[6], qv[7])};
    q2 = h ? u32x2{0u, 0u} : u32x2{pk2(qv[8], qv[9]), pk2(qv[10], qv[11])};
  }
  STAMPX(3);
}

constexpr unsigned L2_CO = 60 * KV_TILE, L2_CR = L2_CO + 16, L2_CP = L2_CO + 32, L2_CS = L2_CO + 48, L2_CD = L2_CO + 112, L2_CQ = L2_CO + 128, L2_CF = L2_CO + 144, L2_DUMP = L2_CO + 256;
constexpr unsigned L2_RT = 52 * KV_TILE;
constexpr unsigned L3_WO = 4 * KV_TILE, L3_RED = L3_WO + 102400;
#define PRIO_M 3
#define EXP2(a) __builtin_amdgcn_exp2f(a)

DI f32x16 attn_exact(rsrc_t ws, u32x4 q1, u32x2 q2, int lane) {
  const int r32 = lane & 31, h = lane >> 5, rv = r32 < 21 ? r32 : 21;
  float mx = -3.0e38f;
#pragma unroll 1
  for (int t = 0; t < 64; ++t) {
    const unsigned base = W_KV + t * KV_TILE;
    f32x16 s = MFMA8(asbf4(bld64<AUX_SC1>(ws, base + KV_K2 + r32 * 8)), asbf4(q2), f32x16{});
    s = MFMA16(asbf8(bld128<AUX_SC1>(ws, base + lane * 16)), asbf8(q1), s);
#pragma unroll
    for (int j = 0; j < 16; ++j) mx = fmaxf(mx, s[j]);
  }
  mx = swap32_max(mx);
  f32x16 o = {};
#pragma unroll 1
  for (int t = 0; t < 64; ++t) {
    const unsigned base = W_KV + t * KV_TILE;
    f32x16 s = MFMA8(asbf4(bld64<AUX_SC1>(ws, base + KV_K2 + r32 * 8)), asbf4(q2), f32x16{});
    s = MFMA16(asbf8(bld128<AUX_SC1>(ws, base + lane * 16)), asbf8(q1), s);
    u32x4 pp[2];
#pragma unroll
    for (int j = 0; j < 8; ++j) pp[j >> 2][j & 3] = pk2(EXP2(s[2 * j] - mx), EXP2(s[2 * j + 1] - mx));
#pragma unroll
    for (int e = 0; e < 2; ++e) o = MFMA16(asbf8(bld128<AUX_SC1>(ws, base + KV_V + (2 * e + h) * KV_VS + rv * 16)), asbf8(pp[e]), o);
  }
  return o;
}

DI void phase_attn(lptr lds, rsrc_t ws, int b, const u32x4 q1, const u32x2 q2, unsigned epoch, float kmax2) {
  const int tid = threadIdx.x, lane = tid & 63, w = rfl(tid >> 6);
  const int r32 = lane & 31, h = lane >> 5;
  const int xcd = b & 7, rk = b >> 3, tg = 4 * xcd + tgl_of(rk), hg = hg_of(rk);
  const int hh = w & 3, th = w >> 2;
  const unsigned lds0 = (unsigned)(uintptr_t)lds, l16 = lane * 16;
  if (tid < 10) *(LAS unsigned*)(lds + L2_CO + tid * 16) = 0u;
  f32x16 cb;
  {
    float n2 = 0.f;
#pragma unroll
    for (int j = 0; j < 4; ++j) n2 = dot2(q1[j], q1[j], n2);
    n2 = dot2(q2[0], q2[0], n2); n2 = dot2(q2[1], q2[1], n2);
    const float ref = sqrtf(swap32_sum(n2) * kmax2) * 1.001f;
#pragma unroll
    for (int j = 0; j < 16; ++j) cb[j] = -ref;
    PIN(cb);
  }
  const int rv = r32 < 21 ? r32 : 21;
  const unsigned oK1 = l16, oK2 = KV_K2 + r32 * 8, oV = KV_V + h * KV_VS + rv * 16;
  f32x16 o = {};
  LBAR();
  STAMPX(9);
  {
    lptr aK1 = lds + oK1; lptr aK2 = lds + oK2; lptr aV = lds + oV;
    u32x4 k1 = lds128(aK1); u32x2 k2 = lds64(aK2);
    f32x16 sc = MFMA8(asbf4(k2), asbf4(q2), cb); sc = MFMA16(asbf8(k1), asbf8(q1), sc);
    k1 = lds128(aK1 + KV_TILE); k2 = lds64(aK2 + KV_TILE);
    u32x4 v0 = lds128(aV), v1 = {}, p0, p1 = {};
    u32x4 k1b, v0b, v1b; u32x2 k2b;
    f32x16 sn;
#define GRP(MF, SC, i, P, j) do { MF; SBAR(); { const float e0 = EXP2(SC[i]), e1 = EXP2(SC[i + 1]), e2 = EXP2(SC[i + 2]), e3 = EXP2(SC[i + 3]); P[j] = pk2(e0, e1); P[j + 1] = pk2(e2, e3); } PIN(P); SBAR(); } while (0)
#define TILE(SC, SN, K1, K2, V0, V1, K1N, K2N, V0N, V1N, PK1, PK2, PV0, PV1) do { \
      WAITL(); \
      K2N = lds64(PK2); K1N = lds128(PK1); V0N = lds128(PV0); V1N = lds128(PV1); SBAR(); \
      GRP(SN = MFMA8(asbf4(K2), asbf4(q2), cb), SC, 0, p0, 0); \
      GRP(o = MFMA16(asbf8(V1), asbf8(p1), o), SC, 4, p0, 2); \
      GRP(SN = MFMA16(asbf8(K1), asbf8(q1), SN), SC, 8, p1, 0); \
      GRP(o = MFMA16(asbf8(V0), asbf8(p0), o), SC, 12, p1, 2); } while (0)
#define TA(T) TILE(sc, sn, k1, k2, v0, v1, k1b, k2b, v0b, v1b, aK1 + ((T) + 2) * KV_TILE, aK2 + ((T) + 2) * KV_TILE, aV + ((T) + 1) * KV_TILE, aV + (T) * KV_TILE + 2 * KV_VS)
#define TB(T) TILE(sn, sc, k1b, k2b, v0b, v1b, k1, k2, v0, v1, aK1 + ((T) + 2) * KV_TILE, aK2 + ((T) + 2) * KV_TILE, aV + ((T) + 1) * KV_TILE, aV + (T) * KV_TILE + 2 * KV_VS)
#define ADV() do { aK1 += 8 * KV_TILE; aK2 += 8 * KV_TILE; aV += 8 * KV_TILE; } while (0)
#define BODY8() do { TA(0); TB(1); TA(2); TB(3); TA(4); TB(5); TA(6); TB(7); ADV(); } while (0)
    if (th == 1) {
      const char* kvg = kargs()->ws + W_KV;
#define DMA3(c) do { dma_kv3(kvg, lds0, c, hh, lane); SBAR(); } while (0)
      __builtin_amdgcn_s_setprio(2);
      PIN(sc);
      DMA3(3); TA(0); DMA3(4); TB(1); DMA3(5); TA(2); DMA3(6); TB(3);
      DMA3(7); TA(4); DMA3(8); TB(5); DMA3(9); TA(6); DMA3(10); TB(7); ADV();
      if (lane == 0) lds_add1(lds + L2_CP);
      WAITV(6); if (lane == 0) lds_add1(lds + L2_CS);
      lds_wait_ge(lds + L2_CS, 4u);
      DMA3(11); TA(0); DMA3(12); TB(1); TA(2); TB(3); TA(4); TB(5); TA(6); TB(7); ADV();
      WAITV(0); if (lane == 0) lds_add1(lds + L2_CS + 16);
#pragma nounroll
      for (int m = 2; m < 6; ++m) {
        if (m == 4) { lds_wait_ge(lds + L2_CS + 16, 4u); lds_wait_ge(lds + L2_CR, 4u); DMA3(13); DMA3(14); }
        if (m == 5) { WAITV(0); if (lane == 0) lds_add1(lds + L2_CS + 32); lds_wait_ge(lds + L2_CP, 8u); DMA3(15); }
        if (m == PRIO_M) __builtin_amdgcn_s_setprio(0);
        BODY8();
      }
      WAITV(0); if (lane == 0) lds_add1(lds + L2_CS + 48);
#undef DMA3
    } else {
      __builtin_amdgcn_s_setprio(1);
      const int ga = b * 4 + hh;
      auto gu_dma = [&](int gj) {
        kargs_t pa = kargs(); const int cgf = gj / 40, r = gj % 40;
        const char* src = ((r & 1) ? pa->wu : pa->wg) + (size_t)(32 * (r >> 1)) * (FF * 2) + cgf * 128;
        unsigned vo = (lane >> 3) * (FF * 2) + (lane & 7) * 16, dst = lds0 + L2_RT + hh * 4096;
#pragma unroll
        for (int i = 0; i < 4; ++i) { glds(vo, src, dst); vo += 8 * FF * 2; dst += 1024; }
      };
      auto gu_store = [&](int gj) {
        const int cgf = gj / 40, r = gj % 40, i16 = lane & 15, g4 = lane >> 4;
        lptr a = lds + L2_RT + hh * 4096 + (8 * g4 + (i16 >> 2)) * 128 + (i16 & 3) * 8;
        const unsigned o1 = W_WGU + ((cgf * 80 + (r >> 1)) * 2 + (r & 1)) * 1024 + l16, o2 = W_CSP + ((cgf * 8 + (r & 1)) * 20 + (r >> 1)) * 64 + g4 * 16;
#pragma unroll
        for (int u = 0; u < 4; ++u) {
          const bf16x8 f = cat8(trread(a + u * 32), trread(a + u * 32 + 512));
          bst128<AUX_SC1>(ws, o1 + u * 40960, asu4(f));
          const f32x4 cs = MFMAS(f, asbf8(u32x4{ONES2, ONES2, ONES2, ONES2}), f32x4{});
          if (i16 == 0) bst128<AUX_SC1>(ws, o2 + u * 2560, __builtin_bit_cast(u32x4, cs));
        }
      };
      gu_dma(ga);
      {
        kargs_t pa = kargs();
        const int nl = ga < 986 ? 24 : (ga == 986 ? 16 : 0);
        if (lane < nl) touch(lane * 128, ga < 720 ? pa->wd + (size_t)ga * 3072 : pa->wo + (size_t)(ga - 720) * 3072, lds0 + L2_DUMP);
      }
      PIN(sc);
#pragma nounroll
      for (int m = 0; m < 6; ++m) {
        if (m == 1) {
          if (lane == 0) lds_add1(lds + L2_CP);
          u32x4 rd[3];
          WAITV(0);
          {
            kargs_t pa = kargs();
            const char* src = ga < 720 ? pa->wd + (size_t)ga * 3072 : pa->wo + (size_t)(ga < 987 ? ga - 720 : 0) * 3072;
#pragma unroll
            for (int i = 0; i < 3; ++i) rd[i] = *(const GAS u32x4*)(src + ((ga == 986 && i == 2) ? 0 : i * 1024) + l16);
          }
          gu_store(ga);
          if (ga < 987) {
            const bool dn = ga < 720; const int j0 = dn ? ga * 3 : (ga - 720) * 3;
#pragma unroll
            for (int i = 0; i < 3; ++i) { const int a5 = (j0 + i) * 4 + (lane >> 4), f = (a5 * 52429) >> 18, c = (a5 - 5 * f) * 16 + (lane & 15), cg = (c * 6554) >> 16, cc = c - cg * 10;
              const unsigned o = dn ? W_WD + (cg * FF + ((f & ~12) | ((f & 4) << 1) | ((f & 8) >> 1))) * 160 : W_WO + (cg * DM + f) * 160;
              if (ga < 986 || i < 2) bst128<AUX_SC1>(ws, o + cc * 16, rd[i]); }
          }
          if (ga >= 968) { WAITL(); gu_dma(ga + 56); WAITV(0); gu_store(ga + 56); }
          WAITL(); if (lane == 0) lds_add1(lds + L2_CR);
          lds_wait_ge(lds + L2_CS, 4u);
        }
        if (m == 2) {
          WAITV(0);
          unsigned old = 0; if (lane == 0) old = __hip_atomic_fetch_add((LAS unsigned*)(lds + L2_CD), 1u, __ATOMIC_RELAXED, __HIP_MEMORY_SCOPE_WORKGROUP);
          if (lane == 0 && old == 3u) __hip_atomic_store((GAS unsigned*)(kargs()->ws + W_FRT) + b, epoch, RLX_AGENT);
        }
        if (m == 4) {
          char* wsp = kargs()->ws;
          wait_flags<4>(wsp, W_FRT, epoch, lane);
          if (lane == 0) lds_add1(lds + L2_CF);
          if (b == 0 && w == 0 && lane == 0) __hip_atomic_store((GAS unsigned*)(wsp + W_EPOCH), epoch, RLX_AGENT);
          const int ngl = rk & 3, u0 = 27 * (xcd & 3) + 7 * ngl, part = (rk >> 2) * 4 + hh;
          const int nl = (ngl == 3 ? 6 : 7) * 10;
          touch_sc1((part * nl + lane) * 128, wsp + W_WGU + (size_t)u0 * 40960, lds0 + L2_DUMP);
          if (lane < nl - 64) touch_sc1((part * nl + 64 + lane) * 128, wsp + W_WGU + (size_t)u0 * 40960, lds0 + L2_DUMP);
          if (lane < 50) touch_sc1(((tgl_of(rk) * 4 + hh) * 50 + lane) * 128, wsp + W_WO + hg * DM * 160, lds0 + L2_DUMP);
          lds_wait_ge(lds + L2_CS + 16, 4u);
        }
        BODY8();
      }
    }
    lds_wait_ge(lds + L2_CS + 32, 4u);
    TA(0); TB(1);
    if (lane == 0) lds_add1(lds + L2_CQ);
    TA(2); TB(3); TA(4); TB(5); TA(6); TB(7); ADV();
    lds_wait_ge(lds + L2_CQ, 8u); lds_wait_ge(lds + L2_CF, 1u);
    const char* wsrc = kargs()->ws + W_WO + hg * DM * 160 + w * 1024; unsigned wdst = lds0 + L3_WO + w * 1024, wvo = l16;
#define WO2() do { glds_sc1(wvo, wsrc, wdst); glds_sc1(wvo + 8192, wsrc, wdst + 8192); wvo += 16384; wdst += 16384; SBAR(); } while (0)
    lds_wait_ge(lds + L2_CS + 48, 4u);
    {
      lptr zK1 = lds + oK1; lptr zK2 = lds + oK2; lptr zV = lds + oV;
      WO2(); TA(0); WO2(); TB(1);
      WO2(); TILE(sc, sn, k1, k2, v0, v1, k1b, k2b, v0b, v1b, zK1, zK2, aV + 3 * KV_TILE, aV + 2 * KV_TILE + 2 * KV_VS);
      WO2(); TILE(sn, sc, k1b, k2b, v0b, v1b, k1, k2, v0, v1, zK1 + KV_TILE, zK2 + KV_TILE, zV, aV + 3 * KV_TILE + 2 * KV_VS);
      aK1 = zK1; aK2 = zK2; aV = zV;
      WO2(); TA(0); WO2(); TB(1);
      if (w < 4) glds_sc1(wvo, wsrc, wdst);
      TA(2); TB(3);
    }
#undef WO2
    WAITL();
    o = MFMA16(asbf8(v1), asbf8(p1), o);
    __builtin_amdgcn_s_setprio(0);
  }
  STAMPX(10);
  float lsum = __uint_as_float(__builtin_amdgcn_permlane32_swap(__float_as_uint(o[8]), __float_as_uint(o[8]), false, false)[1]);
  if (__builtin_expect(__any(!(lsum >= 5.421010862427522e-20f)), 0)) {
    o = attn_exact(ws, q1, q2, lane);
    lsum = __uint_as_float(__builtin_amdgcn_permlane32_swap(__float_as_uint(o[8]), __float_as_uint(o[8]), false, false)[1]);
  }
  {
    const float inv = __builtin_amdgcn_rcpf(lsum);
    const u32x4 oa = {pk2(o[0] * inv, o[1] * inv), pk2(o[2] * inv, o[3] * inv), pk2(o[4] * inv, o[5] * inv), pk2(o[6] * inv, o[7] * inv)};
    const u32x2 ob = {pk2(o[8] * inv, o[9] * inv), pk2(o[10] * inv, o[11] * inv)};
    const unsigned tile16 = tg * 4 + th * 2 + (r32 >> 4), g0 = 10 * hg + 5 * (hh >> 1);
    bst128<AUX_SC1>(ws, W_OF + ((tile16 * 80 + g0 + 2 * (hh & 1) + h) * 16 + (r32 & 15)) * 16, oa);
    if (h == 0) bst64<AUX_SC1>(ws, W_OF + ((tile16 * 80 + g0 + 4) * 16 + (r32 & 15)) * 16 + 8 * (hh & 1), ob);
  }
  WAITV(0);
  LBAR();
  if (tid == 0) (void)__hip_atomic_fetch_add((GAS unsigned*)(kargs()->ws + W_CNTO + tg * 256), 1u, RLX_AGENT);
}

DI void phase_oproj(lptr lds, rsrc_t ws, int b, u32x2 (&hres)[5], unsigned epoch) {
  kargs_t pa = kargs();
  const int tid = threadIdx.x, lane = tid & 63, w = rfl(tid >> 6);
  const int xcd = b & 7, rk = b >> 3, tg = 4 * xcd + tgl_of(rk), cg = hg_of(rk);
  const int mt = w & 3, kh = w >> 2, t16 = lane & 15, g = lane >> 4, tq = t16 >> 2, tp = t16 & 3;
  const int tok = tg * 64 + mt * 16 + t16;
  u32x2 xr[5] = {};
  if (kh == 0) {
#pragma unroll
    for (int nt = 0; nt < 5; ++nt) xr[nt] = *(const GAS u32x2*)(pa->x + (size_t)tok * 1280 + cg * 160 + nt * 32 + g * 8);
  }
  unsigned r1[10], r2[10];
#pragma unroll
  for (int i = 0; i < 10; ++i) {
    const int t = 4 * i + g, a5 = (t * 13) >> 6, m = t - 5 * a5, pair = 8 * kh + a5;
    const int last = m >> 2, ra = pair * 40 + (m >> 1) * 20 + (m & 1) * 4 - last * 24;
    r1[i] = L3_WO + (ra + tq) * 160 + tp * 8; r2[i] = r1[i] + (8 + 12 * last) * 160;
  }
  wait_cnt2(pa->ws, W_CNTO + tg * 256, W_CNTO + tg * 256, 8u, epoch, 0);
  STAMPX(13);
  bf16x8 bf[2][5];
  u32x4 a[10];
#pragma unroll
  for (int i = 0; i < 10; ++i) a[i] = bld128<AUX_SC1>(ws, W_OF + ((tg * 4 + mt) * 80 + 4 * (10 * kh + i)) * 256 + lane * 16);
#pragma unroll
  for (int nt = 0; nt < 5; ++nt) bf[0][nt] = cat8(trread(lds + r1[0] + nt * 32), trread(lds + r2[0] + nt * 32));
  f32x4 acc[5] = {};
#pragma unroll
  for (int i = 0; i < 10; ++i) {
    if (i + 1 < 10) {
#pragma unroll
      for (int nt = 0; nt < 5; ++nt) bf[(i + 1) & 1][nt] = cat8(trread(lds + r1[i + 1] + nt * 32), trread(lds + r2[i + 1] + nt * 32));
    }
    SBAR();
#pragma unroll
    for (int nt = 0; nt < 5; ++nt) acc[nt] = MFMAS(bf[i & 1][nt], asbf8(a[i]), acc[nt]);
    SBAR();
  }
  STAMPX(14);
  LAS f32x4* red = (LAS f32x4*)(lds + L3_RED) + mt * 320 + lane;
  if (kh == 1) {
#pragma unroll
    for (int nt = 0; nt < 5; ++nt) red[nt * 64] = acc[nt];
  }
  LBAR();
  if (kh == 0) {
    float s1 = 0.f, s2 = 0.f;
#pragma unroll
    for (int nt = 0; nt < 5; ++nt) {
      const f32x4 o = red[nt * 64];
      const float h0 = bflo(xr[nt][0]) + acc[nt][0] + o[0], h1 = bfhi(xr[nt][0]) + acc[nt][1] + o[1], h2 = bflo(xr[nt][1]) + acc[nt][2] + o[2], h3 = bfhi(xr[nt][1]) + acc[nt][3] + o[3];
      const u32x2 hv = {pk2(h0, h1), pk2(h2, h3)};
      hres[nt] = hv;
      s1 = dot2(hv[0], ONES2, s1); s1 = dot2(hv[1], ONES2, s1); s2 = dot2(hv[0], hv[0], s2); s2 = dot2(hv[1], hv[1], s2);
      const int gam = 10 * cg + 2 * nt + (g >> 1);
      bst64<AUX_SC1>(ws, W_HF + (((tg * 4 + mt) * 20 + (gam >> 2)) * 64 + (gam & 3) * 16 + t16) * 16 + (g & 1) * 8, hv);
    }
    s1 = swap32_sum(swap16_sum(s1)); s2 = swap32_sum(swap16_sum(s2));
    if (g == 0) bst64<AUX_SC1>(ws, W_HST + ((tg * 8 + cg) * 64 + mt * 16 + t16) * 8, u32x2{__float_as_uint(s1), __float_as_uint(s2)});
  }
  signal_cnt(kargs()->ws, W_CNTH + tg * 256);
}

constexpr unsigned L4_S = 0  , L4_ST = 122880  , L4_CO = 163568;
DI constexpr unsigned l5_slot(int c) { return (c & 3) == 0 ? 123904u : ((c & 3) == 1 ? 40960u : ((c & 3) == 2 ? 71680u : 0u)); }
constexpr unsigned L5_RED = 102400;
static_assert(123904 + 30720 <= L4_CO, "lds");
DI void dma_wd_chunk(const char* wd, unsigned lds0, int w, int lane, int c) {
  const unsigned l16 = lane * 16;
#pragma unroll
  for (int i = 0; i < 4; ++i) { int n = w + 8 * i; const bool dup = i == 3 && n >= 30; if (dup) n -= 8; const int k2 = n >= 15, j = n - 15 * k2;
    if (i < 3 || !dup || lane == 0) glds_sc1(l16 + j * 1024 + k2 * (864 * 160), wd + (size_t)c * (96 * 160), lds0 + l5_slot(c) + n * 1024); }
}
DI void dma_wd_chunk7(const char* wd, unsigned lds0, int w, int lane, int c) {
  const unsigned l16 = lane * 16;
#pragma unroll
  for (int i = 0; i < 5; ++i) { int n = w + 7 * i; const bool dup = i == 4 && n >= 30; if (dup) n -= 7; const int k2 = n >= 15, j = n - 15 * k2;
    if (i < 4 || !dup || lane == 0) glds_sc1(l16 + j * 1024 + k2 * (864 * 160), wd + (size_t)c * (96 * 160), lds0 + l5_slot(c) + n * 1024); }
}
DI void gld128_sc1(u32x4& d, unsigned voff, const char* sbase) { asm volatile("global_load_dwordx4 %0, %1, %2 sc1" : "=v"(d) : "v"(voff), "s"(sbase) : "memory"); }
DI void waitv_tie(int n, u32x4& a, u32x4& b) { asm volatile("s_waitcnt vmcnt(%2)" : "+v"(a), "+v"(b) : "i"(n) : "memory"); }
constexpr int gu_behind(int s) { return s < 5 ? 63 : (s <= 7 ? 13 : (s == 8 ? 8 : (s <= 13 ? 13 : (s == 14 ? 8 : (s == 15 ? 12 : (s == 16 ? 14 : (s == 17 ? 16 : (s == 18 ? 14 : 12)))))))); }
constexpr int GWIN = 5;
DI void phase_gateup(lptr lds, rsrc_t ws, int b, unsigned epoch) {
  char* wsp = kargs()->ws;
  const int tid = threadIdx.x, lane = tid & 63, w = rfl(tid >> 6);
  const int xcd = b & 7, rk = b >> 3, mb = 8 * (xcd >> 2) + (rk >> 2), ngl = rk & 3, u0 = 27 * (xcd & 3) + 7 * ngl, nu = ngl == 3 ? 6 : 7;
  const int t16 = lane & 15, g = lane >> 4;
  const unsigned lds0 = (unsigned)(uintptr_t)lds, l16 = lane * 16;
  const char* wd = wsp + W_WD + (size_t)hg_of(rk) * FF * 160;
  const bool act = w < nu;
  const int u = u0 + (act ? w : 0);
  const char* wsrc = wsp + W_WGU + (size_t)u * 40960;
  u32x4 ag[GWIN], au[GWIN];
  float csp[2][5] = {};
  if (act) {
#pragma unroll
    for (int i = 0; i < GWIN; ++i) { ag[i] = bld128<AUX_SC1>(ws, W_WGU + u * 40960 + l16 + i * 2048); au[i] = bld128<AUX_SC1>(ws, W_WGU + u * 40960 + l16 + i * 2048 + 1024); }
#pragma unroll
    for (int m = 0; m < 2; ++m)
#pragma unroll
      for (int i = 0; i < 5; ++i) csp[m][i] = __uint_as_float(bld32<AUX_SC1>(ws, W_CSP + ((u * 2 + m) * 20 + 5 * g + i) * 64 + t16 * 4));
  }
  SBAR();
  wait_cnt2(wsp, W_CNTH + (2 * mb) * 256, W_CNTH + (2 * mb + 1) * 256, 8u, epoch, 7);
  STAMPX(16);
  const char* hsrc = wsp + W_HF + (size_t)(mb * 8 + w) * 20 * 1024;
  auto dma_h = [&](int sc, int slot) {
#pragma unroll
    for (int i = 0; i < 5; ++i) glds_sc1(l16 + (5 * sc + i) * 1024, hsrc, lds0 + L4_S + slot * 40960 + i * 8192 + w * 1024);
  };
  if (tid == 0) *(LAS unsigned*)(lds + L4_CO) = 0u;
  u32x2 sv[8];
  if (tid < 128) {
#pragma unroll
    for (int c = 0; c < 8; ++c) sv[c] = bld64<AUX_SC1>(ws, W_HST + (((2 * mb + (tid >> 6)) * 8 + c) * 64 + (tid & 63)) * 8);
  }
  dma_h(0, 0);
  SBAR();
  if (tid < 128) {
    float s1 = 0.f, s2 = 0.f;
#pragma unroll
    for (int c = 0; c < 8; ++c) { s1 += __uint_as_float(sv[c][0]); s2 += __uint_as_float(sv[c][1]); }
    const float mean = s1 * (1.f / DM), rstd = rsqrtf(fmaxf(s2 * (1.f / DM) - mean * mean, 0.f) + LN_EPS);
    *(LAS f32x2*)(lds + L4_ST + tid * 8) = f32x2{mean, rstd};
  }
  asm volatile("" ::: "memory"); __builtin_amdgcn_s_waitcnt(0x0F70);
  LBAR();
  STAMPX(17);
  if (act) {
    f32x4 aG[8] = {}, aU[8] = {};
    lptr hb = lds + L4_S + l16;
    u32x4 bb[2][4];
#pragma unroll
    for (int t = 0; t < 4; ++t) bb[0][t] = lds128(hb + t * 1024);
#pragma unroll
    for (int s = 0; s < 20; ++s) {
      const int slot = (s / 5) % 3, nslot = ((s + 1) / 5) % 3;
      if (s == 0) dma_h(1, 1);
      if (s == 3) dma_h(2, 2);
      if (s == 4) { WAITV(13); LBAR(); }
      if (s == 9) { WAITV(12); LBAR(); dma_h(3, 0); }
      if (s == 14) { WAITV(10); LBAR(); }
      if (s == 15) dma_wd_chunk(wd, lds0, w, lane, 0);
      if (s == 16) { BAR(); dma_wd_chunk(wd, lds0, w, lane, 1); }
      if (s == 17) dma_wd_chunk(wd, lds0, w, lane, 2);
      if (s >= GWIN) waitv_tie(gu_behind(s), ag[s % GWIN], au[s % GWIN]);
#pragma unroll
      for (int t = 0; t < 4; ++t) bb[1][t] = lds128(hb + slot * 40960 + (s % 5) * 8192 + (4 + t) * 1024);
      SBAR();
#pragma unroll
      for (int t = 0; t < 4; ++t) { aG[t] = MFMAS(asbf8(ag[s % GWIN]), asbf8(bb[0][t]), aG[t]); aU[t] = MFMAS(asbf8(au[s % GWIN]), asbf8(bb[0][t]), aU[t]); }
      SBAR();
      if (s + 1 < 20) {
#pragma unroll
        for (int t = 0; t < 4; ++t) bb[0][t] = lds128(hb + nslot * 40960 + ((s + 1) % 5) * 8192 + t * 1024);
      }
      SBAR();
#pragma unroll
      for (int t = 0; t < 4; ++t) { aG[4 + t] = MFMAS(asbf8(ag[s % GWIN]), asbf8(bb[1][t]), aG[4 + t]); aU[4 + t] = MFMAS(asbf8(au[s % GWIN]), asbf8(bb[1][t]), aU[4 + t]); }
      SBAR();
      if (s + GWIN < 20) { gld128_sc1(ag[s % GWIN], l16 + (s + GWIN) * 2048, wsrc); gld128_sc1(au[s % GWIN], l16 + (s + GWIN) * 2048 + 1024, wsrc); }
      SBAR();
    }
    STAMPX(18);
    const float sg = swap32_sum(swap16_sum(((csp[0][0] + csp[0][1]) + (csp[0][2] + csp[0][3])) + csp[0][4])), su = swap32_sum(swap16_sum(((csp[1][0] + csp[1][1]) + (csp[1][2] + csp[1][3])) + csp[1][4]));
    float cg4[4], cu4[4];
#pragma unroll
    for (int j = 0; j < 4; ++j) { cg4[j] = __shfl(sg, 4 * g + j); cu4[j] = __shfl(su, 4 * g + j); }
#pragma unroll
    for (int i = 0; i < 4; ++i) {
      u32x2 pk[2];
#pragma unroll
      for (int e = 0; e < 2; ++e) {
        const int t = 2 * i + e;
        const f32x2 mr = *(LAS f32x2*)(lds + L4_ST + (t * 16 + t16) * 8);
        const float r2 = mr[1] * mr[1];
        float hv[4];
#pragma unroll
        for (int j = 0; j < 4; ++j) hv[j] = (aG[t][j] - mr[0] * cg4[j]) * (aU[t][j] - mr[0] * cu4[j]) * r2;
        pk[e] = u32x2{pk2(hv[0], hv[1]), pk2(hv[2], hv[3])};
      }
      const auto s0 = __builtin_amdgcn_permlane16_swap(pk[0][0], pk[1][0], false, false), s1 = __builtin_amdgcn_permlane16_swap(pk[0][1], pk[1][1], false, false);
      bst128<AUX_SC1>(ws, W_GF + (((mb * 8 + 2 * i + (g & 1)) * 54 + (u >> 1)) * 64 + (2 * (u & 1) + (g >> 1)) * 16 + t16) * 16, u32x4{s0[0], s1[0], s0[1], s1[1]});
    }
  } else {
    dma_h(1, 1); dma_h(2, 2);
    WAITV(5); LBAR();
    WAITV(0); LBAR(); dma_h(3, 0);
    WAITV(0); LBAR(); dma_wd_chunk(wd, lds0, w, lane, 0);
    BAR(); dma_wd_chunk(wd, lds0, w, lane, 1); dma_wd_chunk(wd, lds0, w, lane, 2);
  }
  STAMPX(21);
  BAR();
  if (w < 7) dma_wd_chunk7(wd, lds0, w, lane, 3);
  if (w < 7) WAITV(5); else WAITV(0);
  unsigned old = 0; if (lane == 0) old = __hip_atomic_fetch_add((LAS unsigned*)(lds + L4_CO), 1u, __ATOMIC_RELAXED, __HIP_MEMORY_SCOPE_WORKGROUP);
  if (lane == 0 && old == 7u) (void)__hip_atomic_fetch_add((GAS unsigned*)(wsp + W_CNTG + mb * 256), 1u, RLX_AGENT);
}

DI void phase_down(lptr lds, rsrc_t ws, int b, const u32x2 (&hres)[5], unsigned epoch) {
  char* wsp = kargs()->ws;
  const int tid = threadIdx.x, lane = tid & 63, w = rfl(tid >> 6);
  const int xcd = b & 7, rk = b >> 3, tg = 4 * xcd + tgl_of(rk), cg = hg_of(rk);
  const int mt = w & 3, kh = w >> 2, t16 = lane & 15, g = lane >> 4, tq = t16 >> 2, tp = t16 & 3;
  const unsigned lds0 = (unsigned)(uintptr_t)lds, l16 = lane * 16;
  const char* wd = wsp + W_WD + (size_t)cg * FF * 160;
  wait_cnt2(wsp, W_CNTG + (tg >> 1) * 256, W_CNTG + (tg >> 1) * 256, 16u, epoch, 7);
  STAMPX(20);
  const char* abase = wsp + W_GF + (size_t)((tg * 4 + mt) * 54 + 27 * kh) * 1024;
  u32x4 a[4][3];
#pragma unroll
  for (int c = 0; c < 3; ++c)
#pragma unroll
    for (int k = 0; k < 3; ++k) gld128_sc1(a[c][k], l16 + (3 * c + k) * 1024, abase);
  f32x4 acc[5] = {};
  const unsigned brow = kh * 15360 + (16 * (g >> 1) + 4 * (g & 1) + tq) * 160 + tp * 8;
#define DOWN_WAIT(N, c) asm volatile("s_waitcnt vmcnt(" #N ")\n\ts_barrier" : "+v"(a[(c) & 3][0]), "+v"(a[(c) & 3][1]), "+v"(a[(c) & 3][2]) :: "memory")
  bf16x8 bf[2][5];
  { lptr bb = lds + l5_slot(0) + brow;
#pragma unroll
    for (int nt = 0; nt < 5; ++nt) bf[0][nt] = cat8(trread(bb + nt * 32), trread(bb + nt * 32 + 1280)); }
#pragma unroll
  for (int c = 0; c < 9; ++c) {
    asm volatile("" ::: "memory"); WAITL();
    if (c <= 1) DOWN_WAIT(6, c); else if (c <= 6) DOWN_WAIT(10, c); else if (c == 7) DOWN_WAIT(3, c); else DOWN_WAIT(0, c);
    if (c >= 1 && c + 3 < 9) dma_wd_chunk(wd, lds0, w, lane, c + 3);
    if (c + 3 < 9) {
#pragma unroll
      for (int k = 0; k < 3; ++k) gld128_sc1(a[(c + 3) & 3][k], l16 + (3 * (c + 3) + k) * 1024, abase); }
#pragma unroll
    for (int k = 0; k < 3; ++k) {
      const int sn = 3 * c + k + 1;
      if (sn < 27) { lptr bb = lds + l5_slot(sn / 3) + brow + (sn % 3) * 5120;
#pragma unroll
        for (int nt = 0; nt < 5; ++nt) bf[sn & 1][nt] = cat8(trread(bb + nt * 32), trread(bb + nt * 32 + 1280)); }
      SBAR();
#pragma unroll
      for (int nt = 0; nt < 5; ++nt) acc[nt] = MFMAS(bf[(3 * c + k) & 1][nt], asbf8(a[c & 3][k]), acc[nt]);
      SBAR();
    }
  }
  STAMPX(22);
  LAS f32x4* red = (LAS f32x4*)(lds + L5_RED) + mt * 320 + lane;
  if (kh == 1) {
#pragma unroll
    for (int nt = 0; nt < 5; ++nt) red[nt * 64] = acc[nt];
  }
  LBAR();
  if (kh == 0) {
    const int tok = tg * 64 + mt * 16 + t16; char* outp = kargs()->out;
#pragma unroll
    for (int nt = 0; nt < 5; ++nt) {
      const f32x4 o = red[nt * 64];
      const u32x2 r = {pk2(bflo(hres[nt][0]) + acc[nt][0] + o[0], bfhi(hres[nt][0]) + acc[nt][1] + o[1]), pk2(bflo(hres[nt][1]) + acc[nt][2] + o[2], bfhi(hres[nt][1]) + acc[nt][3] + o[3])};
      *(GAS u32x2*)(outp + (size_t)tok * 1280 + cg * 160 + nt * 32 + g * 8) = r;
    }
  }
}

#define CODE_BYTES 49536
__global__ void __launch_bounds__(NTHR, 2) k_block(Params p) {
  extern __shared__ __attribute__((aligned(16))) char smem[];
  lptr lds = (lptr)smem;
  const rsrc_t ws = __builtin_amdgcn_make_buffer_rsrc((void*)p.ws, 0, (int)W_END, 0x00020000);
  const int b = blockIdx.x;
  {
    unsigned long long pc; asm volatile("s_getpc_b64 %0" : "=s"(pc));
    const int ln = (b >> 3) * 16 + (threadIdx.x & 15);
    if (threadIdx.x >= 448 && (threadIdx.x & 63) < 16 && ln * 128 < CODE_BYTES) touch(ln * 128, (const char*)(pc & ~127ull), (unsigned)(uintptr_t)smem + 163584);
  }
  u32x4 q1; u32x2 q2; unsigned epoch; float kmax2 = 0.f;
  u32x2 hres[5];
  STAMPX(0);
  phase1(lds, ws, b, q1, q2, epoch, kmax2);
  STAMPX(5);
  phase_attn(lds, ws, b, q1, q2, epoch, kmax2);
  STAMPX(12);
  phase_oproj(lds, ws, b, hres, epoch);
  STAMPX(15);
  phase_gateup(lds, ws, b, epoch);
  STAMPX(19);
  phase_down(lds, ws, b, hres, epoch);
  STAMPX(23);
}

extern "C" void kernel_launch(void* const* d_in, const int* in_sizes, int n_in, void* d_out, int out_size, void* d_ws, size_t ws_size, hipStream_t stream) {
  Params p;
  p.x = (const char*)d_in[0]; p.wq = (const char*)d_in[1]; p.wk = (const char*)d_in[2]; p.wv = (const char*)d_in[3]; p.wo = (const char*)d_in[4];
  p.wg = (const char*)d_in[6]; p.wu = (const char*)d_in[7]; p.wd = (const char*)d_in[8];
  p.out = (char*)d_out; p.ws = (char*)d_ws;
  constexpr int LDS_BYTES = 163840;
  (void)hipFuncSetAttribute((const void*)k_block, hipFuncAttributeMaxDynamicSharedMemorySize, LDS_BYTES);
  k_block<<<NWG, NTHR, LDS_BYTES, stream>>>(p);
}
```

```cpp
#include <hip/hip_runtime.h>
#include <stdint.h>

#define DI __device__ __forceinline__
#define LAS __attribute__((address_space(3)))
#define GAS __attribute__((address_space(1)))
typedef short bf16x8 __attribute__((ext_vector_type(8)));
typedef short bf16x4 __attribute__((ext_vector_type(4)));
typedef __bf16 bfv2 __attribute__((ext_vector_type(2)));
typedef float f32x2 __attribute__((ext_vector_type(2)));
typedef float f32x4 __attribute__((ext_vector_type(4)));
typedef float f32x16 __attribute__((ext_vector_type(16)));
typedef unsigned u32x2 __attribute__((ext_vector_type(2)));
typedef unsigned u32x4 __attribute__((ext_vector_type(4)));
typedef LAS char* lptr;

constexpr int SEQ = 2048, DM = 640, FF = 1728;
constexpr int NWG = 256, NTHR = 512;
constexpr float LN_EPS = 1e-5f;
constexpr float QSCALE = 0.22360679774997896f * 1.4426950408889634f;
constexpr unsigned KV_TILE = 2688, KV_K2 = 1024, KV_V = 1280, KV_VS = 352;

constexpr unsigned W_EPOCH = 0, W_TMO = 256;
constexpr unsigned W_FKV = 1024  , W_FRT = 2048  ;
constexpr unsigned W_CNTO = 8192, W_CNTH = 16384, W_CNTG = 24576;
constexpr unsigned W_KV = 65536;
constexpr unsigned W_OF = W_KV + 64 * KV_TILE;
constexpr unsigned W_HF = W_OF + SEQ * DM * 2;
constexpr unsigned W_HST = W_HF + SEQ * DM * 2;
constexpr unsigned W_GF = W_HST + 32 * 8 * 64 * 8;
constexpr unsigned W_WGU = W_GF + 128 * 54 * 1024;
constexpr unsigned W_WD = W_WGU + 108 * 40 * 1024;
constexpr unsigned W_WO = W_WD + 8 * FF * 160;
constexpr unsigned W_CSP = W_WO + 8 * DM * 160;
constexpr unsigned W_END = W_CSP + 108 * 2 * 20 * 64;

#define STAMPW() do { } while (0)
#define STAMP(i) do { } while (0)
#define STW 0
#define STAMPX(i) do { STAMP(i); if (STW == (i)) STAMPW(); } while (0)

template <int V> struct IC { static constexpr int value = V; };
struct Params {
  const char* x; const char* wq; const char* wk; const char* wv; const char* wo;
  const char* wg; const char* wu; const char* wd;
  char* out; char* ws;
};

typedef const __attribute__((address_space(4))) Params* kargs_t;
DI kargs_t kargs() { kargs_t a = (kargs_t)__builtin_amdgcn_kernarg_segment_ptr(); asm volatile("" : "+s"(a)); return a; }
constexpr int AUX_SC1 = 16;
#define MFMA16(a, b, c) __builtin_amdgcn_mfma_f32_32x32x16_bf16((a), (b), (c), 0, 0, 0)
#define MFMA8(a, b, c) __builtin_amdgcn_mfma_f32_32x32x8bf16_1k((a), (b), (c), 0, 0, 0)
#define MFMAS(a, b, c) __builtin_amdgcn_mfma_f32_16x16x32_bf16((a), (b), (c), 0, 0, 0)
#define SBAR() __builtin_amdgcn_sched_barrier(0)
#define WAITV(n) asm volatile("s_waitcnt vmcnt(" #n ")" ::: "memory")
#define BAR() asm volatile("s_barrier" ::: "memory")
#define WAITL() __builtin_amdgcn_s_waitcnt(0xC07F)
#define LBAR() do { asm volatile("" ::: "memory"); WAITL(); BAR(); } while (0)
#define PIN(x) asm volatile("" : "+v"(x))

DI unsigned pk2(float lo, float hi) { f32x2 v = {lo, hi}; return __builtin_bit_cast(unsigned, __builtin_convertvector(v, bfv2)); }
DI float bflo(unsigned v) { return __uint_as_float(v << 16); }
DI float bfhi(unsigned v) { return __uint_as_float(v & 0xffff0000u); }
DI float dot2(unsigned a, unsigned b, float c) { return __builtin_amdgcn_fdot2_f32_bf16(__builtin_bit_cast(bfv2, a), __builtin_bit_cast(bfv2, b), c, false); }
constexpr unsigned ONES2 = 0x3f803f80u;
DI float swap32_sum(float v) { auto r = __builtin_amdgcn_permlane32_swap(__float_as_uint(v), __float_as_uint(v), false, false); return __uint_as_float(r[0]) + __uint_as_float(r[1]); }
DI float swap32_max(float v) { auto r = __builtin_amdgcn_permlane32_swap(__float_as_uint(v), __float_as_uint(v), false, false); return fmaxf(__uint_as_float(r[0]), __uint_as_float(r[1])); }
DI float swap16_sum(float v) { auto r = __builtin_amdgcn_permlane16_swap(__float_as_uint(v), __float_as_uint(v), false, false); return __uint_as_float(r[0]) + __uint_as_float(r[1]); }
template <int CTRL> DI float dppf(float v) { return __uint_as_float(__builtin_amdgcn_update_dpp(0u, __float_as_uint(v), CTRL, 0xf, 0xf, true)); }
DI float sum4(float v) { v += dppf<0xB1>(v); return v + dppf<0x4E>(v); }
DI float sum8(float v) { v += dppf<0xB1>(v); v += dppf<0x4E>(v); return v + dppf<0x141>(v); }
DI float max16(float v) { v = fmaxf(v, dppf<0x128>(v)); v = fmaxf(v, dppf<0x124>(v)); v = fmaxf(v, dppf<0x122>(v)); return fmaxf(v, dppf<0x121>(v)); }
DI float max64(float v) { v = max16(v); auto r = __builtin_amdgcn_permlane16_swap(__float_as_uint(v), __float_as_uint(v), false, false);
  return swap32_max(fmaxf(__uint_as_float(r[0]), __uint_as_float(r[1]))); }
typedef short v4i16_t __attribute__((ext_vector_type(4)));
DI bf16x4 trread(lptr p) { return __builtin_bit_cast(bf16x4, __builtin_amdgcn_ds_read_tr16_b64_v4i16((LAS v4i16_t*)p)); }
DI bf16x8 cat8(bf16x4 a, bf16x4 b) { return __builtin_shufflevector(a, b, 0, 1, 2, 3, 4, 5, 6, 7); }
DI u32x4 lds128(lptr p) { return *(LAS u32x4*)p; }
DI u32x2 lds64(lptr p) { return *(LAS u32x2*)p; }
DI bf16x8 asbf8(u32x4 v) { return __builtin_bit_cast(bf16x8, v); }
DI bf16x4 asbf4(u32x2 v) { return __builtin_bit_cast(bf16x4, v); }
DI u32x4 asu4(bf16x8 v) { return __builtin_bit_cast(u32x4, v); }
DI unsigned rfl(unsigned v) { return __builtin_amdgcn_readfirstlane(v); }
typedef __amdgpu_buffer_rsrc_t rsrc_t;
template <int AUX> DI u32x4 bld128(rsrc_t r, unsigned off) { return __builtin_amdgcn_raw_buffer_load_b128(r, off, 0, AUX); }
template <int AUX> DI u32x2 bld64(rsrc_t r, unsigned off) { return __builtin_amdgcn_raw_buffer_load_b64(r, off, 0, AUX); }
template <int AUX> DI unsigned bld32(rsrc_t r, unsigned off) { return __builtin_amdgcn_raw_buffer_load_b32(r, off, 0, AUX); }
template <int AUX> DI void bst128(rsrc_t r, unsigned off, u32x4 v) { __builtin_amdgcn_raw_buffer_store_b128(v, r, off, 0, AUX); }
template <int AUX> DI void bst64(rsrc_t r, unsigned off, u32x2 v) { __builtin_amdgcn_raw_buffer_store_b64(v, r, off, 0, AUX); }
template <int AUX> DI void bst32(rsrc_t r, unsigned off, unsigned v) { __builtin_amdgcn_raw_buffer_store_b32(v, r, off, 0, AUX); }
DI void glds(unsigned voff, const char* sbase, unsigned lds_base) {
  asm volatile("s_mov_b32 m0, %2\n\ts_nop 0\n\tglobal_load_lds_dwordx4 %0, %1" :: "v"(voff), "s"(sbase), "s"(rfl(lds_base)) : "memory");
}
DI void glds_sc1(unsigned voff, const char* sbase, unsigned lds_base) {
  asm volatile("s_mov_b32 m0, %2\n\ts_nop 0\n\tglobal_load_lds_dwordx4 %0, %1 sc1" :: "v"(voff), "s"(sbase), "s"(rfl(lds_base)) : "memory");
}
DI void touch(unsigned voff, const char* sbase, unsigned lds_dump) {
  asm volatile("s_mov_b32 m0, %2\n\ts_nop 0\n\tglobal_load_lds_dword %0, %1" :: "v"(voff), "s"(sbase), "s"(rfl(lds_dump)) : "memory");
}
DI void touch_sc1(unsigned voff, const char* sbase, unsigned lds_dump) {
  asm volatile("s_mov_b32 m0, %2\n\ts_nop 0\n\tglobal_load_lds_dword %0, %1 sc1" :: "v"(voff), "s"(sbase), "s"(rfl(lds_dump)) : "memory");
}

DI int hg_of(int rk) { return ((rk >> 4) << 2) | (rk & 3); }
DI int tgl_of(int rk) { return (rk >> 2) & 3; }

#define RLX_AGENT __ATOMIC_RELAXED, __HIP_MEMORY_SCOPE_AGENT
constexpr unsigned SPIN_CAP = 1u << 17;
DI bool spin_fail(unsigned& spins, char* wsp, unsigned epoch) {
  __builtin_amdgcn_s_sleep(1);
  GAS unsigned* tmo = (GAS unsigned*)(wsp + W_TMO);
  if (++spins > SPIN_CAP) { __hip_atomic_store(tmo, epoch, RLX_AGENT); return true; }
  return (spins & 255u) == 0u && rfl(__hip_atomic_load(tmo, RLX_AGENT)) == epoch;
}
template <int NW> DI void wait_flags(char* wsp, unsigned off, unsigned epoch, int lane) {
  for (unsigned spins = 0;;) {
    bool ok = true;
#pragma unroll
    for (int i = 0; i < NW; ++i) ok &= __hip_atomic_load((GAS unsigned*)(wsp + off) + i * 64 + lane, RLX_AGENT) == epoch;
    if (__all(ok)) break;
    if (spin_fail(spins, wsp, epoch)) break;
  }
  __builtin_amdgcn_fence(__ATOMIC_ACQUIRE, "wavefront");
}
DI float wait_granules(char* wsp, unsigned off, unsigned epoch, int lane) {
  unsigned mx;
  for (unsigned spins = 0;;) {
    const unsigned long long a = __hip_atomic_load((GAS unsigned long long*)(wsp + off) + lane, RLX_AGENT), b = __hip_atomic_load((GAS unsigned long long*)(wsp + off) + 64 + lane, RLX_AGENT);
    mx = max((unsigned)a, (unsigned)b);
    if (__all((unsigned)(a >> 32) == epoch && (unsigned)(b >> 32) == epoch)) break;
    if (spin_fail(spins, wsp, epoch)) break;
  }
  __builtin_amdgcn_fence(__ATOMIC_ACQUIRE, "wavefront");
  return __uint_as_float(mx);
}
DI void wait_cnt2(char* wsp, unsigned off0, unsigned off1, unsigned want, unsigned epoch, int poller) {
  if ((int)(threadIdx.x >> 6) == poller) {
    GAS unsigned* word = (GAS unsigned*)(wsp + ((threadIdx.x & 1) ? off1 : off0));
    for (unsigned spins = 0;;) {
      if (__all(__hip_atomic_load(word, RLX_AGENT) >= want)) break;
      if (spin_fail(spins, wsp, epoch)) break;
    }
    __builtin_amdgcn_fence(__ATOMIC_ACQUIRE, "wavefront");
  }
  LBAR();
}
DI void signal_cnt(char* wsp, unsigned off) {
  WAITV(0);
  LBAR();
  if (threadIdx.x == 0) (void)__hip_atomic_fetch_add((GAS unsigned*)(wsp + off), 1u, RLX_AGENT);
}
DI void lds_add1(lptr p) { (void)__hip_atomic_fetch_add((LAS unsigned*)p, 1u, __ATOMIC_RELAXED, __HIP_MEMORY_SCOPE_WORKGROUP); }
DI void lds_wait_ge(lptr p, unsigned want) {
  while ((unsigned)rfl(__hip_atomic_load((LAS unsigned*)p, __ATOMIC_RELAXED, __HIP_MEMORY_SCOPE_WORKGROUP)) < want) __builtin_amdgcn_s_sleep(2);
  asm volatile("" ::: "memory");
}

DI void dma_kv3(const char* kvg, unsigned lds0, int c, int j, int lane) {
  const unsigned dst = lds0 + (c == 15 ? 0 : c) * 4 * KV_TILE, l16 = lane * 16; const char* src = kvg + c * 4 * KV_TILE;
  glds_sc1(l16, src + j * 1024, dst + j * 1024);
  glds_sc1(l16, src + (j + 4) * 1024, dst + (j + 4) * 1024);
  const int n = j == 3 ? 7 : j + 8;
  if (j != 2 || lane < 32) glds_sc1(l16, src + n * 1024, dst + n * 1024);
}

constexpr unsigned L1_X = 0, L1_S = 81920, L1_WK = L1_S + 4 * 10240, L1_PART = L1_WK + 25600;
constexpr unsigned L1_CS = L1_PART  , L1_RED = L1_S  , L1_QI = L1_WK  ;
constexpr unsigned L1_ST = 161792  , L1_KC = L1_ST + 512  , L1_KM = L1_KC + 128, L1_DUMP = L1_KM + 16;
static_assert(L1_PART + 12672 <= 161280 && L1_DUMP + 256 <= 163584, "lds");

DI void phase1(lptr lds, rsrc_t ws, int b, u32x4& q1, u32x2& q2, unsigned& epoch, float& kmax2) {
  kargs_t pa = kargs();
  const int tid = threadIdx.x, lane = tid & 63, w = rfl(tid >> 6);
  const int r32 = lane & 31, h = lane >> 5;
  const int xcd = b & 7, rk = b >> 3, tg = 4 * xcd + tgl_of(rk), hg = hg_of(rk);
  const unsigned lds0 = (unsigned)(uintptr_t)lds, l16 = lane * 16;
  unsigned ep = __hip_atomic_load((GAS unsigned*)(pa->ws + W_EPOCH), RLX_AGENT);
  {
    const char* xb = pa->x + (size_t)(tg * 64 + 8 * w) * 1280;
    const int tl = lane >> 4;
    unsigned v0 = tl * 1280 + ((lane ^ (8 * w + tl)) & 15) * 16, v1 = (4 + tl) * 1280 + ((lane ^ (8 * w + 4 + tl)) & 15) * 16, xd = lds0 + L1_X + 8 * w * 256;
#pragma unroll 1
    for (int a = 0; a < 5; ++a) { glds(v0, xb, xd); glds(v1, xb, xd + 1024); v0 += 256; v1 += 256; xd += 16384; }
  }
  if (hg < 4) {
    const char* W = (hg < 2 ? pa->wk : pa->wv) + w * 1024;
#pragma unroll
    for (int i = 0; i < 3; ++i) glds(l16 + i * 8192, W, lds0 + L1_WK + w * 1024 + i * 8192);
    if (w == 0) glds(l16 + 3 * 8192, W, lds0 + L1_WK + 3 * 8192);
  }
  const char* wqb = pa->wq + hg * 160;
  unsigned vq[3], dq[3];
#pragma unroll
  for (int i = 0; i < 3; ++i) {
    const int n0 = w + 7 * i, n = n0 < 20 ? n0 : 13, q = n * 64 + lane, slot = (q * 6554) >> 16, row = (slot & ~12) | ((slot & 4) << 1) | ((slot & 8) >> 1);
    vq[i] = row * 1280 + (q - slot * 10) * 16; dq[i] = n * 1024;
  }
  const bool m3 = w < 6 || lane == 0;
  auto dma_pair = [&](const char* src, unsigned dst) {
    if (w < 7) {
      glds(vq[0], src, dst + dq[0]);
      glds(vq[1], src, dst + dq[1]);
      if (m3) glds(vq[2], src, dst + dq[2]);
    }
  };
  SBAR();
  STAMPX(8);
  const int i16 = lane & 15, tq = i16 >> 2, tp = i16 & 3, grp = (lane >> 4) & 1;
  WAITV(0);
  LBAR();
  PIN(ep); epoch = rfl(ep) + 1u;
  STAMPX(1);
  const int kth = hg & 1; const bool isV = hg >= 2;
  f32x16 kacc = {}; float kcs = 0.f; unsigned kn2 = 0u;
  auto dma_first = [&]() { dma_pair(wqb, lds0 + L1_S); dma_pair(wqb + 128 * 1280, lds0 + L1_S + 20480); };
  if (w >= 4) {
    dma_first();
#pragma unroll
    for (int ps = 0; ps < 2; ++ps) {
      const int t = (ps ? 32 - 32 * kth : 32 * kth) + 8 * (w - 4) + (lane >> 3), e = lane & 7;
      u32x4 v[10];
#pragma unroll
      for (int i = 0; i < 10; ++i) v[i] = lds128(lds + L1_X + (i >> 1) * 16384 + t * 256 + ((e + 8 * (i & 1) + 8 * ((t >> 1) & 1)) & 15) * 16);
      float a4[4] = {}, b4[4] = {};
#pragma unroll
      for (int i = 0; i < 10; ++i)
#pragma unroll
        for (int j = 0; j < 4; ++j) { a4[j] = dot2(v[i][j], v[i][j], a4[j]); b4[j] = dot2(v[i][j], ONES2, b4[j]); }
      const float sq = sum8((a4[0] + a4[1]) + (a4[2] + a4[3])), sm = sum8((b4[0] + b4[1]) + (b4[2] + b4[3]));
      const float mean = sm * (1.f / DM), rstd = rsqrtf(fmaxf(sq * (1.f / DM) - mean * mean, 0.f) + LN_EPS);
      if (e == 0) { ((LAS float*)(lds + L1_ST))[t] = mean; ((LAS float*)(lds + L1_ST))[64 + t] = rstd; }
      if (ps == 0) {
        LBAR(); dma_pair(wqb + 256 * 1280, lds0 + L1_WK);
        const int ln = (tgl_of(rk) * 4 + (w - 4)) * 32 + r32;
        if (h == 0) touch((ln >> 1) * 1280 + (ln & 1) * 128, wqb + 384 * 1280 - ((hg * 160) & 127), lds0 + L1_DUMP);
      }
    }
  } else if (hg < 4) {
    lptr wb = lds + L1_WK + (160 * w + 8 * h + tq) * 40 + grp * 32 + 8 * tp;
    const int tok = kth * 32 + r32;
    u32x4 xf[10]; bf16x8 wf[10];
#pragma unroll
    for (int i = 0; i < 10; ++i) { const int c = 20 * w + 2 * i + h;
      xf[i] = lds128(lds + L1_X + (c >> 4) * 16384 + tok * 256 + ((c ^ tok) & 15) * 16); wf[i] = cat8(trread(wb + i * 640), trread(wb + i * 640 + 160)); }
    SBAR();
    if (w != 0) dma_first();
    if (isV) {
#pragma unroll
      for (int i = 0; i < 10; ++i) kacc = MFMA16(asbf8(xf[i]), wf[i], kacc);
    } else {
#pragma unroll
      for (int i = 0; i < 10; ++i) kacc = MFMA16(wf[i], asbf8(xf[i]), kacc);
    }
    float c4[4] = {};
#pragma unroll
    for (int i = 0; i < 10; ++i) { const u32x4 wu = asu4(wf[i]);
#pragma unroll
      for (int j = 0; j < 4; ++j) c4[j] = dot2(wu[j], ONES2, c4[j]); }
    kcs = swap32_sum((c4[0] + c4[1]) + (c4[2] + c4[3]));
    if (w > 0) {
      LAS f32x4* pa = (LAS f32x4*)(lds + L1_PART + (w - 1) * 4224) + lane;
#pragma unroll
      for (int g = 0; g < 4; ++g) pa[g * 64] = f32x4{kacc[4 * g], kacc[4 * g + 1], kacc[4 * g + 2], kacc[4 * g + 3]};
      if (h == 0) ((LAS float*)(lds + L1_PART + (w - 1) * 4224 + 4096))[r32] = kcs;
    }
  } else dma_first();
  if (w < 4) {
    LBAR();
    if (w != 0 || hg >= 4) dma_pair(wqb + 256 * 1280, lds0 + L1_WK);
  }
  if (hg < 4 && w == 0) {
#pragma unroll
    for (int v = 0; v < 3; ++v) {
      LAS f32x4* pa = (LAS f32x4*)(lds + L1_PART + v * 4224) + lane;
#pragma unroll
      for (int g = 0; g < 4; ++g) { const f32x4 t = pa[g * 64]; kacc[4 * g] += t[0]; kacc[4 * g + 1] += t[1]; kacc[4 * g + 2] += t[2]; kacc[4 * g + 3] += t[3]; }
      kcs += ((LAS float*)(lds + L1_PART + v * 4224 + 4096))[r32];
    }
    const int tile = tg * 2 + kth;
    const LAS float* st = (const LAS float*)(lds + L1_ST) + kth * 32;
    if (lane == 0 && hg == 0) { bst32<AUX_SC1>(ws, W_CNTO + tg * 256, 0u); bst32<AUX_SC1>(ws, W_CNTH + tg * 256, 0u); }
    if (lane == 0 && hg == 1 && !(tg & 1)) bst32<AUX_SC1>(ws, W_CNTG + (tg >> 1) * 256, 0u);
    if (!isV) {
      LAS float* tab = (LAS float*)(lds + L1_KC);
      if (h == 0) tab[r32] = kcs;
      const float mean = st[r32], rstd = st[64 + r32];
      asm volatile("" ::: "memory"); WAITL(); asm volatile("" ::: "memory");
      float kv[12];
#pragma unroll
      for (int g = 0; g < 3; ++g) { const f32x4 c4 = *(LAS f32x4*)(tab + 8 * g + 4 * h);
#pragma unroll
        for (int j = 0; j < 4; ++j) kv[4 * g + j] = (kacc[4 * g + j] - mean * c4[j]) * rstd; }
      const u32x4 k1 = {pk2(kv[0], kv[1]), pk2(kv[2], kv[3]), pk2(kv[4], kv[5]), pk2(kv[6], kv[7])};
      const u32x2 k2 = {pk2(kv[8], kv[9]), pk2(kv[10], kv[11])};
      bst128<AUX_SC1>(ws, W_KV + tile * KV_TILE + l16, k1);
      if (h == 0) bst64<AUX_SC1>(ws, W_KV + tile * KV_TILE + KV_K2 + r32 * 8, k2);
      float n2 = 0.f;
#pragma unroll
      for (int j = 0; j < 4; ++j) n2 = dot2(k1[j], k1[j], n2);
      if (h == 0) { n2 = dot2(k2[0], k2[0], n2); n2 = dot2(k2[1], k2[1], n2); }
      kn2 = __float_as_uint(max64(swap32_sum(n2)));
    } else {
      const unsigned cv = r32 == 20 ? ONES2 : 0u;
#pragma unroll
      for (int s = 0; s < 2; ++s) {
        float vv[8];
#pragma unroll
        for (int g = 0; g < 2; ++g) { const f32x4 m4 = *(const LAS f32x4*)(st + 16 * s + 8 * g + 4 * h), r4 = *(const LAS f32x4*)(st + 64 + 16 * s + 8 * g + 4 * h);
#pragma unroll
          for (int j = 0; j < 4; ++j) vv[4 * g + j] = (kacc[8 * s + 4 * g + j] - m4[j] * kcs) * r4[j]; }
        u32x4 o = {pk2(vv[0], vv[1]), pk2(vv[2], vv[3]), pk2(vv[4], vv[5]), pk2(vv[6], vv[7])};
        if (r32 >= 20) o = u32x4{cv, cv, cv, cv};
        if (r32 < 22) bst128<AUX_SC1>(ws, W_KV + tile * KV_TILE + KV_V + (2 * s + h) * KV_VS + r32 * 16, o);
      }
    }
  }
  if (hg < 4 && w == 0) {
    dma_first(); dma_pair(wqb + 256 * 1280, lds0 + L1_WK);
    WAITV(9);
    if (lane == 0) __hip_atomic_store((GAS unsigned long long*)(pa->ws + W_FKV) + tg * 4 + hg, ((unsigned long long)epoch << 32) | kn2, RLX_AGENT);
    WAITV(4);
  } else if (w >= 4) WAITV(4); else WAITV(3);
  LBAR();
  STAMPX(2);
  {
    const int mt = w & 3, kh = w >> 2, hh = w & 3, th = w >> 2, t16 = lane & 15, g = lane >> 4, tk = mt * 16 + t16;
    f32x4 acc[5] = {}, csa[5] = {};
    const unsigned xo0 = L1_X + tk * 256 + (((8 * kh + g) ^ tk) & 15) * 16, xo1 = L1_X + tk * 256 + (((8 * kh + 4 + g) ^ tk) & 15) * 16;
    const unsigned wrow = kh * 10240 + (16 * (g >> 1) + 4 * (g & 1) + tq) * 160 + tp * 8;
    const bf16x8 ones8 = asbf8(u32x4{ONES2, ONES2, ONES2, ONES2});
    unsigned xa = 0, wa = L1_S + wrow;
    bf16x8 bf[2][5]; u32x4 xf[2];
#define Q_RD(e, XA, WA) do { xf[e] = lds128(lds + ((e) ? xo1 : xo0) + (XA)); lptr wp_ = lds + (WA) + (e) * 5120; \
      _Pragma("unroll") for (int nt = 0; nt < 5; ++nt) bf[e][nt] = cat8(trread(wp_ + nt * 32), trread(wp_ + nt * 32 + 1280)); SBAR(); } while (0)
#define Q_MM(e, CS) do { _Pragma("unroll") for (int nt = 0; nt < 5; ++nt) acc[nt] = MFMAS(bf[e][nt], asbf8(xf[e]), acc[nt]); \
      if (CS) { _Pragma("unroll") for (int nt = 0; nt < 5; ++nt) csa[nt] = MFMAS(bf[e][nt], ones8, csa[nt]); } SBAR(); } while (0)
    Q_RD(0, xa, wa);
#pragma unroll 1
    for (int pr = 0; pr < 4; ++pr) {
      const bool cs = pr == mt;
      Q_RD(1, xa, wa);
      Q_MM(0, cs);
      if (pr == 1 || pr == 2) WAITV(3); else if (pr == 3) WAITV(0);
      LBAR();
      if (pr < 2) dma_pair(wqb + (size_t)(pr + 3) * 128 * 1280, lds0 + L1_S + pr * 20480);
      xa += 16384;
      wa = (pr == 1 ? L1_WK : (pr == 2 ? L1_S : L1_S + 20480)) + wrow;
      Q_RD(0, xa, wa);
      Q_MM(1, cs);
    }
    unsigned long long ga = 0, gb = 0;
    if (w == 7) {
      asm volatile("global_load_dwordx2 %0, %2, %3 sc1\n\tglobal_load_dwordx2 %1, %2, %3 offset:512 sc1" : "=&v"(ga), "=&v"(gb) : "v"(lane * 8), "s"(pa->ws + W_FKV) : "memory");
      Q_RD(1, xa, wa); Q_MM(0, false); Q_MM(1, false);
      asm volatile("s_waitcnt vmcnt(0)" : "+v"(ga), "+v"(gb) :: "memory");
      float km = __uint_as_float(max((unsigned)ga, (unsigned)gb));
      if (!__all((unsigned)(ga >> 32) == epoch && (unsigned)(gb >> 32) == epoch)) km = wait_granules(pa->ws, W_FKV, epoch, lane);
      km = max64(km);
      if (lane == 0) *(LAS float*)(lds + L1_KM) = km;
    } else { Q_RD(1, xa, wa); Q_MM(0, mt == 0); Q_MM(1, mt == 0); }
#undef Q_RD
#undef Q_MM
    LAS f32x4* red = (LAS f32x4*)(lds + L1_RED) + mt * 320 + lane;
    if (kh == 0) { red[3 * 64] = acc[3]; red[4 * 64] = acc[4]; } else { red[0] = acc[0]; red[64] = acc[1]; red[128] = acc[2]; }
    if (t16 == 0) {
#pragma unroll
      for (int nt = 0; nt < 5; ++nt) *(LAS f32x4*)(lds + L1_CS + w * 320 + nt * 64 + g * 16) = csa[nt];
    }
    const float mean = ((LAS float*)(lds + L1_ST))[tk], rstd = ((LAS float*)(lds + L1_ST))[64 + tk] * QSCALE;
    LBAR();
    kmax2 = *(LAS float*)(lds + L1_KM);
    if (th == 0) { dma_kv3(pa->ws + W_KV, lds0, 0, hh, lane); dma_kv3(pa->ws + W_KV, lds0, 2, hh, lane); } else dma_kv3(pa->ws + W_KV, lds0, 1, hh, lane);
    auto finish = [&](auto NT0, auto N) {
      constexpr int nt0 = NT0.value, n = N.value;
      f32x4 c8[n], a[n];
#pragma unroll
      for (int i = 0; i < n; ++i) { c8[i] = *(LAS f32x4*)(lds + L1_CS + (lane & 7) * 320 + (nt0 + i) * 64 + g * 16); a[i] = acc[nt0 + i] + red[(nt0 + i) * 64]; }
#pragma unroll
      for (int i = 0; i < n; ++i)
#pragma unroll
        for (int j = 0; j < 4; ++j) c8[i][j] += dppf<0xB1>(c8[i][j]);
#pragma unroll
      for (int i = 0; i < n; ++i)
#pragma unroll
        for (int j = 0; j < 4; ++j) c8[i][j] += dppf<0x4E>(c8[i][j]);
#pragma unroll
      for (int i = 0; i < n; ++i)
#pragma unroll
        for (int j = 0; j < 4; ++j) c8[i][j] += dppf<0x141>(c8[i][j]);
      SBAR();
#pragma unroll
      for (int i = 0; i < n; ++i) {
        float qv[4];
#pragma unroll
        for (int j = 0; j < 4; ++j) qv[j] = (a[i][j] - mean * c8[i][j]) * rstd;
        *(LAS u32x2*)(lds + L1_QI + tk * 168 + (nt0 + i) * 32 + g * 8) = u32x2{pk2(qv[0], qv[1]), pk2(qv[2], qv[3])};
      }
    };
    if (kh == 0) finish(IC<0>{}, IC<3>{}); else finish(IC<3>{}, IC<2>{});
    WAITV(0);
    LBAR();
    lptr qp = lds + L1_QI + (th * 32 + r32) * 168 + hh * 40 + 8 * h;
    const u32x2 qa = lds64(qp), qb = lds64(qp + 16), qc = lds64(qp + 32 - 8 * h);
    q1 = u32x4{qa[0], qa[1], qb[0], qb[1]};
    q2 = h ? u32x2{0u, 0u} : qc;
  }
  STAMPX(3);
}

constexpr unsigned L2_CO = 60 * KV_TILE, L2_CR = L2_CO + 16, L2_CP = L2_CO + 32, L2_CS = L2_CO + 48, L2_CD = L2_CO + 112, L2_CQ = L2_CO + 128, L2_CF = L2_CO + 144, L2_DUMP = L2_CO + 256;
constexpr unsigned L2_RT = 52 * KV_TILE;
constexpr unsigned L3_WO = 4 * KV_TILE, L3_RED = L3_WO + 102400;
#define PRIO_M 3
#define EXP2(a) __builtin_amdgcn_exp2f(a)

DI f32x16 attn_exact(rsrc_t ws, u32x4 q1, u32x2 q2, int lane) {
  const int r32 = lane & 31, h = lane >> 5, rv = r32 < 21 ? r32 : 21;
  float mx = -3.0e38f;
#pragma unroll 1
  for (int t = 0; t < 64; ++t) {
    const unsigned base = W_KV + t * KV_TILE;
    f32x16 s = MFMA8(asbf4(bld64<AUX_SC1>(ws, base + KV_K2 + r32 * 8)), asbf4(q2), f32x16{});
    s = MFMA16(asbf8(bld128<AUX_SC1>(ws, base + lane * 16)), asbf8(q1), s);
#pragma unroll
    for (int j = 0; j < 16; ++j) mx = fmaxf(mx, s[j]);
  }
  mx = swap32_max(mx);
  f32x16 o = {};
#pragma unroll 1
  for (int t = 0; t < 64; ++t) {
    const unsigned base = W_KV + t * KV_TILE;
    f32x16 s = MFMA8(asbf4(bld64<AUX_SC1>(ws, base + KV_K2 + r32 * 8)), asbf4(q2), f32x16{});
    s = MFMA16(asbf8(bld128<AUX_SC1>(ws, base + lane * 16)), asbf8(q1), s);
    u32x4 pp[2];
#pragma unroll
    for (int j = 0; j < 8; ++j) pp[j >> 2][j & 3] = pk2(EXP2(s[2 * j] - mx), EXP2(s[2 * j + 1] - mx));
#pragma unroll
    for (int e = 0; e < 2; ++e) o = MFMA16(asbf8(bld128<AUX_SC1>(ws, base + KV_V + (2 * e + h) * KV_VS + rv * 16)), asbf8(pp[e]), o);
  }
  return o;
}

DI void phase_attn(lptr lds, rsrc_t ws, int b, const u32x4 q1, const u32x2 q2, unsigned epoch, float kmax2) {
  const int tid = threadIdx.x, lane = tid & 63, w = rfl(tid >> 6);
  const int r32 = lane & 31, h = lane >> 5;
  const int xcd = b & 7, rk = b >> 3, tg = 4 * xcd + tgl_of(rk), hg = hg_of(rk);
  const int hh = w & 3, th = w >> 2;
  const unsigned lds0 = (unsigned)(uintptr_t)lds, l16 = lane * 16;
  if (tid < 10) *(LAS unsigned*)(lds + L2_CO + tid * 16) = 0u;
  f32x16 cb;
  {
    float n2 = 0.f;
#pragma unroll
    for (int j = 0; j < 4; ++j) n2 = dot2(q1[j], q1[j], n2);
    n2 = dot2(q2[0], q2[0], n2); n2 = dot2(q2[1], q2[1], n2);
    const float ref = sqrtf(swap32_sum(n2) * kmax2) * 1.001f;
#pragma unroll
    for (int j = 0; j < 16; ++j) cb[j] = -ref;
    PIN(cb);
  }
  const int rv = r32 < 21 ? r32 : 21;
  const unsigned oK1 = l16, oK2 = KV_K2 + r32 * 8, oV = KV_V + h * KV_VS + rv * 16;
  f32x16 o = {};
  LBAR();
  STAMPX(9);
  {
    lptr aK1 = lds + oK1; lptr aK2 = lds + oK2; lptr aV = lds + oV;
    u32x4 k1 = lds128(aK1); u32x2 k2 = lds64(aK2);
    f32x16 sc = MFMA8(asbf4(k2), asbf4(q2), cb); sc = MFMA16(asbf8(k1), asbf8(q1), sc);
    k1 = lds128(aK1 + KV_TILE); k2 = lds64(aK2 + KV_TILE);
    u32x4 v0 = lds128(aV), v1 = {}, p0, p1 = {};
    u32x4 k1b, v0b, v1b; u32x2 k2b;
    f32x16 sn;
#define GRP(MF, SC, i, P, j) do { MF; SBAR(); { const float e0 = EXP2(SC[i]), e1 = EXP2(SC[i + 1]), e2 = EXP2(SC[i + 2]), e3 = EXP2(SC[i + 3]); P[j] = pk2(e0, e1); P[j + 1] = pk2(e2, e3); } PIN(P); SBAR(); } while (0)
#define TILE(SC, SN, K1, K2, V0, V1, K1N, K2N, V0N, V1N, PK1, PK2, PV0, PV1) do { \
      WAITL(); \
      K2N = lds64(PK2); K1N = lds128(PK1); V0N = lds128(PV0); V1N = lds128(PV1); SBAR(); \
      GRP(SN = MFMA8(asbf4(K2), asbf4(q2), cb), SC, 0, p0, 0); \
      GRP(o = MFMA16(asbf8(V1), asbf8(p1), o), SC, 4, p0, 2); \
      GRP(SN = MFMA16(asbf8(K1), asbf8(q1), SN), SC, 8, p1, 0); \
      GRP(o = MFMA16(asbf8(V0), asbf8(p0), o), SC, 12, p1, 2); } while (0)
#define TA(T) TILE(sc, sn, k1, k2, v0, v1, k1b, k2b, v0b, v1b, aK1 + ((T) + 2) * KV_TILE, aK2 + ((T) + 2) * KV_TILE, aV + ((T) + 1) * KV_TILE, aV + (T) * KV_TILE + 2 * KV_VS)
#define TB(T) TILE(sn, sc, k1b, k2b, v0b, v1b, k1, k2, v0, v1, aK1 + ((T) + 2) * KV_TILE, aK2 + ((T) + 2) * KV_TILE, aV + ((T) + 1) * KV_TILE, aV + (T) * KV_TILE + 2 * KV_VS)
#define ADV() do { aK1 += 8 * KV_TILE; aK2 += 8 * KV_TILE; aV += 8 * KV_TILE; } while (0)
#define BODY8() do { TA(0); TB(1); TA(2); TB(3); TA(4); TB(5); TA(6); TB(7); ADV(); } while (0)
    if (th == 1) {
      const char* kvg = kargs()->ws + W_KV;
#define DMA3(c) do { dma_kv3(kvg, lds0, c, hh, lane); SBAR(); } while (0)
      __builtin_amdgcn_s_setprio(2);
      PIN(sc);
      DMA3(3); TA(0); DMA3(4); TB(1); DMA3(5); TA(2); DMA3(6); TB(3);
      DMA3(7); TA(4); DMA3(8); TB(5); DMA3(9); TA(6); DMA3(10); TB(7); ADV();
      if (lane == 0) lds_add1(lds + L2_CP);
      WAITV(6); if (lane == 0) lds_add1(lds + L2_CS);
      lds_wait_ge(lds + L2_CS, 4u);
      DMA3(11); TA(0); DMA3(12); TB(1); TA(2); TB(3); TA(4); TB(5); TA(6); TB(7); ADV();
      WAITV(0); if (lane == 0) lds_add1(lds + L2_CS + 16);
#pragma nounroll
      for (int m = 2; m < 6; ++m) {
        if (m == 4) { lds_wait_ge(lds + L2_CS + 16, 4u); lds_wait_ge(lds + L2_CR, 4u); DMA3(13); DMA3(14); }
        if (m == 5) { WAITV(0); if (lane == 0) lds_add1(lds + L2_CS + 32); lds_wait_ge(lds + L2_CP, 8u); DMA3(15); }
        if (m == PRIO_M) __builtin_amdgcn_s_setprio(0);
        BODY8();
      }
      WAITV(0); if (lane == 0) lds_add1(lds + L2_CS + 48);
#undef DMA3
    } else {
      __builtin_amdgcn_s_setprio(1);
      const int ga = b * 4 + hh;
      const bool two = b < 56 && hh == (b & 3);
      auto gu_dma = [&](int gj, int blk) {
        kargs_t pa = kargs(); const int cgf = gj / 40, r = gj % 40;
        const char* src = ((r & 1) ? pa->wu : pa->wg) + (size_t)(32 * (r >> 1)) * (FF * 2) + cgf * 128;
        unsigned vo = (lane >> 3) * (FF * 2) + (lane & 7) * 16, dst = lds0 + L2_RT + blk * 4096;
#pragma unroll
        for (int i = 0; i < 4; ++i) { glds(vo, src, dst); vo += 8 * FF * 2; dst += 1024; }
      };
      auto gu_store = [&](int gj, int blk) {
        const int cgf = gj / 40, r = gj % 40, i16 = lane & 15, g4 = lane >> 4;
        lptr a = lds + L2_RT + blk * 4096 + (8 * g4 + (i16 >> 2)) * 128 + (i16 & 3) * 8;
        const unsigned o1 = W_WGU + ((cgf * 80 + (r >> 1)) * 2 + (r & 1)) * 1024 + l16, o2 = W_CSP + ((cgf * 8 + (r & 1)) * 20 + (r >> 1)) * 64 + g4 * 16;
#pragma unroll
        for (int u = 0; u < 4; ++u) {
          const bf16x8 f = cat8(trread(a + u * 32), trread(a + u * 32 + 512));
          bst128<AUX_SC1>(ws, o1 + u * 40960, asu4(f));
          const f32x4 cs = MFMAS(f, asbf8(u32x4{ONES2, ONES2, ONES2, ONES2}), f32x4{});
          if (i16 == 0) bst128<AUX_SC1>(ws, o2 + u * 2560, __builtin_bit_cast(u32x4, cs));
        }
      };
      gu_dma(ga, hh);
      if (two) gu_dma(1024 + b, 4);
      {
        kargs_t pa = kargs();
        const int nl = ga < 986 ? 24 : (ga == 986 ? 16 : 0);
        if (lane < nl) touch(lane * 128, ga < 720 ? pa->wd + (size_t)ga * 3072 : pa->wo + (size_t)(ga - 720) * 3072, lds0 + L2_DUMP);
      }
      PIN(sc);
#pragma nounroll
      for (int m = 0; m < 6; ++m) {
        if (m == 1) {
          if (lane == 0) lds_add1(lds + L2_CP);
          u32x4 rd[3];
          WAITV(0);
          {
            kargs_t pa = kargs();
            const char* src = ga < 720 ? pa->wd + (size_t)ga * 3072 : pa->wo + (size_t)(ga < 987 ? ga - 720 : 0) * 3072;
#pragma unroll
            for (int i = 0; i < 3; ++i) rd[i] = *(const GAS u32x4*)(src + ((ga == 986 && i == 2) ? 0 : i * 1024) + l16);
          }
          gu_store(ga, hh);
          if (two) gu_store(1024 + b, 4);
          if (ga < 987) {
            const bool dn = ga < 720; const int j0 = dn ? ga * 3 : (ga - 720) * 3;
#pragma unroll
            for (int i = 0; i < 3; ++i) { const int a5 = (j0 + i) * 4 + (lane >> 4), f = (a5 * 52429) >> 18, c = (a5 - 5 * f) * 16 + (lane & 15), cg = (c * 6554) >> 16, cc = c - cg * 10;
              const unsigned o = dn ? W_WD + (cg * FF + ((f & ~12) | ((f & 4) << 1) | ((f & 8) >> 1))) * 160 : W_WO + (cg * DM + f) * 160;
              if (ga < 986 || i < 2) bst128<AUX_SC1>(ws, o + cc * 16, rd[i]); }
          }
          WAITL(); if (lane == 0) lds_add1(lds + L2_CR);
          lds_wait_ge(lds + L2_CS, 4u);
        }
        if (m == 2) {
          WAITV(0);
          unsigned old = 0; if (lane == 0) old = __hip_atomic_fetch_add((LAS unsigned*)(lds + L2_CD), 1u, __ATOMIC_RELAXED, __HIP_MEMORY_SCOPE_WORKGROUP);
          if (lane == 0 && old == 3u) __hip_atomic_store((GAS unsigned*)(kargs()->ws + W_FRT) + b, epoch, RLX_AGENT);
        }
        if (m == 4) {
          char* wsp = kargs()->ws;
          wait_flags<4>(wsp, W_FRT, epoch, lane);
          if (lane == 0) lds_add1(lds + L2_CF);
          if (b == 0 && w == 0 && lane == 0) __hip_atomic_store((GAS unsigned*)(wsp + W_EPOCH), epoch, RLX_AGENT);
          const int ngl = rk & 3, u0 = 27 * (xcd & 3) + 7 * ngl, part = (rk >> 2) * 4 + hh;
          const int nl = (ngl == 3 ? 6 : 7) * 10;
          touch_sc1((part * nl + lane) * 128, wsp + W_WGU + (size_t)u0 * 40960, lds0 + L2_DUMP);
          if (lane < nl - 64) touch_sc1((part * nl + 64 + lane) * 128, wsp + W_WGU + (size_t)u0 * 40960, lds0 + L2_DUMP);
          if (lane < 50) touch_sc1(((tgl_of(rk) * 4 + hh) * 50 + lane) * 128, wsp + W_WO + hg * DM * 160, lds0 + L2_DUMP);
          lds_wait_ge(lds + L2_CS + 16, 4u);
        }
        BODY8();
      }
    }
    lds_wait_ge(lds + L2_CS + 32, 4u);
    TA(0); TB(1);
    if (lane == 0) lds_add1(lds + L2_CQ);
    TA(2); TB(3); TA(4); TB(5); TA(6); TB(7); ADV();
    lds_wait_ge(lds + L2_CQ, 8u); lds_wait_ge(lds + L2_CF, 1u);
    const char* wsrc = kargs()->ws + W_WO + hg * DM * 160 + w * 1024; unsigned wdst = lds0 + L3_WO + w * 1024, wvo = l16;
#define WO2() do { glds_sc1(wvo, wsrc, wdst); glds_sc1(wvo + 8192, wsrc, wdst + 8192); wvo += 16384; wdst += 16384; SBAR(); } while (0)
    lds_wait_ge(lds + L2_CS + 48, 4u);
    {
      lptr zK1 = lds + oK1; lptr zK2 = lds + oK2; lptr zV = lds + oV;
      WO2(); TA(0); WO2(); TB(1);
      WO2(); TILE(sc, sn, k1, k2, v0, v1, k1b, k2b, v0b, v1b, zK1, zK2, aV + 3 * KV_TILE, aV + 2 * KV_TILE + 2 * KV_VS);
      WO2(); TILE(sn, sc, k1b, k2b, v0b, v1b, k1, k2, v0, v1, zK1 + KV_TILE, zK2 + KV_TILE, zV, aV + 3 * KV_TILE + 2 * KV_VS);
      aK1 = zK1; aK2 = zK2; aV = zV;
      WO2(); TA(0); WO2(); TB(1);
      if (w < 4) glds_sc1(wvo, wsrc, wdst);
      TA(2); TB(3);
    }
#undef WO2
    WAITL();
    o = MFMA16(asbf8(v1), asbf8(p1), o);
    __builtin_amdgcn_s_setprio(0);
  }
  STAMPX(10);
  float lsum = __uint_as_float(__builtin_amdgcn_permlane32_swap(__float_as_uint(o[8]), __float_as_uint(o[8]), false, false)[1]);
  if (__builtin_expect(__any(!(lsum >= 5.421010862427522e-20f)), 0)) {
    o = attn_exact(ws, q1, q2, lane);
    lsum = __uint_as_float(__builtin_amdgcn_permlane32_swap(__float_as_uint(o[8]), __float_as_uint(o[8]), false, false)[1]);
  }
  {
    const float inv = __builtin_amdgcn_rcpf(lsum);
    const u32x4 oa = {pk2(o[0] * inv, o[1] * inv), pk2(o[2] * inv, o[3] * inv), pk2(o[4] * inv, o[5] * inv), pk2(o[6] * inv, o[7] * inv)};
    const u32x2 ob = {pk2(o[8] * inv, o[9] * inv), pk2(o[10] * inv, o[11] * inv)};
    const unsigned tile16 = tg * 4 + th * 2 + (r32 >> 4), g0 = 10 * hg + 5 * (hh >> 1);
    bst128<AUX_SC1>(ws, W_OF + ((tile16 * 80 + g0 + 2 * (hh & 1) + h) * 16 + (r32 & 15)) * 16, oa);
    if (h == 0) bst64<AUX_SC1>(ws, W_OF + ((tile16 * 80 + g0 + 4) * 16 + (r32 & 15)) * 16 + 8 * (hh & 1), ob);
  }
  WAITV(0);
  LBAR();
  if (tid == 0) (void)__hip_atomic_fetch_add((GAS unsigned*)(kargs()->ws + W_CNTO + tg * 256), 1u, RLX_AGENT);
}

DI void phase_oproj(lptr lds, rsrc_t ws, int b, u32x2 (&hres)[5], unsigned epoch) {
  kargs_t pa = kargs();
  const int tid = threadIdx.x, lane = tid & 63, w = rfl(tid >> 6);
  const int xcd = b & 7, rk = b >> 3, tg = 4 * xcd + tgl_of(rk), cg = hg_of(rk);
  const int mt = w & 3, kh = w >> 2, t16 = lane & 15, g = lane >> 4, tq = t16 >> 2, tp = t16 & 3;
  const int tok = tg * 64 + mt * 16 + t16;
  u32x2 xr[5] = {};
  if (kh == 0) {
#pragma unroll
    for (int nt = 0; nt < 5; ++nt) xr[nt] = *(const GAS u32x2*)(pa->x + (size_t)tok * 1280 + cg * 160 + nt * 32 + g * 8);
  }
  unsigned r1[10], r2[10];
#pragma unroll
  for (int i = 0; i < 10; ++i) {
    const int t = 4 * i + g, a5 = (t * 13) >> 6, m = t - 5 * a5, pair = 8 * kh + a5;
    const int last = m >> 2, ra = pair * 40 + (m >> 1) * 20 + (m & 1) * 4 - last * 24;
    r1[i] = L3_WO + (ra + tq) * 160 + tp * 8; r2[i] = r1[i] + (8 + 12 * last) * 160;
  }
  wait_cnt2(pa->ws, W_CNTO + tg * 256, W_CNTO + tg * 256, 8u, epoch, 0);
  STAMPX(13);
  bf16x8 bf[2][5];
  u32x4 a[10];
#pragma unroll
  for (int i = 0; i < 10; ++i) a[i] = bld128<AUX_SC1>(ws, W_OF + ((tg * 4 + mt) * 80 + 4 * (10 * kh + i)) * 256 + lane * 16);
#pragma unroll
  for (int nt = 0; nt < 5; ++nt) bf[0][nt] = cat8(trread(lds + r1[0] + nt * 32), trread(lds + r2[0] + nt * 32));
  f32x4 acc[5] = {};
#pragma unroll
  for (int i = 0; i < 10; ++i) {
    if (i + 1 < 10) {
#pragma unroll
      for (int nt = 0; nt < 5; ++nt) bf[(i + 1) & 1][nt] = cat8(trread(lds + r1[i + 1] + nt * 32), trread(lds + r2[i + 1] + nt * 32));
    }
    SBAR();
#pragma unroll
    for (int nt = 0; nt < 5; ++nt) acc[nt] = MFMAS(bf[i & 1][nt], asbf8(a[i]), acc[nt]);
    SBAR();
  }
  STAMPX(14);
  LAS f32x4* red = (LAS f32x4*)(lds + L3_RED) + mt * 320 + lane;
  if (kh == 1) {
#pragma unroll
    for (int nt = 0; nt < 5; ++nt) red[nt * 64] = acc[nt];
  }
  LBAR();
  if (kh == 0) {
    float s1 = 0.f, s2 = 0.f;
#pragma unroll
    for (int nt = 0; nt < 5; ++nt) {
      const f32x4 o = red[nt * 64];
      const float h0 = bflo(xr[nt][0]) + acc[nt][0] + o[0], h1 = bfhi(xr[nt][0]) + acc[nt][1] + o[1], h2 = bflo(xr[nt][1]) + acc[nt][2] + o[2], h3 = bfhi(xr[nt][1]) + acc[nt][3] + o[3];
      const u32x2 hv = {pk2(h0, h1), pk2(h2, h3)};
      hres[nt] = hv;
      s1 = dot2(hv[0], ONES2, s1); s1 = dot2(hv[1], ONES2, s1); s2 = dot2(hv[0], hv[0], s2); s2 = dot2(hv[1], hv[1], s2);
      const int gam = 10 * cg + 2 * nt + (g >> 1);
      bst64<AUX_SC1>(ws, W_HF + (((tg * 4 + mt) * 20 + (gam >> 2)) * 64 + (gam & 3) * 16 + t16) * 16 + (g & 1) * 8, hv);
    }
    s1 = swap32_sum(swap16_sum(s1)); s2 = swap32_sum(swap16_sum(s2));
    if (g == 0) bst64<AUX_SC1>(ws, W_HST + ((tg * 8 + cg) * 64 + mt * 16 + t16) * 8, u32x2{__float_as_uint(s1), __float_as_uint(s2)});
  }
  signal_cnt(kargs()->ws, W_CNTH + tg * 256);
}

constexpr unsigned L4_S = 0  , L4_ST = 122880  , L4_CO = 163568;
DI constexpr unsigned l5_slot(int c) { return (c & 3) == 0 ? 123904u : ((c & 3) == 1 ? 40960u : ((c & 3) == 2 ? 71680u : 0u)); }
constexpr unsigned L5_RED = 102400;
static_assert(123904 + 30720 <= L4_CO, "lds");
DI void dma_wd_chunk(const char* wd, unsigned lds0, int w, int lane, int c) {
  const unsigned l16 = lane * 16;
#pragma unroll
  for (int i = 0; i < 4; ++i) { int n = w + 8 * i; const bool dup = i == 3 && n >= 30; if (dup) n -= 8; const int k2 = n >= 15, j = n - 15 * k2;
    if (i < 3 || !dup || lane == 0) glds_sc1(l16 + j * 1024 + k2 * (864 * 160), wd + (size_t)c * (96 * 160), lds0 + l5_slot(c) + n * 1024); }
}
DI void dma_wd_chunk7(const char* wd, unsigned lds0, int w, int lane, int c) {
  const unsigned l16 = lane * 16;
#pragma unroll
  for (int i = 0; i < 5; ++i) { int n = w + 7 * i; const bool dup = i == 4 && n >= 30; if (dup) n -= 7; const int k2 = n >= 15, j = n - 15 * k2;
    if (i < 4 || !dup || lane == 0) glds_sc1(l16 + j * 1024 + k2 * (864 * 160), wd + (size_t)c * (96 * 160), lds0 + l5_slot(c) + n * 1024); }
}
DI void gld128_sc1(u32x4& d, unsigned voff, const char* sbase) { asm volatile("global_load_dwordx4 %0, %1, %2 sc1" : "=v"(d) : "v"(voff), "s"(sbase) : "memory"); }
DI void waitv_tie(int n, u32x4& a, u32x4& b) { asm volatile("s_waitcnt vmcnt(%2)" : "+v"(a), "+v"(b) : "i"(n) : "memory"); }
constexpr int gu_behind(int s) { return s < 5 ? 63 : (s <= 7 ? 13 : (s == 8 ? 8 : (s <= 13 ? 13 : (s == 14 ? 8 : (s == 15 ? 12 : (s == 16 ? 14 : (s == 17 ? 16 : (s == 18 ? 14 : 12)))))))); }
constexpr int GWIN = 5;
DI void phase_gateup(lptr lds, rsrc_t ws, int b, unsigned epoch) {
  char* wsp = kargs()->ws;
  const int tid = threadIdx.x, lane = tid & 63, w = rfl(tid >> 6);
  const int xcd = b & 7, rk = b >> 3, mb = 8 * (xcd >> 2) + (rk >> 2), ngl = rk & 3, u0 = 27 * (xcd & 3) + 7 * ngl, nu = ngl == 3 ? 6 : 7;
  const int t16 = lane & 15, g = lane >> 4;
  const unsigned lds0 = (unsigned)(uintptr_t)lds, l16 = lane * 16;
  const char* wd = wsp + W_WD + (size_t)hg_of(rk) * FF * 160;
  const bool act = w < nu;
  const int u = u0 + (act ? w : 0);
  const char* wsrc = wsp + W_WGU + (size_t)u * 40960;
  u32x4 ag[GWIN], au[GWIN];
  float csp[2][5] = {};
  if (act) {
#pragma unroll
    for (int i = 0; i < GWIN; ++i) { ag[i] = bld128<AUX_SC1>(ws, W_WGU + u * 40960 + l16 + i * 2048); au[i] = bld128<AUX_SC1>(ws, W_WGU + u * 40960 + l16 + i * 2048 + 1024); }
#pragma unroll
    for (int m = 0; m < 2; ++m)
#pragma unroll
      for (int i = 0; i < 5; ++i) csp[m][i] = __uint_as_float(bld32<AUX_SC1>(ws, W_CSP + ((u * 2 + m) * 20 + 5 * g + i) * 64 + t16 * 4));
  }
  SBAR();
  wait_cnt2(wsp, W_CNTH + (2 * mb) * 256, W_CNTH + (2 * mb + 1) * 256, 8u, epoch, 7);
  STAMPX(16);
  const char* hsrc = wsp + W_HF + (size_t)(mb * 8 + w) * 20 * 1024;
  auto dma_h = [&](int sc, int slot) {
#pragma unroll
    for (int i = 0; i < 5; ++i) glds_sc1(l16 + (5 * sc + i) * 1024, hsrc, lds0 + L4_S + slot * 40960 + i * 8192 + w * 1024);
  };
  if (tid == 0) *(LAS unsigned*)(lds + L4_CO) = 0u;
  u32x2 sv[8];
  if (tid < 128) {
#pragma unroll
    for (int c = 0; c < 8; ++c) sv[c] = bld64<AUX_SC1>(ws, W_HST + (((2 * mb + (tid >> 6)) * 8 + c) * 64 + (tid & 63)) * 8);
  }
  dma_h(0, 0);
  SBAR();
  if (tid < 128) {
    float s1 = 0.f, s2 = 0.f;
#pragma unroll
    for (int c = 0; c < 8; ++c) { s1 += __uint_as_float(sv[c][0]); s2 += __uint_as_float(sv[c][1]); }
    const float mean = s1 * (1.f / DM), rstd = rsqrtf(fmaxf(s2 * (1.f / DM) - mean * mean, 0.f) + LN_EPS);
    *(LAS f32x2*)(lds + L4_ST + tid * 8) = f32x2{mean, rstd};
  }
  asm volatile("" ::: "memory"); __builtin_amdgcn_s_waitcnt(0x0F70);
  LBAR();
  STAMPX(17);
  if (act) {
    f32x4 aG[8] = {}, aU[8] = {};
    lptr hb = lds + L4_S + l16;
    u32x4 bb[2][4];
#pragma unroll
    for (int t = 0; t < 4; ++t) bb[0][t] = lds128(hb + t * 1024);
#pragma unroll
    for (int s = 0; s < 20; ++s) {
      const int slot = (s / 5) % 3, nslot = ((s + 1) / 5) % 3;
      if (s == 0) dma_h(1, 1);
      if (s == 3) dma_h(2, 2);
      if (s == 4) { WAITV(13); LBAR(); }
      if (s == 9) { WAITV(12); LBAR(); dma_h(3, 0); }
      if (s == 14) { WAITV(10); LBAR(); }
      if (s == 15) dma_wd_chunk(wd, lds0, w, lane, 0);
      if (s == 16) { BAR(); dma_wd_chunk(wd, lds0, w, lane, 1); }
      if (s == 17) dma_wd_chunk(wd, lds0, w, lane, 2);
      if (s >= GWIN) waitv_tie(gu_behind(s), ag[s % GWIN], au[s % GWIN]);
#pragma unroll
      for (int t = 0; t < 4; ++t) bb[1][t] = lds128(hb + slot * 40960 + (s % 5) * 8192 + (4 + t) * 1024);
      SBAR();
#pragma unroll
      for (int t = 0; t < 4; ++t) { aG[t] = MFMAS(asbf8(ag[s % GWIN]), asbf8(bb[0][t]), aG[t]); aU[t] = MFMAS(asbf8(au[s % GWIN]), asbf8(bb[0][t]), aU[t]); }
      SBAR();
      if (s + 1 < 20) {
#pragma unroll
        for (int t = 0; t < 4; ++t) bb[0][t] = lds128(hb + nslot * 40960 + ((s + 1) % 5) * 8192 + t * 1024);
      }
      SBAR();
#pragma unroll
      for (int t = 0; t < 4; ++t) { aG[4 + t] = MFMAS(asbf8(ag[s % GWIN]), asbf8(bb[1][t]), aG[4 + t]); aU[4 + t] = MFMAS(asbf8(au[s % GWIN]), asbf8(bb[1][t]), aU[4 + t]); }
      SBAR();
      if (s + GWIN < 20) { gld128_sc1(ag[s % GWIN], l16 + (s + GWIN) * 2048, wsrc); gld128_sc1(au[s % GWIN], l16 + (s + GWIN) * 2048 + 1024, wsrc); }
      SBAR();
    }
    STAMPX(18);
    const float sg = swap32_sum(swap16_sum(((csp[0][0] + csp[0][1]) + (csp[0][2] + csp[0][3])) + csp[0][4])), su = swap32_sum(swap16_sum(((csp[1][0] + csp[1][1]) + (csp[1][2] + csp[1][3])) + csp[1][4]));
    float cg4[4], cu4[4];
#pragma unroll
    for (int j = 0; j < 4; ++j) { cg4[j] = __shfl(sg, 4 * g + j); cu4[j] = __shfl(su, 4 * g + j); }
#pragma unroll
    for (int i = 0; i < 4; ++i) {
      u32x2 pk[2];
#pragma unroll
      for (int e = 0; e < 2; ++e) {
        const int t = 2 * i + e;
        const f32x2 mr = *(LAS f32x2*)(lds + L4_ST + (t * 16 + t16) * 8);
        const float r2 = mr[1] * mr[1];
        float hv[4];
#pragma unroll
        for (int j = 0; j < 4; ++j) hv[j] = (aG[t][j] - mr[0] * cg4[j]) * (aU[t][j] - mr[0] * cu4[j]) * r2;
        pk[e] = u32x2{pk2(hv[0], hv[1]), pk2(hv[2], hv[3])};
      }
      const auto s0 = __builtin_amdgcn_permlane16_swap(pk[0][0], pk[1][0], false, false), s1 = __builtin_amdgcn_permlane16_swap(pk[0][1], pk[1][1], false, false);
      bst128<AUX_SC1>(ws, W_GF + (((mb * 8 + 2 * i + (g & 1)) * 54 + (u >> 1)) * 64 + (2 * (u & 1) + (g >> 1)) * 16 + t16) * 16, u32x4{s0[0], s1[0], s0[1], s1[1]});
    }
  } else {
    dma_h(1, 1); dma_h(2, 2);
    WAITV(5); LBAR();
    WAITV(0); LBAR(); dma_h(3, 0);
    WAITV(0); LBAR(); dma_wd_chunk(wd, lds0, w, lane, 0);
    BAR(); dma_wd_chunk(wd, lds0, w, lane, 1); dma_wd_chunk(wd, lds0, w, lane, 2);
  }
  STAMPX(21);
  BAR();
  if (w < 7) dma_wd_chunk7(wd, lds0, w, lane, 3);
  if (w < 7) WAITV(5); else WAITV(0);
  unsigned old = 0; if (lane == 0) old = __hip_atomic_fetch_add((LAS unsigned*)(lds + L4_CO), 1u, __ATOMIC_RELAXED, __HIP_MEMORY_SCOPE_WORKGROUP);
  if (lane == 0 && old == 7u) (void)__hip_atomic_fetch_add((GAS unsigned*)(wsp + W_CNTG + mb * 256), 1u, RLX_AGENT);
}

DI void phase_down(lptr lds, rsrc_t ws, int b, const u32x2 (&hres)[5], unsigned epoch) {
  char* wsp = kargs()->ws;
  const int tid = threadIdx.x, lane = tid & 63, w = rfl(tid >> 6);
  const int xcd = b & 7, rk = b >> 3, tg = 4 * xcd + tgl_of(rk), cg = hg_of(rk);
  const int mt = w & 3, kh = w >> 2, t16 = lane & 15, g = lane >> 4, tq = t16 >> 2, tp = t16 & 3;
  const unsigned lds0 = (unsigned)(uintptr_t)lds, l16 = lane * 16;
  const char* wd = wsp + W_WD + (size_t)cg * FF * 160;
  wait_cnt2(wsp, W_CNTG + (tg >> 1) * 256, W_CNTG + (tg >> 1) * 256, 16u, epoch, 7);
  STAMPX(20);
  const char* abase = wsp + W_GF + (size_t)((tg * 4 + mt) * 54 + 27 * kh) * 1024;
  u32x4 a[4][3];
#pragma unroll
  for (int c = 0; c < 3; ++c)
#pragma unroll
    for (int k = 0; k < 3; ++k) gld128_sc1(a[c][k], l16 + (3 * c + k) * 1024, abase);
  f32x4 acc[5] = {};
  const unsigned brow = kh * 15360 + (16 * (g >> 1) + 4 * (g & 1) + tq) * 160 + tp * 8;
#define DOWN_WAIT(N, c) asm volatile("s_waitcnt vmcnt(" #N ")\n\ts_barrier" : "+v"(a[(c) & 3][0]), "+v"(a[(c) & 3][1]), "+v"(a[(c) & 3][2]) :: "memory")
  bf16x8 bf[2][5];
  { lptr bb = lds + l5_slot(0) + brow;
#pragma unroll
    for (int nt = 0; nt < 5; ++nt) bf[0][nt] = cat8(trread(bb + nt * 32), trread(bb + nt * 32 + 1280)); }
#pragma unroll
  for (int c = 0; c < 9; ++c) {
    asm volatile("" ::: "memory"); WAITL();
    if (c <= 1) DOWN_WAIT(6, c); else if (c <= 6) DOWN_WAIT(10, c); else if (c == 7) DOWN_WAIT(3, c); else DOWN_WAIT(0, c);
    if (c >= 1 && c + 3 < 9) dma_wd_chunk(wd, lds0, w, lane, c + 3);
    if (c + 3 < 9) {
#pragma unroll
      for (int k = 0; k < 3; ++k) gld128_sc1(a[(c + 3) & 3][k], l16 + (3 * (c + 3) + k) * 1024, abase); }
#pragma unroll
    for (int k = 0; k < 3; ++k) {
      const int sn = 3 * c + k + 1;
      if (sn < 27) { lptr bb = lds + l5_slot(sn / 3) + brow + (sn % 3) * 5120;
#pragma unroll
        for (int nt = 0; nt < 5; ++nt) bf[sn & 1][nt] = cat8(trread(bb + nt * 32), trread(bb + nt * 32 + 1280)); }
      SBAR();
#pragma unroll
      for (int nt = 0; nt < 5; ++nt) acc[nt] = MFMAS(bf[(3 * c + k) & 1][nt], asbf8(a[c & 3][k]), acc[nt]);
      SBAR();
    }
  }
  STAMPX(22);
  LAS f32x4* red = (LAS f32x4*)(lds + L5_RED) + mt * 320 + lane;
  if (kh == 1) {
#pragma unroll
    for (int nt = 0; nt < 5; ++nt) red[nt * 64] = acc[nt];
  }
  LBAR();
  if (kh == 0) {
    const int tok = tg * 64 + mt * 16 + t16; char* outp = kargs()->out;
#pragma unroll
    for (int nt = 0; nt < 5; ++nt) {
      const f32x4 o = red[nt * 64];
      const u32x2 r = {pk2(bflo(hres[nt][0]) + acc[nt][0] + o[0], bfhi(hres[nt][0]) + acc[nt][1] + o[1]), pk2(bflo(hres[nt][1]) + acc[nt][2] + o[2], bfhi(hres[nt][1]) + acc[nt][3] + o[3])};
      *(GAS u32x2*)(outp + (size_t)tok * 1280 + cg * 160 + nt * 32 + g * 8) = r;
    }
  }
}

#define CODE_BYTES 51584
__global__ void __launch_bounds__(NTHR, 2) k_block(Params p) {
  extern __shared__ __attribute__((aligned(16))) char smem[];
  lptr lds = (lptr)smem;
  const rsrc_t ws = __builtin_amdgcn_make_buffer_rsrc((void*)p.ws, 0, (int)W_END, 0x00020000);
  const int b = blockIdx.x;
  {
    unsigned long long pc; asm volatile("s_getpc_b64 %0" : "=s"(pc));
    const int ln = (b >> 3) * 16 + (threadIdx.x & 15);
    if (threadIdx.x >= 448 && (threadIdx.x & 63) < 16 && ln * 128 < CODE_BYTES) touch(ln * 128, (const char*)(pc & ~127ull), (unsigned)(uintptr_t)smem + 163584);
  }
  u32x4 q1; u32x2 q2; unsigned epoch; float kmax2 = 0.f;
  u32x2 hres[5];
  STAMPX(0);
  phase1(lds, ws, b, q1, q2, epoch, kmax2);
  STAMPX(5);
  phase_attn(lds, ws, b, q1, q2, epoch, kmax2);
  STAMPX(12);
  phase_oproj(lds, ws, b, hres, epoch);
  STAMPX(15);
  phase_gateup(lds, ws, b, epoch);
  STAMPX(19);
  phase_down(lds, ws, b, hres, epoch);
  STAMPX(23);
}

extern "C" void kernel_launch(void* const* d_in, const int* in_sizes, int n_in, void* d_out, int out_size, void* d_ws, size_t ws_size, hipStream_t stream) {
  Params p;
  p.x = (const char*)d_in[0]; p.wq = (const char*)d_in[1]; p.wk = (const char*)d_in[2]; p.wv = (const char*)d_in[3]; p.wo = (const char*)d_in[4];
  p.wg = (const char*)d_in[6]; p.wu = (const char*)d_in[7]; p.wd = (const char*)d_in[8];
  p.out = (char*)d_out; p.ws = (char*)d_ws;
  constexpr int LDS_BYTES = 163840;
  (void)hipFuncSetAttribute((const void*)k_block, hipFuncAttributeMaxDynamicSharedMemorySize, LDS_BYTES);
  k_block<<<NWG, NTHR, LDS_BYTES, stream>>>(p);
}
```
